```python
import torch

def _softmax(x, axis=-1):
    return torch.softmax(x, dim=axis)
def _gelu(x):
    return torch.nn.functional.gelu(x, approximate="tanh")
def _silu(x):
    return torch.nn.functional.silu(x)
def _sigmoid(x):
    return torch.sigmoid(x)
def _layernorm(x, axis=-1, eps=1e-5):
    m = x.mean(dim=axis, keepdim=True)
    v = x.var(dim=axis, keepdim=True, unbiased=False)
    return (x - m) / torch.sqrt(v + eps)
def _scatter_add(tgt, idx, src, axis=0):
    return tgt.clone().index_add_(axis, idx, src)
def _segment_sum(src, seg, n):
    out = torch.zeros((n,) + src.shape[1:], dtype=src.dtype)
    return out.index_add_(0, seg, src)
def _pad(x, pw):
    flat = tuple(v for pair in reversed(pw) for v in pair)
    return torch.nn.functional.pad(x, flat)

kernel_name = 'standalone__mha_softmax__near_ridge'

def setup_inputs():

    _w = (lambda a: (lambda t: t.to(torch.bfloat16) if t.is_floating_point() else t)(torch.as_tensor(a)))
    B, H, Sq, Sk, D = 32, 32, 256, 512, 128
    return {
        "q": _w((lambda *s: torch.randn(*s, dtype=torch.bfloat16) * 0.8)(B, H, Sq, D)),
        "k": _w((lambda *s: torch.randn(*s, dtype=torch.bfloat16) * 0.8)(B, H, Sk, D)),
        "v": _w((lambda *s: torch.randn(*s, dtype=torch.bfloat16) * 0.05)(B, H, Sk, D)),
    }

def reference(q, k, v):
    scale = q.shape[-1] ** -0.5
    scores = torch.einsum("bhqd,bhkd->bhqk", q, k) * scale
    p = _softmax(scores, axis=-1)
    return torch.einsum("bhqk,bhkd->bhqd", p, v)
```

```cpp
#include <hip/hip_runtime.h>
#include <hip/hip_bf16.h>

__device__ __forceinline__ void mha_body(unsigned ql, unsigned qh, unsigned kl, unsigned kh, unsigned vl, unsigned vh, unsigned ol, unsigned oh,
                                         unsigned wl, unsigned wh, unsigned wg, unsigned wv, unsigned lds) {
  asm volatile(
    "v_mbcnt_lo_u32_b32 v0, -1, 0\n"
    "v_mbcnt_hi_u32_b32 v0, -1, v0\n"
    "s_lshl_b32 s52, %[wv], 12\n"
    "s_add_u32 s52, s52, %[lds]\n"
    "s_mul_i32 s53, %[wv], 12288\n"
    "s_mov_b32 s63, %[wg]\n"
    "s_lshl_b32 s58, s63, 16\n"
    "s_add_u32 s44, %[p0l], s58\n"
    "s_addc_u32 s45, %[p0h], 0\n"
    "s_and_b32 s45, s45, 0xffff\n"
    "s_mov_b32 s46, 0x10000\n"
    "s_mov_b32 s47, 0x20000\n"
    "s_lshl_b32 s58, s63, 17\n"
    "s_add_u32 s36, %[p1l], s58\n"
    "s_addc_u32 s37, %[p1h], 0\n"
    "s_and_b32 s37, s37, 0xffff\n"
    "s_mov_b32 s38, 0x20000\n"
    "s_mov_b32 s39, 0x20000\n"
    "v_and_b32 v1, 15, v0\n"
    "v_lshrrev_b32 v2, 4, v0\n"
    "v_and_b32 v11, 3, v0\n"
    "v_bfe_u32 v13, v0, 4, 1\n"
    "v_lshrrev_b32 v12, 5, v0\n"
    "v_bfe_u32 v14, v0, 2, 3\n"
    "v_lshlrev_b32 v14, 8, v14\n"
    "v_lshl_add_u32 v14, v12, 6, v14\n"
    "s_lshl_b32 s58, %[wv], 12\n"
    "v_add_u32 v14, s58, v14\n"
    "v_sub_u32 v15, 4, v13\n"
    "v_and_b32 v15, 3, v15\n"
    "v_xor_b32 v15, v11, v15\n"
    "v_lshl_add_u32 v181, v15, 4, v14\n"
    "v_sub_u32 v15, 2, v13\n"
    "v_and_b32 v15, 3, v15\n"
    "v_xor_b32 v15, v11, v15\n"
    "v_lshl_add_u32 v182, v15, 4, v14\n"
    "s_nop 0\n"
    "s_add_u32 s58, s53, 0x0\n"
    "s_add_u32 m0, s52, 0x20000\n"
    "s_nop 0\n"
    "buffer_load_dwordx4 v181, s[44:47], s58 offen offset:0 nt lds\n"
    "s_add_u32 m0, s52, 0x1fc00\n"
    "s_nop 0\n"
    "buffer_load_dwordx4 v182, s[44:47], s58 offen offset:2048 nt lds\n"
    "s_add_u32 s58, s53, 0x80\n"
    "s_add_u32 m0, s52, 0x24000\n"
    "s_nop 0\n"
    "buffer_load_dwordx4 v181, s[44:47], s58 offen offset:0 nt lds\n"
    "s_add_u32 m0, s52, 0x23c00\n"
    "s_nop 0\n"
    "buffer_load_dwordx4 v182, s[44:47], s58 offen offset:2048 nt lds\n"
    "s_add_u32 m0, s52, 0x0\n"
    "s_nop 0\n"
    "buffer_load_dwordx4 v181, s[36:39], 0 offen offset:0 nt lds\n"
    "buffer_load_dwordx4 v182, s[36:39], 0 offen offset:2048 nt lds\n"
    "s_add_u32 m0, s52, 0x380\n"
    "s_nop 0\n"
    "buffer_load_dwordx4 v181, s[36:39], 0 offen offset:128 nt lds\n"
    "buffer_load_dwordx4 v182, s[36:39], 0 offen offset:2176 nt lds\n"
    "s_add_u32 s58, s53, 0x1000\n"
    "s_add_u32 m0, s52, 0x20800\n"
    "s_nop 0\n"
    "buffer_load_dwordx4 v181, s[44:47], s58 offen offset:0 nt lds\n"
    "s_add_u32 m0, s52, 0x20400\n"
    "s_nop 0\n"
    "buffer_load_dwordx4 v182, s[44:47], s58 offen offset:2048 nt lds\n"
    "s_add_u32 s58, s53, 0x1080\n"
    "s_add_u32 m0, s52, 0x24800\n"
    "s_nop 0\n"
    "buffer_load_dwordx4 v181, s[44:47], s58 offen offset:0 nt lds\n"
    "s_add_u32 m0, s52, 0x24400\n"
    "s_nop 0\n"
    "buffer_load_dwordx4 v182, s[44:47], s58 offen offset:2048 nt lds\n"
    "s_getpc_b64 s[64:65]\n"
    "Lmha_PCP:\n"
    "s_add_u32 s64, s64, Lmha_FIRST-Lmha_PCP\n"
    "s_addc_u32 s65, s65, 0\n"
    "s_lshl_b32 s58, %[wv], 13\n"
    "v_lshlrev_b32 v17, 7, v0\n"
    "v_add_u32 v17, s58, v17\n"
    "s_mov_b32 s58, Lmha_SAFE-Lmha_FIRST-4\n"
    "v_min_u32 v17, s58, v17\n"
    "v_and_b32 v17, -4, v17\n"
    "global_load_dword v247, v17, s[64:65]\n"
    "v_add_u32 v17, 0x8000, v17\n"
    "v_min_u32 v17, s58, v17\n"
    "v_and_b32 v17, -4, v17\n"
    "global_load_dword v246, v17, s[64:65]\n"
    "s_memrealtime s[94:95]\n"
    "s_waitcnt lgkmcnt(0)\n"
    "s_add_u32 s78, s94, 300\n"
    "s_lshl_b32 s58, s63, 17\n"
    "s_add_u32 s40, %[p2l], s58\n"
    "s_addc_u32 s41, %[p2h], 0\n"
    "s_and_b32 s41, s41, 0xffff\n"
    "s_mov_b32 s42, 0x20000\n"
    "s_mov_b32 s43, 0x20000\n"
    "v_lshrrev_b32 v15, 1, v1\n"
    "v_lshlrev_b32 v15, 8, v15\n"
    "v_lshl_add_u32 v15, v2, 5, v15\n"
    "v_and_b32 v16, 1, v0\n"
    "v_lshl_add_u32 v15, v16, 4, v15\n"
    "s_lshl_b32 s58, %[wv], 12\n"
    "v_add_u32 v183, s58, v15\n"
    "s_add_u32 s58, s53, 0x2000\n"
    "s_add_u32 m0, s52, 0x18000\n"
    "s_nop 0\n"
    "buffer_load_dwordx4 v181, s[44:47], s58 offen offset:0 nt lds\n"
    "s_add_u32 m0, s52, 0x17c00\n"
    "s_nop 0\n"
    "buffer_load_dwordx4 v182, s[44:47], s58 offen offset:2048 nt lds\n"
    "s_add_u32 s58, s53, 0x3000\n"
    "s_add_u32 m0, s52, 0x18800\n"
    "s_nop 0\n"
    "buffer_load_dwordx4 v181, s[44:47], s58 offen offset:0 nt lds\n"
    "s_add_u32 m0, s52, 0x18400\n"
    "s_nop 0\n"
    "buffer_load_dwordx4 v182, s[44:47], s58 offen offset:2048 nt lds\n"
    "s_mov_b32 s54, 0x3e0293ee\n"
    "s_mov_b32 s55, 0xbe0293ee\n"
    "s_mov_b32 s57, 0\n"
    "s_mov_b32 s62, 0\n"
    "s_mov_b64 s[60:61], 0\n"
    "s_movk_i32 s66, 0x1000\n"
    "s_movk_i32 s67, 0x2000\n"
    "s_movk_i32 s68, 0x3000\n"
    "s_mov_b32 s70, 0x4000\n"
    "s_mov_b32 s71, 0x8000\n"
    "s_mov_b32 s72, 0xc000\n"
    "s_mov_b32 s73, 0x10000\n"
    "s_mov_b32 s74, 0x14000\n"
    "s_mov_b32 s75, 0x18000\n"
    "s_mov_b32 s76, 0x1c000\n"
    "s_lshl_b32 s58, s63, 16\n"
    "s_add_u32 s48, %[p3l], s58\n"
    "s_addc_u32 s49, %[p3h], 0\n"
    "s_sub_u32 s48, s48, 0x1000000\n"
    "s_subb_u32 s49, s49, 0\n"
    "s_and_b32 s49, s49, 0xffff\n"
    "s_mov_b32 s50, 0x10000\n"
    "s_mov_b32 s51, 0x20000\n"
    "s_mov_b32 s32, 0\n"
    "s_mov_b32 s33, 0\n"
    "s_mov_b32 s34, 0\n"
    "s_mov_b32 s35, 0x20000\n"
    "v_mov_b32 v206, 0x4e800000\n"
    "v_lshrrev_b32 v3, 2, v1\n"
    "v_sub_u32 v3, 4, v3\n"
    "v_and_b32 v3, 3, v3\n"
    "v_xor_b32 v3, v2, v3\n"
    "v_and_b32 v5, 7, v1\n"
    "v_lshlrev_b32 v6, 6, v5\n"
    "v_lshl_add_u32 v6, v3, 4, v6\n"
    "v_lshrrev_b32 v4, 3, v1\n"
    "v_lshl_add_u32 v176, v4, 11, v6\n"
    "v_add_u32 v176, %[lds], v176\n"
    "v_add_u32 v177, 0x10000, v176\n"
    "v_lshl_add_u32 v180, v4, 10, v6\n"
    "v_add_u32 v180, s52, v180\n"
    "v_add_u32 v180, 0x20000, v180\n"
    "v_lshrrev_b32 v7, 1, v2\n"
    "v_lshlrev_b32 v7, 11, v7\n"
    "v_and_b32 v8, 1, v2\n"
    "v_lshl_add_u32 v7, v8, 7, v7\n"
    "v_bfe_u32 v8, v0, 2, 2\n"
    "v_lshl_add_u32 v7, v8, 5, v7\n"
    "v_lshl_add_u32 v7, v11, 3, v7\n"
    "v_add_u32 v7, %[lds], v7\n"
    "v_add_u32 v178, 0x4000, v7\n"
    "v_add_u32 v179, 0x14000, v7\n"
    "v_lshlrev_b32 v7, 4, v0\n"
    "s_lshl_b32 s58, %[wv], 14\n"
    "v_add_u32 v184, s58, v7\n"
    "v_add_u32 v7, 0, v2\n"
    "v_xor_b32 v7, v1, v7\n"
    "v_lshlrev_b32 v7, 4, v7\n"
    "v_lshl_add_u32 v7, v2, 8, v7\n"
    "v_add_u32 v7, s52, v7\n"
    "v_add_u32 v185, 0x24000, v7\n"
    "v_add_u32 v7, 4, v2\n"
    "v_xor_b32 v7, v1, v7\n"
    "v_lshlrev_b32 v7, 4, v7\n"
    "v_lshl_add_u32 v7, v2, 8, v7\n"
    "v_add_u32 v7, s52, v7\n"
    "v_add_u32 v186, 0x24000, v7\n"
    "v_lshrrev_b32 v7, 1, v2\n"
    "v_xor_b32 v7, v7, v5\n"
    "v_lshlrev_b32 v7, 4, v7\n"
    "v_lshl_add_u32 v7, v1, 8, v7\n"
    "v_and_b32 v8, 1, v2\n"
    "v_lshl_add_u32 v7, v8, 3, v7\n"
    "v_add_u32 v7, s52, v7\n"
    "v_add_u32 v7, 0x24000, v7\n"
    "v_xor_b32 v187, 0x0, v7\n"
    "v_xor_b32 v188, 0x20, v7\n"
    "v_xor_b32 v189, 0x40, v7\n"
    "v_xor_b32 v190, 0x60, v7\n"
    "v_xor_b32 v191, 0x80, v7\n"
    "v_xor_b32 v192, 0xa0, v7\n"
    "v_xor_b32 v193, 0xc0, v7\n"
    "v_xor_b32 v194, 0xe0, v7\n"
    "v_mov_b32 v7, 0x3f803f80\n"
    "v_accvgpr_write_b32 a240, v7\n"
    "v_accvgpr_write_b32 a241, v7\n"
    "v_accvgpr_write_b32 a242, v7\n"
    "v_accvgpr_write_b32 a243, v7\n"
    "v_subrev_u32 v207, 0x8000, v180\n"
    "s_waitcnt vmcnt(10) lgkmcnt(0)\n"
    "s_barrier\n"
    "ds_read_b128 a[0:3], v180 offset:0\n"
    "ds_read_b128 a[4:7], v180 offset:512\n"
    "ds_read_b128 a[8:11], v180 offset:16384\n"
    "ds_read_b128 a[12:15], v180 offset:16896\n"
    "ds_read_b128 a[128:131], v176 offset:0\n"
    "ds_read_b128 a[132:135], v176 offset:512\n"
    "ds_read_b128 a[136:139], v176 offset:1024\n"
    "ds_read_b128 a[140:143], v176 offset:1536\n"
    "ds_read_b128 a[144:147], v176 offset:4096\n"
    "ds_read_b128 a[148:151], v176 offset:4608\n"
    "ds_read_b128 a[152:155], v176 offset:5120\n"
    "ds_read_b128 a[156:159], v176 offset:5632\n"
    "s_waitcnt lgkmcnt(0)\n"
    "s_mov_b32 s79, 3\n"
    "s_mov_b32 s65, 684\n"
    "Lmha_FIRST:\n"
    "v_mfma_f32_16x16x32_bf16 v[128:131], a[128:131], a[0:3], 0\n"
    "s_waitcnt vmcnt(6)\n"
    "s_add_u32 s58, s53, 0x2080\n"
    "s_add_u32 m0, s52, 0x1c000\n"
    "ds_read_b128 a[16:19], v180 offset:2048\n"
    "v_mfma_f32_16x16x32_bf16 v[128:131], a[132:135], a[4:7], v[128:131]\n"
    "ds_read_b128 a[20:23], v180 offset:2560\n"
    "buffer_load_dwordx4 v181, s[44:47], s58 offen offset:0 nt lds\n"
    "v_mfma_f32_16x16x32_bf16 v[128:131], a[136:139], a[8:11], v[128:131]\n"
    "s_add_u32 m0, s52, 0x1bc00\n"
    "ds_read_b128 a[24:27], v180 offset:18432\n"
    "v_mfma_f32_16x16x32_bf16 v[128:131], a[140:143], a[12:15], v[128:131]\n"
    "ds_read_b128 a[28:31], v180 offset:18944\n"
    "buffer_load_dwordx4 v182, s[44:47], s58 offen offset:2048 nt lds\n"
    "v_mfma_f32_16x16x32_bf16 v[132:135], a[144:147], a[0:3], 0\n"
    "s_add_u32 s58, s53, 0x3080\n"
    "s_add_u32 m0, s52, 0x1c800\n"
    "v_mfma_f32_16x16x32_bf16 v[132:135], a[148:151], a[4:7], v[132:135]\n"
    "buffer_load_dwordx4 v181, s[44:47], s58 offen offset:0 nt lds\n"
    "v_mfma_f32_16x16x32_bf16 v[132:135], a[152:155], a[8:11], v[132:135]\n"
    "s_add_u32 m0, s52, 0x1c400\n"
    "v_mfma_f32_16x16x32_bf16 v[132:135], a[156:159], a[12:15], v[132:135]\n"
    "s_waitcnt lgkmcnt(0)\n"
    "buffer_load_dwordx4 v182, s[44:47], s58 offen offset:2048 nt lds\n"
    "v_mfma_f32_16x16x32_bf16 v[136:139], a[128:131], a[16:19], 0\n"
    "s_add_u32 m0, s52, 0x4000\n"
    "v_mfma_f32_16x16x32_bf16 v[136:139], a[132:135], a[20:23], v[136:139]\n"
    "buffer_load_dwordx4 v183, s[40:43], 0 offen offset:0 nt lds\n"
    "v_mfma_f32_16x16x32_bf16 v[136:139], a[136:139], a[24:27], v[136:139]\n"
    "v_mfma_f32_16x16x32_bf16 v[136:139], a[140:143], a[28:31], v[136:139]\n"
    "buffer_load_dwordx4 v183, s[40:43], 0 offen offset:2048 nt lds\n"
    "v_max3_f32 v204, v128, v129, v130\n"
    "v_max3_f32 v205, v131, v132, v133\n"
    "v_max3_f32 v204, v204, v134, v135\n"
    "v_mfma_f32_16x16x32_bf16 v[140:143], a[144:147], a[16:19], 0\n"
    "s_add_u32 m0, s52, 0x4380\n"
    "v_max_f32 v204, v204, v205\n"
    "v_mov_b32 v205, v204\n"
    "v_mfma_f32_16x16x32_bf16 v[140:143], a[148:151], a[20:23], v[140:143]\n"
    "buffer_load_dwordx4 v183, s[40:43], 0 offen offset:128 nt lds\n"
    "v_permlane32_swap_b32 v204, v205\n"
    "v_max_f32 v204, v204, v205\n"
    "v_mov_b32 v205, v204\n"
    "v_mfma_f32_16x16x32_bf16 v[140:143], a[152:155], a[24:27], v[140:143]\n"
    "s_nop 0\n"
    "v_permlane16_swap_b32 v204, v205\n"
    "v_max_f32 v204, v204, v205\n"
    "v_mul_f32 v248, -1.0, v204\n"
    "v_mul_f32 v249, -1.0, v204\n"
    "v_mfma_f32_16x16x32_bf16 v[140:143], a[156:159], a[28:31], v[140:143]\n"
    "buffer_load_dwordx4 v183, s[40:43], 0 offen offset:2176 nt lds\n"
    "v_mul_f32 v250, -1.0, v204\n"
    "v_mul_f32 v251, -1.0, v204\n"
    "v_sub_f32 v128, v128, v204\n"
    "s_add_u32 m0, s52, 0x8000\n"
    "v_sub_f32 v129, v129, v204\n"
    "v_sub_f32 v130, v130, v204\n"
    "v_sub_f32 v131, v131, v204\n"
    "v_sub_f32 v132, v132, v204\n"
    "buffer_load_dwordx4 v181, s[36:39], s70 offen offset:0 nt lds\n"
    "v_sub_f32 v133, v133, v204\n"
    "v_sub_f32 v134, v134, v204\n"
    "v_sub_f32 v135, v135, v204\n"
    "s_waitcnt vmcnt(5)\n"
    "v_mul_f32 v128, s54, v128\n"
    "v_mul_f32 v129, s54, v129\n"
    "v_mul_f32 v130, s54, v130\n"
    "v_mul_f32 v131, s54, v131\n"
    "ds_read_b128 a[32:35], v207 offset:0\n"
    "buffer_load_dwordx4 v182, s[36:39], s70 offen offset:2048 nt lds\n"
    "v_mul_f32 v132, s54, v132\n"
    "s_add_u32 m0, s52, 0x8380\n"
    "ds_read_b128 a[36:39], v207 offset:512\n"
    "v_exp_f32 v128, v128\n"
    "v_mul_f32 v133, s54, v133\n"
    "ds_read_b128 a[48:51], v207 offset:2048\n"
    "buffer_load_dwordx4 v181, s[36:39], s70 offen offset:128 nt lds\n"
    "v_exp_f32 v129, v129\n"
    "ds_read_b128 a[52:55], v207 offset:2560\n"
    "v_exp_f32 v130, v130\n"
    "v_cvt_pk_bf16_f32 v160, v128, v129\n"
    "ds_read_b128 a[40:43], v207 offset:16384\n"
    "buffer_load_dwordx4 v182, s[36:39], s70 offen offset:2176 nt lds\n"
    "v_exp_f32 v131, v131\n"
    "s_add_u32 m0, s52, 0xc000\n"
    "ds_read_b128 a[44:47], v207 offset:16896\n"
    "v_exp_f32 v132, v132\n"
    "v_cvt_pk_bf16_f32 v161, v130, v131\n"
    "ds_read_b128 a[56:59], v207 offset:18432\n"
    "buffer_load_dwordx4 v183, s[40:43], s70 offen offset:0 nt lds\n"
    "v_exp_f32 v133, v133\n"
    "ds_read_b128 a[60:63], v207 offset:18944\n"
    "v_mul_f32 v134, s54, v134\n"
    "v_exp_f32 v134, v134\n"
    "buffer_load_dwordx4 v183, s[40:43], s70 offen offset:2048 nt lds\n"
    "v_mul_f32 v135, s54, v135\n"
    "v_exp_f32 v135, v135\n"
    "v_cvt_pk_bf16_f32 v162, v132, v133\n"
    "s_waitcnt vmcnt(6) lgkmcnt(0)\n"
    "s_barrier\n"
    "v_cvt_pk_bf16_f32 v163, v134, v135\n"
    "v_max3_f32 v204, v136, v137, v138\n"
    "v_max3_f32 v205, v139, v140, v141\n"
    "v_max3_f32 v204, v204, v142, v143\n"
    "s_add_u32 m0, s52, 0xc380\n"
    "ds_read_b64_tr_b16 a[160:161], v178 offset:0\n"
    "ds_read_b64_tr_b16 a[162:163], v178 offset:4096\n"
    "v_max_f32 v204, v204, v205\n"
    "v_mov_b32 v205, v204\n"
    "ds_read_b64_tr_b16 a[164:165], v178 offset:256\n"
    "buffer_load_dwordx4 v183, s[40:43], s70 offen offset:128 nt lds\n"
    "v_permlane32_swap_b32 v204, v205\n"
    "v_max_f32 v204, v204, v205\n"
    "v_mov_b32 v205, v204\n"
    "ds_read_b64_tr_b16 a[166:167], v178 offset:4352\n"
    "s_nop 0\n"
    "v_permlane16_swap_b32 v204, v205\n"
    "v_max_f32 v204, v204, v205\n"
    "v_mul_f32 v252, -1.0, v204\n"
    "ds_read_b64_tr_b16 a[168:169], v178 offset:512\n"
    "ds_read_b64_tr_b16 a[170:171], v178 offset:4608\n"
    "buffer_load_dwordx4 v183, s[40:43], s70 offen offset:2176 nt lds\n"
    "v_mul_f32 v253, -1.0, v204\n"
    "v_mul_f32 v254, -1.0, v204\n"
    "ds_read_b64_tr_b16 a[172:173], v178 offset:768\n"
    "v_mul_f32 v255, -1.0, v204\n"
    "v_sub_f32 v136, v136, v204\n"
    "v_sub_f32 v137, v137, v204\n"
    "v_mfma_f32_16x16x32_bf16 v[144:147], a[128:131], a[32:35], 0\n"
    "ds_read_b64_tr_b16 a[174:175], v178 offset:4864\n"
    "v_sub_f32 v138, v138, v204\n"
    "v_sub_f32 v139, v139, v204\n"
    "v_sub_f32 v140, v140, v204\n"
    "v_mfma_f32_16x16x32_bf16 v[144:147], a[132:135], a[36:39], v[144:147]\n"
    "ds_read_b64_tr_b16 a[176:177], v178 offset:1024\n"
    "ds_read_b64_tr_b16 a[178:179], v178 offset:5120\n"
    "v_sub_f32 v141, v141, v204\n"
    "v_sub_f32 v142, v142, v204\n"
    "v_mfma_f32_16x16x32_bf16 v[144:147], a[136:139], a[40:43], v[144:147]\n"
    "ds_read_b64_tr_b16 a[180:181], v178 offset:1280\n"
    "v_sub_f32 v143, v143, v204\n"
    "v_mul_f32 v136, s54, v136\n"
    "v_exp_f32 v136, v136\n"
    "v_mfma_f32_16x16x32_bf16 v[144:147], a[140:143], a[44:47], v[144:147]\n"
    "ds_read_b64_tr_b16 a[182:183], v178 offset:5376\n"
    "v_mul_f32 v137, s54, v137\n"
    "v_exp_f32 v137, v137\n"
    "v_mfma_f32_16x16x32_bf16 v[148:151], a[144:147], a[32:35], 0\n"
    "ds_read_b64_tr_b16 a[184:185], v178 offset:1536\n"
    "ds_read_b64_tr_b16 a[186:187], v178 offset:5632\n"
    "v_mul_f32 v138, s54, v138\n"
    "v_mul_f32 v139, s54, v139\n"
    "v_mfma_f32_16x16x32_bf16 v[148:151], a[148:151], a[36:39], v[148:151]\n"
    "ds_read_b64_tr_b16 a[188:189], v178 offset:1792\n"
    "v_exp_f32 v138, v138\n"
    "v_cvt_pk_bf16_f32 v164, v136, v137\n"
    "v_mfma_f32_16x16x32_bf16 v[148:151], a[152:155], a[40:43], v[148:151]\n"
    "ds_read_b64_tr_b16 a[190:191], v178 offset:5888\n"
    "v_exp_f32 v139, v139\n"
    "v_mul_f32 v140, s54, v140\n"
    "v_mfma_f32_16x16x32_bf16 v[148:151], a[156:159], a[44:47], v[148:151]\n"
    "v_exp_f32 v140, v140\n"
    "v_mul_f32 v141, s54, v141\n"
    "v_cvt_pk_bf16_f32 v165, v138, v139\n"
    "v_mfma_f32_16x16x32_bf16 v[152:155], a[128:131], a[48:51], 0\n"
    "s_waitcnt lgkmcnt(0)\n"
    "s_add_u32 m0, s52, 0x10000\n"
    "v_exp_f32 v141, v141\n"
    "v_mul_f32 v142, s54, v142\n"
    "v_mul_f32 v143, s54, v143\n"
    "v_mfma_f32_16x16x32_bf16 v[152:155], a[132:135], a[52:55], v[152:155]\n"
    "buffer_load_dwordx4 v181, s[36:39], s71 offen offset:0 nt lds\n"
    "v_exp_f32 v142, v142\n"
    "v_cvt_pk_bf16_f32 v166, v140, v141\n"
    "v_mfma_f32_16x16x32_bf16 v[152:155], a[136:139], a[56:59], v[152:155]\n"
    "ds_read_b128 a[128:131], v176 offset:8192\n"
    "v_exp_f32 v143, v143\n"
    "v_mfma_f32_16x16x32_bf16 v[152:155], a[140:143], a[60:63], v[152:155]\n"
    "buffer_load_dwordx4 v182, s[36:39], s71 offen offset:2048 nt lds\n"
    "v_cvt_pk_bf16_f32 v167, v142, v143\n"
    "v_max3_f32 v204, v144, v145, v146\n"
    "v_max3_f32 v205, v147, v148, v149\n"
    "v_mfma_f32_16x16x32_bf16 v[156:159], a[144:147], a[48:51], 0\n"
    "s_add_u32 m0, s52, 0x10380\n"
    "ds_read_b128 a[132:135], v176 offset:8704\n"
    "v_max3_f32 v204, v204, v150, v151\n"
    "v_max_f32 v204, v204, v205\n"
    "v_mov_b32 v205, v204\n"
    "v_mfma_f32_16x16x32_bf16 v[156:159], a[148:151], a[52:55], v[156:159]\n"
    "buffer_load_dwordx4 v181, s[36:39], s71 offen offset:128 nt lds\n"
    "v_permlane32_swap_b32 v204, v205\n"
    "v_max_f32 v204, v204, v205\n"
    "v_mfma_f32_16x16x32_bf16 v[156:159], a[152:155], a[56:59], v[156:159]\n"
    "ds_read_b128 a[136:139], v176 offset:9216\n"
    "v_mov_b32 v205, v204\n"
    "v_mfma_f32_16x16x32_bf16 v[156:159], a[156:159], a[60:63], v[156:159]\n"
    "buffer_load_dwordx4 v182, s[36:39], s71 offen offset:2176 nt lds\n"
    "v_permlane16_swap_b32 v204, v205\n"
    "v_max_f32 v204, v204, v205\n"
    "v_mul_f32 v196, -1.0, v204\n"
    "v_mfma_f32_16x16x32_bf16 v[0:3], a[160:163], v[160:163], 0\n"
    "ds_read_b128 a[140:143], v176 offset:9728\n"
    "v_mul_f32 v197, -1.0, v204\n"
    "v_mul_f32 v198, -1.0, v204\n"
    "v_mul_f32 v199, -1.0, v204\n"
    "v_mfma_f32_16x16x32_bf16 v[4:7], a[164:167], v[160:163], 0\n"
    "v_sub_f32 v144, v144, v204\n"
    "v_sub_f32 v145, v145, v204\n"
    "v_sub_f32 v146, v146, v204\n"
    "v_mfma_f32_16x16x32_bf16 v[8:11], a[168:171], v[160:163], 0\n"
    "s_add_u32 m0, s52, 0x14000\n"
    "ds_read_b128 a[144:147], v176 offset:12288\n"
    "v_sub_f32 v147, v147, v204\n"
    "v_sub_f32 v148, v148, v204\n"
    "v_sub_f32 v149, v149, v204\n"
    "v_mfma_f32_16x16x32_bf16 v[12:15], a[172:175], v[160:163], 0\n"
    "buffer_load_dwordx4 v183, s[40:43], s71 offen offset:0 nt lds\n"
    "v_sub_f32 v150, v150, v204\n"
    "v_sub_f32 v151, v151, v204\n"
    "v_mul_f32 v144, s54, v144\n"
    "v_mfma_f32_16x16x32_bf16 v[16:19], a[176:179], v[160:163], 0\n"
    "ds_read_b128 a[148:151], v176 offset:12800\n"
    "v_exp_f32 v144, v144\n"
    "v_mul_f32 v145, s54, v145\n"
    "v_mfma_f32_16x16x32_bf16 v[20:23], a[180:183], v[160:163], 0\n"
    "v_exp_f32 v145, v145\n"
    "v_mul_f32 v146, s54, v146\n"
    "v_mfma_f32_16x16x32_bf16 v[24:27], a[184:187], v[160:163], 0\n"
    "ds_read_b128 a[152:155], v176 offset:13312\n"
    "v_exp_f32 v146, v146\n"
    "v_mul_f32 v147, s54, v147\n"
    "v_mfma_f32_16x16x32_bf16 v[28:31], a[188:191], v[160:163], 0\n"
    "buffer_load_dwordx4 v183, s[40:43], s71 offen offset:2048 nt lds\n"
    "v_exp_f32 v147, v147\n"
    "v_cvt_pk_bf16_f32 v168, v144, v145\n"
    "v_mfma_f32_16x16x32_bf16 a[224:227], a[240:243], v[160:163], 0\n"
    "ds_read_b128 a[156:159], v176 offset:13824\n"
    "v_mul_f32 v148, s54, v148\n"
    "v_exp_f32 v148, v148\n"
    "v_mfma_f32_16x16x32_bf16 v[32:35], a[160:163], v[164:167], 0\n"
    "v_mul_f32 v149, s54, v149\n"
    "v_exp_f32 v149, v149\n"
    "v_mfma_f32_16x16x32_bf16 v[36:39], a[164:167], v[164:167], 0\n"
    "s_add_u32 m0, s52, 0x14380\n"
    "v_cvt_pk_bf16_f32 v169, v146, v147\n"
    "v_mul_f32 v150, s54, v150\n"
    "v_exp_f32 v150, v150\n"
    "v_mfma_f32_16x16x32_bf16 v[40:43], a[168:171], v[164:167], 0\n"
    "buffer_load_dwordx4 v183, s[40:43], s71 offen offset:128 nt lds\n"
    "v_mul_f32 v151, s54, v151\n"
    "v_exp_f32 v151, v151\n"
    "v_cvt_pk_bf16_f32 v170, v148, v149\n"
    "v_mfma_f32_16x16x32_bf16 v[44:47], a[172:175], v[164:167], 0\n"
    "v_cvt_pk_bf16_f32 v171, v150, v151\n"
    "v_max3_f32 v204, v152, v153, v154\n"
    "v_max3_f32 v205, v155, v156, v157\n"
    "v_mfma_f32_16x16x32_bf16 v[48:51], a[176:179], v[164:167], 0\n"
    "v_max3_f32 v204, v204, v158, v159\n"
    "v_max_f32 v204, v204, v205\n"
    "v_mov_b32 v205, v204\n"
    "v_mfma_f32_16x16x32_bf16 v[52:55], a[180:183], v[164:167], 0\n"
    "ds_read_b64_tr_b16 a[192:193], v178 offset:8192\n"
    "v_permlane32_swap_b32 v204, v205\n"
    "v_max_f32 v204, v204, v205\n"
    "v_mfma_f32_16x16x32_bf16 v[56:59], a[184:187], v[164:167], 0\n"
    "buffer_load_dwordx4 v183, s[40:43], s71 offen offset:2176 nt lds\n"
    "v_mov_b32 v205, v204\n"
    "v_mfma_f32_16x16x32_bf16 v[60:63], a[188:191], v[164:167], 0\n"
    "s_waitcnt lgkmcnt(1)\n"
    "ds_read_b64_tr_b16 a[194:195], v178 offset:12288\n"
    "v_permlane16_swap_b32 v204, v205\n"
    "v_max_f32 v204, v204, v205\n"
    "v_mfma_f32_16x16x32_bf16 a[228:231], a[240:243], v[164:167], 0\n"
    "v_mul_f32 v212, -1.0, v204\n"
    "v_mul_f32 v213, -1.0, v204\n"
    "buffer_store_dword v184, v184, s[32:35], 0 offen\n"
    "buffer_store_dword v184, v184, s[32:35], 0 offen\n"
    "buffer_store_dword v184, v184, s[32:35], 0 offen\n"
    "buffer_store_dword v184, v184, s[32:35], 0 offen\n"
    "buffer_store_dword v184, v184, s[32:35], 0 offen\n"
    "buffer_store_dword v184, v184, s[32:35], 0 offen\n"
    "buffer_store_dword v184, v184, s[32:35], 0 offen\n"
    "buffer_store_dword v184, v184, s[32:35], 0 offen\n"
    "buffer_store_dword v184, v184, s[32:35], 0 offen\n"
    "buffer_store_dword v184, v184, s[32:35], 0 offen\n"
    "buffer_store_dword v184, v184, s[32:35], 0 offen\n"
    "buffer_store_dword v184, v184, s[32:35], 0 offen\n"
    "buffer_store_dword v184, v184, s[32:35], 0 offen\n"
    "buffer_store_dword v184, v184, s[32:35], 0 offen\n"
    "buffer_store_dword v184, v184, s[32:35], 0 offen\n"
    "buffer_store_dword v184, v184, s[32:35], 0 offen\n"
    "s_branch Lmha_LOOP\n"
    "Lmha_LOOP:\n"
    "v_mfma_f32_16x16x32_bf16 v[128:131], a[128:131], a[0:3], v[248:251]\n"
    "v_mul_f32 v214, -1.0, v204\n"
    "v_mul_f32 v215, -1.0, v204\n"
    "v_sub_f32 v152, v152, v204\n"
    "v_sub_f32 v153, v153, v204\n"
    "v_mfma_f32_16x16x32_bf16 v[128:131], a[132:135], a[4:7], v[128:131]\n"
    "v_sub_f32 v154, v154, v204\n"
    "v_sub_f32 v155, v155, v204\n"
    "v_sub_f32 v156, v156, v204\n"
    "v_mfma_f32_16x16x32_bf16 v[128:131], a[136:139], a[8:11], v[128:131]\n"
    "ds_read_b64_tr_b16 a[196:197], v178 offset:8448\n"
    "v_sub_f32 v157, v157, v204\n"
    "v_sub_f32 v158, v158, v204\n"
    "v_sub_f32 v159, v159, v204\n"
    "v_mfma_f32_16x16x32_bf16 v[128:131], a[140:143], a[12:15], v[128:131]\n"
    "v_mul_f32 v152, s54, v152\n"
    "v_exp_f32 v152, v152\n"
    "v_mfma_f32_16x16x32_bf16 v[132:135], a[144:147], a[0:3], v[248:251]\n"
    "v_mul_f32 v153, s54, v153\n"
    "v_exp_f32 v153, v153\n"
    "v_mul_f32 v154, s54, v154\n"
    "v_mfma_f32_16x16x32_bf16 v[132:135], a[148:151], a[4:7], v[132:135]\n"
    "v_exp_f32 v154, v154\n"
    "v_mul_f32 v155, s54, v155\n"
    "v_mfma_f32_16x16x32_bf16 v[132:135], a[152:155], a[8:11], v[132:135]\n"
    "ds_read_b64_tr_b16 a[198:199], v178 offset:12544\n"
    "v_exp_f32 v155, v155\n"
    "v_mfma_f32_16x16x32_bf16 v[132:135], a[156:159], a[12:15], v[132:135]\n"
    "v_cvt_pk_bf16_f32 v172, v152, v153\n"
    "v_mul_f32 v156, s54, v156\n"
    "v_exp_f32 v156, v156\n"
    "v_mfma_f32_16x16x32_bf16 v[136:139], a[128:131], a[16:19], v[252:255]\n"
    "v_mul_f32 v157, s54, v157\n"
    "v_exp_f32 v157, v157\n"
    "v_mfma_f32_16x16x32_bf16 v[136:139], a[132:135], a[20:23], v[136:139]\n"
    "v_cvt_pk_bf16_f32 v173, v154, v155\n"
    "v_mul_f32 v158, s54, v158\n"
    "v_exp_f32 v158, v158\n"
    "v_mfma_f32_16x16x32_bf16 v[136:139], a[136:139], a[24:27], v[136:139]\n"
    "v_mul_f32 v159, s54, v159\n"
    "v_exp_f32 v159, v159\n"
    "v_cvt_pk_bf16_f32 v174, v156, v157\n"
    "v_mfma_f32_16x16x32_bf16 v[136:139], a[140:143], a[28:31], v[136:139]\n"
    "v_cvt_pk_bf16_f32 v175, v158, v159\n"
    "v_mul_f32 v128, s54, v128\n"
    "v_mfma_f32_16x16x32_bf16 v[140:143], a[144:147], a[16:19], v[252:255]\n"
    "ds_read_b64_tr_b16 a[200:201], v178 offset:8704\n"
    "v_mul_f32 v129, s54, v129\n"
    "v_mfma_f32_16x16x32_bf16 v[140:143], a[148:151], a[20:23], v[140:143]\n"
    "v_exp_f32 v128, v128\n"
    "v_mul_f32 v130, s54, v130\n"
    "v_mfma_f32_16x16x32_bf16 v[140:143], a[152:155], a[24:27], v[140:143]\n"
    "ds_read_b64_tr_b16 a[202:203], v178 offset:12800\n"
    "v_mul_f32 v131, s54, v131\n"
    "v_mfma_f32_16x16x32_bf16 v[140:143], a[156:159], a[28:31], v[140:143]\n"
    "v_exp_f32 v129, v129\n"
    "v_mfma_f32_16x16x32_bf16 v[64:67], a[160:163], v[168:171], 0\n"
    "ds_read_b64_tr_b16 a[204:205], v178 offset:8960\n"
    "v_cvt_pk_bf16_f32 v160, v128, v129\n"
    "v_mfma_f32_16x16x32_bf16 v[68:71], a[164:167], v[168:171], 0\n"
    "v_exp_f32 v130, v130\n"
    "v_mfma_f32_16x16x32_bf16 v[72:75], a[168:171], v[168:171], 0\n"
    "ds_read_b64_tr_b16 a[206:207], v178 offset:13056\n"
    "v_mul_f32 v132, s54, v132\n"
    "v_mfma_f32_16x16x32_bf16 v[76:79], a[172:175], v[168:171], 0\n"
    "v_exp_f32 v131, v131\n"
    "v_mul_f32 v133, s54, v133\n"
    "v_mfma_f32_16x16x32_bf16 v[80:83], a[176:179], v[168:171], 0\n"
    "ds_read_b64_tr_b16 a[208:209], v178 offset:9216\n"
    "v_cvt_pk_bf16_f32 v161, v130, v131\n"
    "v_mfma_f32_16x16x32_bf16 v[84:87], a[180:183], v[168:171], 0\n"
    "v_exp_f32 v132, v132\n"
    "v_mfma_f32_16x16x32_bf16 v[88:91], a[184:187], v[168:171], 0\n"
    "ds_read_b64_tr_b16 a[210:211], v178 offset:13312\n"
    "v_mul_f32 v134, s54, v134\n"
    "v_mfma_f32_16x16x32_bf16 v[92:95], a[188:191], v[168:171], 0\n"
    "v_exp_f32 v133, v133\n"
    "v_mfma_f32_16x16x32_bf16 a[232:235], a[240:243], v[168:171], 0\n"
    "ds_read_b64_tr_b16 a[212:213], v178 offset:9472\n"
    "v_mul_f32 v135, s54, v135\n"
    "v_mfma_f32_16x16x32_bf16 v[96:99], a[160:163], v[172:175], 0\n"
    "v_exp_f32 v134, v134\n"
    "v_cvt_pk_bf16_f32 v162, v132, v133\n"
    "v_mfma_f32_16x16x32_bf16 v[100:103], a[164:167], v[172:175], 0\n"
    "ds_read_b64_tr_b16 a[214:215], v178 offset:13568\n"
    "v_mfma_f32_16x16x32_bf16 v[104:107], a[168:171], v[172:175], 0\n"
    "v_exp_f32 v135, v135\n"
    "v_mfma_f32_16x16x32_bf16 v[108:111], a[172:175], v[172:175], 0\n"
    "v_cvt_pk_bf16_f32 v163, v134, v135\n"
    "v_mul_f32 v136, s54, v136\n"
    "v_mfma_f32_16x16x32_bf16 v[112:115], a[176:179], v[172:175], 0\n"
    "v_exp_f32 v136, v136\n"
    "v_mfma_f32_16x16x32_bf16 v[116:119], a[180:183], v[172:175], 0\n"
    "ds_read_b64_tr_b16 a[216:217], v178 offset:9728\n"
    "v_mul_f32 v137, s54, v137\n"
    "v_mfma_f32_16x16x32_bf16 v[120:123], a[184:187], v[172:175], 0\n"
    "v_exp_f32 v137, v137\n"
    "v_mfma_f32_16x16x32_bf16 v[124:127], a[188:191], v[172:175], 0\n"
    "v_mul_f32 v138, s54, v138\n"
    "v_mul_f32 v139, s54, v139\n"
    "v_mfma_f32_16x16x32_bf16 a[236:239], a[240:243], v[172:175], 0\n"
    "v_exp_f32 v138, v138\n"
    "v_mfma_f32_16x16x32_bf16 v[144:147], a[128:131], a[32:35], v[196:199]\n"
    "ds_read_b64_tr_b16 a[218:219], v178 offset:13824\n"
    "v_cvt_pk_bf16_f32 v164, v136, v137\n"
    "v_mfma_f32_16x16x32_bf16 v[144:147], a[132:135], a[36:39], v[144:147]\n"
    "v_exp_f32 v139, v139\n"
    "v_mfma_f32_16x16x32_bf16 v[144:147], a[136:139], a[40:43], v[144:147]\n"
    "ds_read_b64_tr_b16 a[220:221], v178 offset:9984\n"
    "v_mul_f32 v140, s54, v140\n"
    "v_mul_f32 v141, s54, v141\n"
    "v_mfma_f32_16x16x32_bf16 v[144:147], a[140:143], a[44:47], v[144:147]\n"
    "v_exp_f32 v140, v140\n"
    "v_mfma_f32_16x16x32_bf16 v[148:151], a[144:147], a[32:35], v[196:199]\n"
    "ds_read_b64_tr_b16 a[222:223], v178 offset:14080\n"
    "v_cvt_pk_bf16_f32 v165, v138, v139\n"
    "v_mfma_f32_16x16x32_bf16 v[148:151], a[148:151], a[36:39], v[148:151]\n"
    "v_exp_f32 v141, v141\n"
    "v_mfma_f32_16x16x32_bf16 v[148:151], a[152:155], a[40:43], v[148:151]\n"
    "v_mul_f32 v142, s54, v142\n"
    "v_mul_f32 v143, s54, v143\n"
    "v_mfma_f32_16x16x32_bf16 v[148:151], a[156:159], a[44:47], v[148:151]\n"
    "v_exp_f32 v142, v142\n"
    "v_mfma_f32_16x16x32_bf16 v[152:155], a[128:131], a[48:51], v[212:215]\n"
    "s_waitcnt vmcnt(24) lgkmcnt(0)\n"
    "s_barrier\n"
    "s_cmp_ge_u32 s57, s79\n"
    "s_cbranch_scc1 Lmha_PQ_T1\n"
    "Lmha_PL_T1:\n"
    "s_memrealtime s[94:95]\n"
    "s_waitcnt lgkmcnt(0)\n"
    "s_sub_u32 s94, s94, s78\n"
    "s_cmp_lt_i32 s94, 0\n"
    "s_cbranch_scc0 Lmha_PD_T1\n"
    "s_sleep 1\n"
    "s_branch Lmha_PL_T1\n"
    "Lmha_PD_T1:\n"
    "s_add_u32 s78, s78, s94\n"
    "s_lshr_b32 s94, s65, 2\n"
    "s_add_u32 s78, s78, s94\n"
    "s_sub_u32 s65, s65, 0\n"
    "Lmha_PQ_T1:\n"
    "v_cvt_pk_bf16_f32 v166, v140, v141\n"
    "v_mfma_f32_16x16x32_bf16 v[152:155], a[132:135], a[52:55], v[152:155]\n"
    "v_exp_f32 v143, v143\n"
    "v_mfma_f32_16x16x32_bf16 v[152:155], a[136:139], a[56:59], v[152:155]\n"
    "ds_read_b128 a[128:131], v176 offset:32768\n"
    "v_cvt_pk_bf16_f32 v167, v142, v143\n"
    "v_mfma_f32_16x16x32_bf16 v[152:155], a[140:143], a[60:63], v[152:155]\n"
    "v_mul_f32 v144, s54, v144\n"
    "v_mul_f32 v145, s54, v145\n"
    "v_mfma_f32_16x16x32_bf16 v[156:159], a[144:147], a[48:51], v[212:215]\n"
    "ds_read_b128 a[132:135], v176 offset:33280\n"
    "v_mul_f32 v146, s54, v146\n"
    "v_mfma_f32_16x16x32_bf16 v[156:159], a[148:151], a[52:55], v[156:159]\n"
    "v_exp_f32 v144, v144\n"
    "v_mfma_f32_16x16x32_bf16 v[156:159], a[152:155], a[56:59], v[156:159]\n"
    "ds_read_b128 a[136:139], v176 offset:33792\n"
    "v_mul_f32 v147, s54, v147\n"
    "v_mfma_f32_16x16x32_bf16 v[156:159], a[156:159], a[60:63], v[156:159]\n"
    "v_exp_f32 v145, v145\n"
    "v_mfma_f32_16x16x32_bf16 v[0:3], a[192:195], v[160:163], v[0:3]\n"
    "ds_read_b128 a[140:143], v176 offset:34304\n"
    "v_cvt_pk_bf16_f32 v168, v144, v145\n"
    "v_mfma_f32_16x16x32_bf16 v[4:7], a[196:199], v[160:163], v[4:7]\n"
    "v_exp_f32 v146, v146\n"
    "v_mfma_f32_16x16x32_bf16 v[8:11], a[200:203], v[160:163], v[8:11]\n"
    "ds_read_b128 a[144:147], v176 offset:36864\n"
    "v_mul_f32 v148, s54, v148\n"
    "v_mfma_f32_16x16x32_bf16 v[12:15], a[204:207], v[160:163], v[12:15]\n"
    "v_exp_f32 v147, v147\n"
    "v_mul_f32 v149, s54, v149\n"
    "v_mfma_f32_16x16x32_bf16 v[16:19], a[208:211], v[160:163], v[16:19]\n"
    "ds_read_b128 a[148:151], v176 offset:37376\n"
    "v_cvt_pk_bf16_f32 v169, v146, v147\n"
    "v_mfma_f32_16x16x32_bf16 v[20:23], a[212:215], v[160:163], v[20:23]\n"
    "v_exp_f32 v148, v148\n"
    "v_mfma_f32_16x16x32_bf16 v[24:27], a[216:219], v[160:163], v[24:27]\n"
    "ds_read_b128 a[152:155], v176 offset:37888\n"
    "v_mul_f32 v150, s54, v150\n"
    "v_mfma_f32_16x16x32_bf16 v[28:31], a[220:223], v[160:163], v[28:31]\n"
    "v_exp_f32 v149, v149\n"
    "v_mfma_f32_16x16x32_bf16 a[224:227], a[240:243], v[160:163], a[224:227]\n"
    "ds_read_b128 a[156:159], v176 offset:38400\n"
    "v_mul_f32 v151, s54, v151\n"
    "v_mfma_f32_16x16x32_bf16 v[32:35], a[192:195], v[164:167], v[32:35]\n"
    "v_exp_f32 v150, v150\n"
    "v_mfma_f32_16x16x32_bf16 v[36:39], a[196:199], v[164:167], v[36:39]\n"
    "v_exp_f32 v151, v151\n"
    "v_mfma_f32_16x16x32_bf16 v[40:43], a[200:203], v[164:167], v[40:43]\n"
    "v_cvt_pk_bf16_f32 v170, v148, v149\n"
    "v_cvt_pk_bf16_f32 v171, v150, v151\n"
    "v_mfma_f32_16x16x32_bf16 v[44:47], a[204:207], v[164:167], v[44:47]\n"
    "v_mul_f32 v152, s54, v152\n"
    "v_mul_f32 v153, s54, v153\n"
    "v_mfma_f32_16x16x32_bf16 v[48:51], a[208:211], v[164:167], v[48:51]\n"
    "v_exp_f32 v152, v152\n"
    "v_mfma_f32_16x16x32_bf16 v[52:55], a[212:215], v[164:167], v[52:55]\n"
    "s_add_u32 m0, s52, 0x18000\n"
    "ds_read_b64_tr_b16 a[160:161], v178 offset:32768\n"
    "v_mul_f32 v154, s54, v154\n"
    "v_mfma_f32_16x16x32_bf16 v[56:59], a[216:219], v[164:167], v[56:59]\n"
    "buffer_load_dwordx4 v181, s[36:39], s72 offen offset:0 nt lds\n"
    "v_mul_f32 v155, s54, v155\n"
    "v_mfma_f32_16x16x32_bf16 v[60:63], a[220:223], v[164:167], v[60:63]\n"
    "s_waitcnt lgkmcnt(1)\n"
    "ds_read_b64_tr_b16 a[162:163], v178 offset:36864\n"
    "v_exp_f32 v153, v153\n"
    "v_mfma_f32_16x16x32_bf16 a[228:231], a[240:243], v[164:167], a[228:231]\n"
    "v_exp_f32 v154, v154\n"
    "v_mfma_f32_16x16x32_bf16 v[128:131], a[128:131], a[0:3], v[248:251]\n"
    "v_exp_f32 v155, v155\n"
    "v_mfma_f32_16x16x32_bf16 v[128:131], a[132:135], a[4:7], v[128:131]\n"
    "buffer_load_dwordx4 v182, s[36:39], s72 offen offset:2048 nt lds\n"
    "v_cvt_pk_bf16_f32 v172, v152, v153\n"
    "v_mfma_f32_16x16x32_bf16 v[128:131], a[136:139], a[8:11], v[128:131]\n"
    "ds_read_b64_tr_b16 a[164:165], v178 offset:33024\n"
    "v_mul_f32 v156, s54, v156\n"
    "v_mfma_f32_16x16x32_bf16 v[128:131], a[140:143], a[12:15], v[128:131]\n"
    "v_exp_f32 v156, v156\n"
    "v_mfma_f32_16x16x32_bf16 v[132:135], a[144:147], a[0:3], v[248:251]\n"
    "s_add_u32 m0, s52, 0x18380\n"
    "v_mul_f32 v157, s54, v157\n"
    "v_cvt_pk_bf16_f32 v173, v154, v155\n"
    "v_mfma_f32_16x16x32_bf16 v[132:135], a[148:151], a[4:7], v[132:135]\n"
    "buffer_load_dwordx4 v181, s[36:39], s72 offen offset:128 nt lds\n"
    "v_mul_f32 v158, s54, v158\n"
    "v_mfma_f32_16x16x32_bf16 v[132:135], a[152:155], a[8:11], v[132:135]\n"
    "ds_read_b64_tr_b16 a[166:167], v178 offset:37120\n"
    "v_exp_f32 v157, v157\n"
    "v_mfma_f32_16x16x32_bf16 v[132:135], a[156:159], a[12:15], v[132:135]\n"
    "v_exp_f32 v158, v158\n"
    "v_mfma_f32_16x16x32_bf16 v[136:139], a[128:131], a[16:19], v[252:255]\n"
    "v_mul_f32 v159, s54, v159\n"
    "v_cvt_pk_bf16_f32 v174, v156, v157\n"
    "v_mfma_f32_16x16x32_bf16 v[136:139], a[132:135], a[20:23], v[136:139]\n"
    "buffer_load_dwordx4 v182, s[36:39], s72 offen offset:2176 nt lds\n"
    "v_mfma_f32_16x16x32_bf16 v[136:139], a[136:139], a[24:27], v[136:139]\n"
    "v_exp_f32 v159, v159\n"
    "v_mfma_f32_16x16x32_bf16 v[136:139], a[140:143], a[28:31], v[136:139]\n"
    "v_cvt_pk_bf16_f32 v175, v158, v159\n"
    "v_mul_f32 v128, s54, v128\n"
    "v_mfma_f32_16x16x32_bf16 v[140:143], a[144:147], a[16:19], v[252:255]\n"
    "s_add_u32 m0, s52, 0x1c000\n"
    "ds_read_b64_tr_b16 a[168:169], v178 offset:33280\n"
    "v_exp_f32 v128, v128\n"
    "v_mfma_f32_16x16x32_bf16 v[140:143], a[148:151], a[20:23], v[140:143]\n"
    "buffer_load_dwordx4 v183, s[40:43], s72 offen offset:0 nt lds\n"
    "v_mul_f32 v129, s54, v129\n"
    "v_mfma_f32_16x16x32_bf16 v[140:143], a[152:155], a[24:27], v[140:143]\n"
    "ds_read_b64_tr_b16 a[170:171], v178 offset:37376\n"
    "v_exp_f32 v129, v129\n"
    "v_mfma_f32_16x16x32_bf16 v[140:143], a[156:159], a[28:31], v[140:143]\n"
    "v_mul_f32 v130, s54, v130\n"
    "v_mul_f32 v131, s54, v131\n"
    "v_mfma_f32_16x16x32_bf16 v[64:67], a[192:195], v[168:171], v[64:67]\n"
    "ds_read_b64_tr_b16 a[172:173], v178 offset:33536\n"
    "v_cvt_pk_bf16_f32 v160, v128, v129\n"
    "v_mfma_f32_16x16x32_bf16 v[68:71], a[196:199], v[168:171], v[68:71]\n"
    "buffer_load_dwordx4 v183, s[40:43], s72 offen offset:2048 nt lds\n"
    "v_exp_f32 v130, v130\n"
    "v_mfma_f32_16x16x32_bf16 v[72:75], a[200:203], v[168:171], v[72:75]\n"
    "ds_read_b64_tr_b16 a[174:175], v178 offset:37632\n"
    "v_mul_f32 v132, s54, v132\n"
    "v_mfma_f32_16x16x32_bf16 v[76:79], a[204:207], v[168:171], v[76:79]\n"
    "v_exp_f32 v131, v131\n"
    "v_mul_f32 v133, s54, v133\n"
    "v_mfma_f32_16x16x32_bf16 v[80:83], a[208:211], v[168:171], v[80:83]\n"
    "s_add_u32 m0, s52, 0x1c380\n"
    "ds_read_b64_tr_b16 a[176:177], v178 offset:33792\n"
    "v_cvt_pk_bf16_f32 v161, v130, v131\n"
    "v_mfma_f32_16x16x32_bf16 v[84:87], a[212:215], v[168:171], v[84:87]\n"
    "buffer_load_dwordx4 v183, s[40:43], s72 offen offset:128 nt lds\n"
    "v_exp_f32 v132, v132\n"
    "v_mfma_f32_16x16x32_bf16 v[88:91], a[216:219], v[168:171], v[88:91]\n"
    "ds_read_b64_tr_b16 a[178:179], v178 offset:37888\n"
    "v_mul_f32 v134, s54, v134\n"
    "v_mfma_f32_16x16x32_bf16 v[92:95], a[220:223], v[168:171], v[92:95]\n"
    "v_exp_f32 v133, v133\n"
    "v_mfma_f32_16x16x32_bf16 a[232:235], a[240:243], v[168:171], a[232:235]\n"
    "ds_read_b64_tr_b16 a[180:181], v178 offset:34048\n"
    "v_exp_f32 v134, v134\n"
    "v_mfma_f32_16x16x32_bf16 v[96:99], a[192:195], v[172:175], v[96:99]\n"
    "buffer_load_dwordx4 v183, s[40:43], s72 offen offset:2176 nt lds\n"
    "v_mul_f32 v135, s54, v135\n"
    "v_mfma_f32_16x16x32_bf16 v[100:103], a[196:199], v[172:175], v[100:103]\n"
    "ds_read_b64_tr_b16 a[182:183], v178 offset:38144\n"
    "v_exp_f32 v135, v135\n"
    "v_mfma_f32_16x16x32_bf16 v[104:107], a[200:203], v[172:175], v[104:107]\n"
    "v_cvt_pk_bf16_f32 v162, v132, v133\n"
    "v_cvt_pk_bf16_f32 v163, v134, v135\n"
    "v_mfma_f32_16x16x32_bf16 v[108:111], a[204:207], v[172:175], v[108:111]\n"
    "v_mul_f32 v136, s54, v136\n"
    "v_mul_f32 v137, s54, v137\n"
    "v_mfma_f32_16x16x32_bf16 v[112:115], a[208:211], v[172:175], v[112:115]\n"
    "v_exp_f32 v136, v136\n"
    "v_mfma_f32_16x16x32_bf16 v[116:119], a[212:215], v[172:175], v[116:119]\n"
    "ds_read_b64_tr_b16 a[184:185], v178 offset:34304\n"
    "v_mul_f32 v138, s54, v138\n"
    "v_mfma_f32_16x16x32_bf16 v[120:123], a[216:219], v[172:175], v[120:123]\n"
    "v_exp_f32 v137, v137\n"
    "v_mfma_f32_16x16x32_bf16 v[124:127], a[220:223], v[172:175], v[124:127]\n"
    "v_exp_f32 v138, v138\n"
    "v_mfma_f32_16x16x32_bf16 a[236:239], a[240:243], v[172:175], a[236:239]\n"
    "v_mul_f32 v139, s54, v139\n"
    "v_cvt_pk_bf16_f32 v164, v136, v137\n"
    "v_mfma_f32_16x16x32_bf16 v[144:147], a[128:131], a[32:35], v[196:199]\n"
    "ds_read_b64_tr_b16 a[186:187], v178 offset:38400\n"
    "v_mul_f32 v140, s54, v140\n"
    "v_mfma_f32_16x16x32_bf16 v[144:147], a[132:135], a[36:39], v[144:147]\n"
    "v_exp_f32 v139, v139\n"
    "v_mfma_f32_16x16x32_bf16 v[144:147], a[136:139], a[40:43], v[144:147]\n"
    "ds_read_b64_tr_b16 a[188:189], v178 offset:34560\n"
    "v_mul_f32 v141, s54, v141\n"
    "v_mfma_f32_16x16x32_bf16 v[144:147], a[140:143], a[44:47], v[144:147]\n"
    "v_exp_f32 v140, v140\n"
    "v_mfma_f32_16x16x32_bf16 v[148:151], a[144:147], a[32:35], v[196:199]\n"
    "ds_read_b64_tr_b16 a[190:191], v178 offset:38656\n"
    "v_cvt_pk_bf16_f32 v165, v138, v139\n"
    "v_mfma_f32_16x16x32_bf16 v[148:151], a[148:151], a[36:39], v[148:151]\n"
    "v_exp_f32 v141, v141\n"
    "v_mfma_f32_16x16x32_bf16 v[148:151], a[152:155], a[40:43], v[148:151]\n"
    "v_mul_f32 v142, s54, v142\n"
    "v_mul_f32 v143, s54, v143\n"
    "v_mfma_f32_16x16x32_bf16 v[148:151], a[156:159], a[44:47], v[148:151]\n"
    "v_exp_f32 v142, v142\n"
    "v_mfma_f32_16x16x32_bf16 v[152:155], a[128:131], a[48:51], v[212:215]\n"
    "s_waitcnt lgkmcnt(0)\n"
    "v_exp_f32 v143, v143\n"
    "v_mfma_f32_16x16x32_bf16 v[152:155], a[132:135], a[52:55], v[152:155]\n"
    "v_cvt_pk_bf16_f32 v166, v140, v141\n"
    "v_cvt_pk_bf16_f32 v167, v142, v143\n"
    "v_mfma_f32_16x16x32_bf16 v[152:155], a[136:139], a[56:59], v[152:155]\n"
    "ds_read_b128 a[128:131], v176 offset:40960\n"
    "v_mfma_f32_16x16x32_bf16 v[152:155], a[140:143], a[60:63], v[152:155]\n"
    "v_mul_f32 v144, s54, v144\n"
    "v_mul_f32 v145, s54, v145\n"
    "v_mfma_f32_16x16x32_bf16 v[156:159], a[144:147], a[48:51], v[212:215]\n"
    "ds_read_b128 a[132:135], v176 offset:41472\n"
    "v_mul_f32 v146, s54, v146\n"
    "v_mfma_f32_16x16x32_bf16 v[156:159], a[148:151], a[52:55], v[156:159]\n"
    "v_exp_f32 v144, v144\n"
    "v_mfma_f32_16x16x32_bf16 v[156:159], a[152:155], a[56:59], v[156:159]\n"
    "ds_read_b128 a[136:139], v176 offset:41984\n"
    "v_mul_f32 v147, s54, v147\n"
    "v_mfma_f32_16x16x32_bf16 v[156:159], a[156:159], a[60:63], v[156:159]\n"
    "v_exp_f32 v145, v145\n"
    "v_mfma_f32_16x16x32_bf16 v[0:3], a[160:163], v[160:163], v[0:3]\n"
    "ds_read_b128 a[140:143], v176 offset:42496\n"
    "v_cvt_pk_bf16_f32 v168, v144, v145\n"
    "v_mfma_f32_16x16x32_bf16 v[4:7], a[164:167], v[160:163], v[4:7]\n"
    "v_exp_f32 v146, v146\n"
    "v_mfma_f32_16x16x32_bf16 v[8:11], a[168:171], v[160:163], v[8:11]\n"
    "ds_read_b128 a[144:147], v176 offset:45056\n"
    "v_mul_f32 v148, s54, v148\n"
    "v_mfma_f32_16x16x32_bf16 v[12:15], a[172:175], v[160:163], v[12:15]\n"
    "v_exp_f32 v147, v147\n"
    "v_mul_f32 v149, s54, v149\n"
    "v_mfma_f32_16x16x32_bf16 v[16:19], a[176:179], v[160:163], v[16:19]\n"
    "ds_read_b128 a[148:151], v176 offset:45568\n"
    "v_cvt_pk_bf16_f32 v169, v146, v147\n"
    "v_mfma_f32_16x16x32_bf16 v[20:23], a[180:183], v[160:163], v[20:23]\n"
    "v_exp_f32 v148, v148\n"
    "v_mfma_f32_16x16x32_bf16 v[24:27], a[184:187], v[160:163], v[24:27]\n"
    "ds_read_b128 a[152:155], v176 offset:46080\n"
    "v_mul_f32 v150, s54, v150\n"
    "v_mfma_f32_16x16x32_bf16 v[28:31], a[188:191], v[160:163], v[28:31]\n"
    "v_exp_f32 v149, v149\n"
    "v_mfma_f32_16x16x32_bf16 a[224:227], a[240:243], v[160:163], a[224:227]\n"
    "ds_read_b128 a[156:159], v176 offset:46592\n"
    "v_mul_f32 v151, s54, v151\n"
    "v_mfma_f32_16x16x32_bf16 v[32:35], a[160:163], v[164:167], v[32:35]\n"
    "v_exp_f32 v150, v150\n"
    "v_mfma_f32_16x16x32_bf16 v[36:39], a[164:167], v[164:167], v[36:39]\n"
    "v_exp_f32 v151, v151\n"
    "v_mfma_f32_16x16x32_bf16 v[40:43], a[168:171], v[164:167], v[40:43]\n"
    "v_cvt_pk_bf16_f32 v170, v148, v149\n"
    "v_cvt_pk_bf16_f32 v171, v150, v151\n"
    "v_mfma_f32_16x16x32_bf16 v[44:47], a[172:175], v[164:167], v[44:47]\n"
    "v_mul_f32 v152, s54, v152\n"
    "v_mul_f32 v153, s54, v153\n"
    "v_mfma_f32_16x16x32_bf16 v[48:51], a[176:179], v[164:167], v[48:51]\n"
    "v_exp_f32 v152, v152\n"
    "v_mfma_f32_16x16x32_bf16 v[52:55], a[180:183], v[164:167], v[52:55]\n"
    "ds_read_b64_tr_b16 a[192:193], v178 offset:40960\n"
    "v_mul_f32 v154, s54, v154\n"
    "v_mfma_f32_16x16x32_bf16 v[56:59], a[184:187], v[164:167], v[56:59]\n"
    "v_exp_f32 v153, v153\n"
    "v_mfma_f32_16x16x32_bf16 v[60:63], a[188:191], v[164:167], v[60:63]\n"
    "s_waitcnt lgkmcnt(1)\n"
    "ds_read_b64_tr_b16 a[194:195], v178 offset:45056\n"
    "v_mul_f32 v155, s54, v155\n"
    "v_mfma_f32_16x16x32_bf16 a[228:231], a[240:243], v[164:167], a[228:231]\n"
    "v_exp_f32 v154, v154\n"
    "v_mfma_f32_16x16x32_bf16 v[128:131], a[128:131], a[0:3], v[248:251]\n"
    "v_exp_f32 v155, v155\n"
    "v_mfma_f32_16x16x32_bf16 v[128:131], a[132:135], a[4:7], v[128:131]\n"
    "v_cvt_pk_bf16_f32 v172, v152, v153\n"
    "v_mul_f32 v156, s54, v156\n"
    "v_mfma_f32_16x16x32_bf16 v[128:131], a[136:139], a[8:11], v[128:131]\n"
    "ds_read_b64_tr_b16 a[196:197], v178 offset:41216\n"
    "v_mul_f32 v157, s54, v157\n"
    "v_mfma_f32_16x16x32_bf16 v[128:131], a[140:143], a[12:15], v[128:131]\n"
    "v_exp_f32 v156, v156\n"
    "v_mfma_f32_16x16x32_bf16 v[132:135], a[144:147], a[0:3], v[248:251]\n"
    "v_exp_f32 v157, v157\n"
    "v_mfma_f32_16x16x32_bf16 v[132:135], a[148:151], a[4:7], v[132:135]\n"
    "v_cvt_pk_bf16_f32 v173, v154, v155\n"
    "v_mul_f32 v158, s54, v158\n"
    "v_mfma_f32_16x16x32_bf16 v[132:135], a[152:155], a[8:11], v[132:135]\n"
    "ds_read_b64_tr_b16 a[198:199], v178 offset:45312\n"
    "v_mul_f32 v159, s54, v159\n"
    "v_mfma_f32_16x16x32_bf16 v[132:135], a[156:159], a[12:15], v[132:135]\n"
    "v_exp_f32 v158, v158\n"
    "v_mfma_f32_16x16x32_bf16 v[136:139], a[128:131], a[16:19], v[252:255]\n"
    "v_exp_f32 v159, v159\n"
    "v_mfma_f32_16x16x32_bf16 v[136:139], a[132:135], a[20:23], v[136:139]\n"
    "v_cvt_pk_bf16_f32 v174, v156, v157\n"
    "v_cvt_pk_bf16_f32 v175, v158, v159\n"
    "v_mfma_f32_16x16x32_bf16 v[136:139], a[136:139], a[24:27], v[136:139]\n"
    "v_mfma_f32_16x16x32_bf16 v[136:139], a[140:143], a[28:31], v[136:139]\n"
    "v_mul_f32 v128, s54, v128\n"
    "v_mul_f32 v129, s54, v129\n"
    "v_mfma_f32_16x16x32_bf16 v[140:143], a[144:147], a[16:19], v[252:255]\n"
    "ds_read_b64_tr_b16 a[200:201], v178 offset:41472\n"
    "v_mul_f32 v130, s54, v130\n"
    "v_mfma_f32_16x16x32_bf16 v[140:143], a[148:151], a[20:23], v[140:143]\n"
    "v_exp_f32 v128, v128\n"
    "v_mfma_f32_16x16x32_bf16 v[140:143], a[152:155], a[24:27], v[140:143]\n"
    "ds_read_b64_tr_b16 a[202:203], v178 offset:45568\n"
    "v_mul_f32 v131, s54, v131\n"
    "v_mfma_f32_16x16x32_bf16 v[140:143], a[156:159], a[28:31], v[140:143]\n"
    "v_exp_f32 v129, v129\n"
    "v_mul_f32 v132, s54, v132\n"
    "v_mfma_f32_16x16x32_bf16 v[64:67], a[160:163], v[168:171], v[64:67]\n"
    "ds_read_b64_tr_b16 a[204:205], v178 offset:41728\n"
    "v_cvt_pk_bf16_f32 v160, v128, v129\n"
    "v_mfma_f32_16x16x32_bf16 v[68:71], a[164:167], v[168:171], v[68:71]\n"
    "v_exp_f32 v130, v130\n"
    "v_mfma_f32_16x16x32_bf16 v[72:75], a[168:171], v[168:171], v[72:75]\n"
    "ds_read_b64_tr_b16 a[206:207], v178 offset:45824\n"
    "v_mul_f32 v133, s54, v133\n"
    "v_mfma_f32_16x16x32_bf16 v[76:79], a[172:175], v[168:171], v[76:79]\n"
    "v_exp_f32 v131, v131\n"
    "v_mfma_f32_16x16x32_bf16 v[80:83], a[176:179], v[168:171], v[80:83]\n"
    "ds_read_b64_tr_b16 a[208:209], v178 offset:41984\n"
    "v_cvt_pk_bf16_f32 v161, v130, v131\n"
    "v_mfma_f32_16x16x32_bf16 v[84:87], a[180:183], v[168:171], v[84:87]\n"
    "v_exp_f32 v132, v132\n"
    "v_mfma_f32_16x16x32_bf16 v[88:91], a[184:187], v[168:171], v[88:91]\n"
    "ds_read_b64_tr_b16 a[210:211], v178 offset:46080\n"
    "v_mul_f32 v134, s54, v134\n"
    "v_mfma_f32_16x16x32_bf16 v[92:95], a[188:191], v[168:171], v[92:95]\n"
    "v_exp_f32 v133, v133\n"
    "v_mul_f32 v135, s54, v135\n"
    "v_mfma_f32_16x16x32_bf16 a[232:235], a[240:243], v[168:171], a[232:235]\n"
    "ds_read_b64_tr_b16 a[212:213], v178 offset:42240\n"
    "v_cvt_pk_bf16_f32 v162, v132, v133\n"
    "v_mfma_f32_16x16x32_bf16 v[96:99], a[160:163], v[172:175], v[96:99]\n"
    "v_exp_f32 v134, v134\n"
    "v_mfma_f32_16x16x32_bf16 v[100:103], a[164:167], v[172:175], v[100:103]\n"
    "ds_read_b64_tr_b16 a[214:215], v178 offset:46336\n"
    "v_mfma_f32_16x16x32_bf16 v[104:107], a[168:171], v[172:175], v[104:107]\n"
    "v_exp_f32 v135, v135\n"
    "v_mfma_f32_16x16x32_bf16 v[108:111], a[172:175], v[172:175], v[108:111]\n"
    "v_cvt_pk_bf16_f32 v163, v134, v135\n"
    "v_mul_f32 v136, s54, v136\n"
    "v_mfma_f32_16x16x32_bf16 v[112:115], a[176:179], v[172:175], v[112:115]\n"
    "v_exp_f32 v136, v136\n"
    "v_mfma_f32_16x16x32_bf16 v[116:119], a[180:183], v[172:175], v[116:119]\n"
    "ds_read_b64_tr_b16 a[216:217], v178 offset:42496\n"
    "v_mul_f32 v137, s54, v137\n"
    "v_mfma_f32_16x16x32_bf16 v[120:123], a[184:187], v[172:175], v[120:123]\n"
    "v_exp_f32 v137, v137\n"
    "v_mfma_f32_16x16x32_bf16 v[124:127], a[188:191], v[172:175], v[124:127]\n"
    "v_mul_f32 v138, s54, v138\n"
    "v_mul_f32 v139, s54, v139\n"
    "v_mfma_f32_16x16x32_bf16 a[236:239], a[240:243], v[172:175], a[236:239]\n"
    "v_exp_f32 v138, v138\n"
    "v_mfma_f32_16x16x32_bf16 v[144:147], a[128:131], a[32:35], v[196:199]\n"
    "ds_read_b64_tr_b16 a[218:219], v178 offset:46592\n"
    "v_cvt_pk_bf16_f32 v164, v136, v137\n"
    "v_mfma_f32_16x16x32_bf16 v[144:147], a[132:135], a[36:39], v[144:147]\n"
    "v_exp_f32 v139, v139\n"
    "v_mfma_f32_16x16x32_bf16 v[144:147], a[136:139], a[40:43], v[144:147]\n"
    "ds_read_b64_tr_b16 a[220:221], v178 offset:42752\n"
    "v_mul_f32 v140, s54, v140\n"
    "v_mul_f32 v141, s54, v141\n"
    "v_mfma_f32_16x16x32_bf16 v[144:147], a[140:143], a[44:47], v[144:147]\n"
    "v_exp_f32 v140, v140\n"
    "v_mfma_f32_16x16x32_bf16 v[148:151], a[144:147], a[32:35], v[196:199]\n"
    "ds_read_b64_tr_b16 a[222:223], v178 offset:46848\n"
    "v_cvt_pk_bf16_f32 v165, v138, v139\n"
    "v_mfma_f32_16x16x32_bf16 v[148:151], a[148:151], a[36:39], v[148:151]\n"
    "v_exp_f32 v141, v141\n"
    "v_mfma_f32_16x16x32_bf16 v[148:151], a[152:155], a[40:43], v[148:151]\n"
    "v_mul_f32 v142, s54, v142\n"
    "v_mul_f32 v143, s54, v143\n"
    "v_mfma_f32_16x16x32_bf16 v[148:151], a[156:159], a[44:47], v[148:151]\n"
    "v_exp_f32 v142, v142\n"
    "v_mfma_f32_16x16x32_bf16 v[152:155], a[128:131], a[48:51], v[212:215]\n"
    "s_waitcnt vmcnt(24) lgkmcnt(0)\n"
    "s_barrier\n"
    "s_cmp_ge_u32 s57, s79\n"
    "s_cbranch_scc1 Lmha_PQ_T2\n"
    "s_cmp_eq_u32 s57, 0\n"
    "s_cbranch_scc0 Lmha_PX_T2\n"
    "s_memrealtime s[94:95]\n"
    "s_memtime s[90:91]\n"
    "s_waitcnt lgkmcnt(0)\n"
    "s_mov_b32 s77, s94\n"
    "s_mov_b32 s69, s90\n"
    "Lmha_PX_T2:\n"
    "Lmha_PL_T2:\n"
    "s_memrealtime s[94:95]\n"
    "s_waitcnt lgkmcnt(0)\n"
    "s_sub_u32 s94, s94, s78\n"
    "s_cmp_lt_i32 s94, 0\n"
    "s_cbranch_scc0 Lmha_PD_T2\n"
    "s_sleep 1\n"
    "s_branch Lmha_PL_T2\n"
    "Lmha_PD_T2:\n"
    "s_add_u32 s78, s78, s94\n"
    "s_lshr_b32 s94, s65, 2\n"
    "s_add_u32 s78, s78, s94\n"
    "s_sub_u32 s65, s65, 0\n"
    "Lmha_PQ_T2:\n"
    "v_cvt_pk_bf16_f32 v166, v140, v141\n"
    "v_mfma_f32_16x16x32_bf16 v[152:155], a[132:135], a[52:55], v[152:155]\n"
    "v_exp_f32 v143, v143\n"
    "v_mfma_f32_16x16x32_bf16 v[152:155], a[136:139], a[56:59], v[152:155]\n"
    "s_add_u32 m0, s52, 0x0\n"
    "ds_read_b128 a[128:131], v177 offset:0\n"
    "v_cvt_pk_bf16_f32 v167, v142, v143\n"
    "v_mfma_f32_16x16x32_bf16 v[152:155], a[140:143], a[60:63], v[152:155]\n"
    "buffer_load_dwordx4 v181, s[36:39], s73 offen offset:0 nt lds\n"
    "v_mul_f32 v144, s54, v144\n"
    "v_mfma_f32_16x16x32_bf16 v[156:159], a[144:147], a[48:51], v[212:215]\n"
    "ds_read_b128 a[132:135], v177 offset:512\n"
    "v_exp_f32 v144, v144\n"
    "v_mfma_f32_16x16x32_bf16 v[156:159], a[148:151], a[52:55], v[156:159]\n"
    "s_add_u32 s44, s44, 0x1000000\n"
    "s_addc_u32 s45, s45, 0\n"
    "s_cmp_lt_u32 s57, 3\n"
    "s_cselect_b32 s46, 0x10000, 0\n"
    "v_mfma_f32_16x16x32_bf16 v[156:159], a[152:155], a[56:59], v[156:159]\n"
    "ds_read_b128 a[136:139], v177 offset:1024\n"
    "v_mul_f32 v145, s54, v145\n"
    "v_mul_f32 v146, s54, v146\n"
    "v_mfma_f32_16x16x32_bf16 v[156:159], a[156:159], a[60:63], v[156:159]\n"
    "v_exp_f32 v145, v145\n"
    "v_mfma_f32_16x16x32_bf16 v[0:3], a[192:195], v[160:163], v[0:3]\n"
    "s_add_u32 s58, s53, 0x0\n"
    "s_add_u32 m0, s52, 0x20000\n"
    "ds_read_b128 a[140:143], v177 offset:1536\n"
    "v_mfma_f32_16x16x32_bf16 v[4:7], a[196:199], v[160:163], v[4:7]\n"
    "buffer_load_dwordx4 v181, s[44:47], s58 offen offset:0 nt lds\n"
    "v_exp_f32 v146, v146\n"
    "v_mfma_f32_16x16x32_bf16 v[8:11], a[200:203], v[160:163], v[8:11]\n"
    "ds_read_b128 a[144:147], v177 offset:4096\n"
    "v_mul_f32 v147, s54, v147\n"
    "v_mfma_f32_16x16x32_bf16 v[12:15], a[204:207], v[160:163], v[12:15]\n"
    "v_exp_f32 v147, v147\n"
    "v_cvt_pk_bf16_f32 v168, v144, v145\n"
    "v_mfma_f32_16x16x32_bf16 v[16:19], a[208:211], v[160:163], v[16:19]\n"
    "ds_read_b128 a[148:151], v177 offset:4608\n"
    "v_mul_f32 v148, s54, v148\n"
    "v_mfma_f32_16x16x32_bf16 v[20:23], a[212:215], v[160:163], v[20:23]\n"
    "v_exp_f32 v148, v148\n"
    "v_mul_f32 v149, s54, v149\n"
    "v_mfma_f32_16x16x32_bf16 v[24:27], a[216:219], v[160:163], v[24:27]\n"
    "s_add_u32 m0, s52, 0x0\n"
    "ds_read_b128 a[152:155], v177 offset:5120\n"
    "v_cvt_pk_bf16_f32 v169, v146, v147\n"
    "v_mfma_f32_16x16x32_bf16 v[28:31], a[220:223], v[160:163], v[28:31]\n"
    "buffer_load_dwordx4 v182, s[36:39], s73 offen offset:2048 nt lds\n"
    "v_mul_f32 v150, s54, v150\n"
    "v_mfma_f32_16x16x32_bf16 a[224:227], a[240:243], v[160:163], a[224:227]\n"
    "ds_read_b128 a[156:159], v177 offset:5632\n"
    "v_exp_f32 v149, v149\n"
    "v_mfma_f32_16x16x32_bf16 v[32:35], a[192:195], v[164:167], v[32:35]\n"
    "v_exp_f32 v150, v150\n"
    "v_mfma_f32_16x16x32_bf16 v[36:39], a[196:199], v[164:167], v[36:39]\n"
    "v_mul_f32 v151, s54, v151\n"
    "v_exp_f32 v151, v151\n"
    "v_mfma_f32_16x16x32_bf16 v[40:43], a[200:203], v[164:167], v[40:43]\n"
    "v_cvt_pk_bf16_f32 v170, v148, v149\n"
    "v_cvt_pk_bf16_f32 v171, v150, v151\n"
    "v_mfma_f32_16x16x32_bf16 v[44:47], a[204:207], v[164:167], v[44:47]\n"
    "v_mul_f32 v152, s54, v152\n"
    "v_mul_f32 v153, s54, v153\n"
    "v_mfma_f32_16x16x32_bf16 v[48:51], a[208:211], v[164:167], v[48:51]\n"
    "v_exp_f32 v152, v152\n"
    "v_mfma_f32_16x16x32_bf16 v[52:55], a[212:215], v[164:167], v[52:55]\n"
    "s_add_u32 m0, s52, 0x380\n"
    "ds_read_b64_tr_b16 a[160:161], v179 offset:0\n"
    "v_mul_f32 v154, s54, v154\n"
    "v_mfma_f32_16x16x32_bf16 v[56:59], a[216:219], v[164:167], v[56:59]\n"
    "buffer_load_dwordx4 v181, s[36:39], s73 offen offset:128 nt lds\n"
    "v_mul_f32 v155, s54, v155\n"
    "v_mfma_f32_16x16x32_bf16 v[60:63], a[220:223], v[164:167], v[60:63]\n"
    "s_waitcnt lgkmcnt(1)\n"
    "ds_read_b64_tr_b16 a[162:163], v179 offset:4096\n"
    "v_mul_f32 v156, s54, v156\n"
    "v_mfma_f32_16x16x32_bf16 a[228:231], a[240:243], v[164:167], a[228:231]\n"
    "v_exp_f32 v153, v153\n"
    "v_mfma_f32_16x16x32_bf16 v[128:131], a[128:131], a[0:3], v[248:251]\n"
    "s_add_u32 m0, s52, 0x1fc00\n"
    "v_exp_f32 v154, v154\n"
    "v_mfma_f32_16x16x32_bf16 v[128:131], a[132:135], a[4:7], v[128:131]\n"
    "buffer_load_dwordx4 v182, s[44:47], s58 offen offset:2048 nt lds\n"
    "v_cvt_pk_bf16_f32 v172, v152, v153\n"
    "v_mfma_f32_16x16x32_bf16 v[128:131], a[136:139], a[8:11], v[128:131]\n"
    "ds_read_b64_tr_b16 a[164:165], v179 offset:256\n"
    "v_exp_f32 v155, v155\n"
    "v_mfma_f32_16x16x32_bf16 v[128:131], a[140:143], a[12:15], v[128:131]\n"
    "v_exp_f32 v156, v156\n"
    "v_mfma_f32_16x16x32_bf16 v[132:135], a[144:147], a[0:3], v[248:251]\n"
    "v_mul_f32 v157, s54, v157\n"
    "v_cvt_pk_bf16_f32 v173, v154, v155\n"
    "v_mfma_f32_16x16x32_bf16 v[132:135], a[148:151], a[4:7], v[132:135]\n"
    "v_exp_f32 v157, v157\n"
    "v_mfma_f32_16x16x32_bf16 v[132:135], a[152:155], a[8:11], v[132:135]\n"
    "ds_read_b64_tr_b16 a[166:167], v179 offset:4352\n"
    "v_mul_f32 v158, s54, v158\n"
    "v_mfma_f32_16x16x32_bf16 v[132:135], a[156:159], a[12:15], v[132:135]\n"
    "v_exp_f32 v158, v158\n"
    "v_mfma_f32_16x16x32_bf16 v[136:139], a[128:131], a[16:19], v[252:255]\n"
    "s_add_u32 m0, s52, 0x380\n"
    "v_mul_f32 v159, s54, v159\n"
    "v_cvt_pk_bf16_f32 v174, v156, v157\n"
    "v_mfma_f32_16x16x32_bf16 v[136:139], a[132:135], a[20:23], v[136:139]\n"
    "buffer_load_dwordx4 v182, s[36:39], s73 offen offset:2176 nt lds\n"
    "v_mfma_f32_16x16x32_bf16 v[136:139], a[136:139], a[24:27], v[136:139]\n"
    "v_exp_f32 v159, v159\n"
    "v_mfma_f32_16x16x32_bf16 v[136:139], a[140:143], a[28:31], v[136:139]\n"
    "v_cvt_pk_bf16_f32 v175, v158, v159\n"
    "v_mul_f32 v128, s54, v128\n"
    "v_mfma_f32_16x16x32_bf16 v[140:143], a[144:147], a[16:19], v[252:255]\n"
    "s_add_u32 s58, s53, 0x1000\n"
    "s_add_u32 m0, s52, 0x20800\n"
    "ds_read_b64_tr_b16 a[168:169], v179 offset:512\n"
    "v_mul_f32 v129, s54, v129\n"
    "v_mfma_f32_16x16x32_bf16 v[140:143], a[148:151], a[20:23], v[140:143]\n"
    "buffer_load_dwordx4 v181, s[44:47], s58 offen offset:0 nt lds\n"
    "v_mul_f32 v130, s54, v130\n"
    "v_mfma_f32_16x16x32_bf16 v[140:143], a[152:155], a[24:27], v[140:143]\n"
    "ds_read_b64_tr_b16 a[170:171], v179 offset:4608\n"
    "v_exp_f32 v128, v128\n"
    "v_mfma_f32_16x16x32_bf16 v[140:143], a[156:159], a[28:31], v[140:143]\n"
    "v_exp_f32 v129, v129\n"
    "v_mfma_f32_16x16x32_bf16 v[64:67], a[192:195], v[168:171], v[64:67]\n"
    "ds_read_b64_tr_b16 a[172:173], v179 offset:768\n"
    "v_mul_f32 v131, s54, v131\n"
    "v_mfma_f32_16x16x32_bf16 v[68:71], a[196:199], v[168:171], v[68:71]\n"
    "v_exp_f32 v130, v130\n"
    "v_cvt_pk_bf16_f32 v160, v128, v129\n"
    "v_mfma_f32_16x16x32_bf16 v[72:75], a[200:203], v[168:171], v[72:75]\n"
    "s_add_u32 m0, s52, 0x20400\n"
    "ds_read_b64_tr_b16 a[174:175], v179 offset:4864\n"
    "v_mul_f32 v132, s54, v132\n"
    "v_mfma_f32_16x16x32_bf16 v[76:79], a[204:207], v[168:171], v[76:79]\n"
    "buffer_load_dwordx4 v182, s[44:47], s58 offen offset:2048 nt lds\n"
    "v_exp_f32 v131, v131\n"
    "v_mfma_f32_16x16x32_bf16 v[80:83], a[208:211], v[168:171], v[80:83]\n"
    "ds_read_b64_tr_b16 a[176:177], v179 offset:1024\n"
    "v_mul_f32 v133, s54, v133\n"
    "v_mfma_f32_16x16x32_bf16 v[84:87], a[212:215], v[168:171], v[84:87]\n"
    "v_exp_f32 v132, v132\n"
    "v_cvt_pk_bf16_f32 v161, v130, v131\n"
    "v_mfma_f32_16x16x32_bf16 v[88:91], a[216:219], v[168:171], v[88:91]\n"
    "ds_read_b64_tr_b16 a[178:179], v179 offset:5120\n"
    "v_mul_f32 v134, s54, v134\n"
    "v_mfma_f32_16x16x32_bf16 v[92:95], a[220:223], v[168:171], v[92:95]\n"
    "v_exp_f32 v133, v133\n"
    "v_mfma_f32_16x16x32_bf16 a[232:235], a[240:243], v[168:171], a[232:235]\n"
    "s_add_u32 m0, s52, 0x4000\n"
    "ds_read_b64_tr_b16 a[180:181], v179 offset:1280\n"
    "v_exp_f32 v134, v134\n"
    "v_mfma_f32_16x16x32_bf16 v[96:99], a[192:195], v[172:175], v[96:99]\n"
    "buffer_load_dwordx4 v183, s[40:43], s73 offen offset:0 nt lds\n"
    "v_mul_f32 v135, s54, v135\n"
    "v_mfma_f32_16x16x32_bf16 v[100:103], a[196:199], v[172:175], v[100:103]\n"
    "ds_read_b64_tr_b16 a[182:183], v179 offset:5376\n"
    "v_exp_f32 v135, v135\n"
    "v_mfma_f32_16x16x32_bf16 v[104:107], a[200:203], v[172:175], v[104:107]\n"
    "v_cvt_pk_bf16_f32 v162, v132, v133\n"
    "v_cvt_pk_bf16_f32 v163, v134, v135\n"
    "v_mfma_f32_16x16x32_bf16 v[108:111], a[204:207], v[172:175], v[108:111]\n"
    "s_add_u32 s58, s53, 0x80\n"
    "s_add_u32 m0, s52, 0x24000\n"
    "v_mul_f32 v136, s54, v136\n"
    "v_mfma_f32_16x16x32_bf16 v[112:115], a[208:211], v[172:175], v[112:115]\n"
    "buffer_load_dwordx4 v181, s[44:47], s58 offen offset:0 nt lds\n"
    "v_mul_f32 v137, s54, v137\n"
    "v_mfma_f32_16x16x32_bf16 v[116:119], a[212:215], v[172:175], v[116:119]\n"
    "ds_read_b64_tr_b16 a[184:185], v179 offset:1536\n"
    "v_exp_f32 v136, v136\n"
    "v_mfma_f32_16x16x32_bf16 v[120:123], a[216:219], v[172:175], v[120:123]\n"
    "v_exp_f32 v137, v137\n"
    "v_mfma_f32_16x16x32_bf16 v[124:127], a[220:223], v[172:175], v[124:127]\n"
    "s_add_u32 m0, s52, 0x4000\n"
    "v_mul_f32 v138, s54, v138\n"
    "v_mul_f32 v139, s54, v139\n"
    "v_mfma_f32_16x16x32_bf16 a[236:239], a[240:243], v[172:175], a[236:239]\n"
    "buffer_load_dwordx4 v183, s[40:43], s73 offen offset:2048 nt lds\n"
    "v_cvt_pk_bf16_f32 v164, v136, v137\n"
    "v_mfma_f32_16x16x32_bf16 v[144:147], a[128:131], a[32:35], v[196:199]\n"
    "ds_read_b64_tr_b16 a[186:187], v179 offset:5632\n"
    "v_mul_f32 v140, s54, v140\n"
    "v_mfma_f32_16x16x32_bf16 v[144:147], a[132:135], a[36:39], v[144:147]\n"
    "v_exp_f32 v138, v138\n"
    "v_mfma_f32_16x16x32_bf16 v[144:147], a[136:139], a[40:43], v[144:147]\n"
    "ds_read_b64_tr_b16 a[188:189], v179 offset:1792\n"
    "v_exp_f32 v139, v139\n"
    "v_mfma_f32_16x16x32_bf16 v[144:147], a[140:143], a[44:47], v[144:147]\n"
    "v_exp_f32 v140, v140\n"
    "v_mfma_f32_16x16x32_bf16 v[148:151], a[144:147], a[32:35], v[196:199]\n"
    "s_add_u32 m0, s52, 0x23c00\n"
    "ds_read_b64_tr_b16 a[190:191], v179 offset:5888\n"
    "v_mul_f32 v141, s54, v141\n"
    "v_mfma_f32_16x16x32_bf16 v[148:151], a[148:151], a[36:39], v[148:151]\n"
    "buffer_load_dwordx4 v182, s[44:47], s58 offen offset:2048 nt lds\n"
    "v_cvt_pk_bf16_f32 v165, v138, v139\n"
    "v_mfma_f32_16x16x32_bf16 v[148:151], a[152:155], a[40:43], v[148:151]\n"
    "v_exp_f32 v141, v141\n"
    "v_mfma_f32_16x16x32_bf16 v[148:151], a[156:159], a[44:47], v[148:151]\n"
    "v_mul_f32 v142, s54, v142\n"
    "v_mul_f32 v143, s54, v143\n"
    "v_mfma_f32_16x16x32_bf16 v[152:155], a[128:131], a[48:51], v[212:215]\n"
    "s_waitcnt lgkmcnt(0)\n"
    "v_exp_f32 v142, v142\n"
    "v_cvt_pk_bf16_f32 v166, v140, v141\n"
    "v_mfma_f32_16x16x32_bf16 v[152:155], a[132:135], a[52:55], v[152:155]\n"
    "v_exp_f32 v143, v143\n"
    "v_mfma_f32_16x16x32_bf16 v[152:155], a[136:139], a[56:59], v[152:155]\n"
    "s_add_u32 m0, s52, 0x4380\n"
    "ds_read_b128 a[128:131], v177 offset:8192\n"
    "v_cvt_pk_bf16_f32 v167, v142, v143\n"
    "v_mfma_f32_16x16x32_bf16 v[152:155], a[140:143], a[60:63], v[152:155]\n"
    "buffer_load_dwordx4 v183, s[40:43], s73 offen offset:128 nt lds\n"
    "v_mul_f32 v144, s54, v144\n"
    "v_mfma_f32_16x16x32_bf16 v[156:159], a[144:147], a[48:51], v[212:215]\n"
    "ds_read_b128 a[132:135], v177 offset:8704\n"
    "v_exp_f32 v144, v144\n"
    "v_mfma_f32_16x16x32_bf16 v[156:159], a[148:151], a[52:55], v[156:159]\n"
    "v_mul_f32 v145, s54, v145\n"
    "v_mul_f32 v146, s54, v146\n"
    "v_mfma_f32_16x16x32_bf16 v[156:159], a[152:155], a[56:59], v[156:159]\n"
    "ds_read_b128 a[136:139], v177 offset:9216\n"
    "v_mul_f32 v147, s54, v147\n"
    "v_mfma_f32_16x16x32_bf16 v[156:159], a[156:159], a[60:63], v[156:159]\n"
    "v_exp_f32 v145, v145\n"
    "v_mfma_f32_16x16x32_bf16 v[0:3], a[160:163], v[160:163], v[0:3]\n"
    "s_add_u32 s58, s53, 0x1080\n"
    "s_add_u32 m0, s52, 0x24800\n"
    "ds_read_b128 a[140:143], v177 offset:9728\n"
    "v_cvt_pk_bf16_f32 v168, v144, v145\n"
    "v_mfma_f32_16x16x32_bf16 v[4:7], a[164:167], v[160:163], v[4:7]\n"
    "buffer_load_dwordx4 v181, s[44:47], s58 offen offset:0 nt lds\n"
    "v_mul_f32 v148, s54, v148\n"
    "v_mfma_f32_16x16x32_bf16 v[8:11], a[168:171], v[160:163], v[8:11]\n"
    "ds_read_b128 a[144:147], v177 offset:12288\n"
    "v_mul_f32 v149, s54, v149\n"
    "v_mfma_f32_16x16x32_bf16 v[12:15], a[172:175], v[160:163], v[12:15]\n"
    "v_exp_f32 v146, v146\n"
    "v_mul_f32 v150, s54, v150\n"
    "v_mfma_f32_16x16x32_bf16 v[16:19], a[176:179], v[160:163], v[16:19]\n"
    "ds_read_b128 a[148:151], v177 offset:12800\n"
    "v_mul_f32 v151, s54, v151\n"
    "v_mfma_f32_16x16x32_bf16 v[20:23], a[180:183], v[160:163], v[20:23]\n"
    "v_exp_f32 v147, v147\n"
    "v_mfma_f32_16x16x32_bf16 v[24:27], a[184:187], v[160:163], v[24:27]\n"
    "s_add_u32 m0, s52, 0x4380\n"
    "ds_read_b128 a[152:155], v177 offset:13312\n"
    "v_exp_f32 v148, v148\n"
    "v_mfma_f32_16x16x32_bf16 v[28:31], a[188:191], v[160:163], v[28:31]\n"
    "buffer_load_dwordx4 v183, s[40:43], s73 offen offset:2176 nt lds\n"
    "v_cvt_pk_bf16_f32 v169, v146, v147\n"
    "v_mfma_f32_16x16x32_bf16 a[224:227], a[240:243], v[160:163], a[224:227]\n"
    "ds_read_b128 a[156:159], v177 offset:13824\n"
    "v_mfma_f32_16x16x32_bf16 v[32:35], a[160:163], v[164:167], v[32:35]\n"
    "v_exp_f32 v149, v149\n"
    "v_mfma_f32_16x16x32_bf16 v[36:39], a[164:167], v[164:167], v[36:39]\n"
    "v_exp_f32 v150, v150\n"
    "v_cvt_pk_bf16_f32 v170, v148, v149\n"
    "v_mfma_f32_16x16x32_bf16 v[40:43], a[168:171], v[164:167], v[40:43]\n"
    "v_exp_f32 v151, v151\n"
    "v_mfma_f32_16x16x32_bf16 v[44:47], a[172:175], v[164:167], v[44:47]\n"
    "s_add_u32 m0, s52, 0x24400\n"
    "v_cvt_pk_bf16_f32 v171, v150, v151\n"
    "v_mul_f32 v152, s54, v152\n"
    "v_mfma_f32_16x16x32_bf16 v[48:51], a[176:179], v[164:167], v[48:51]\n"
    "buffer_load_dwordx4 v182, s[44:47], s58 offen offset:2048 nt lds\n"
    "v_mul_f32 v153, s54, v153\n"
    "v_mfma_f32_16x16x32_bf16 v[52:55], a[180:183], v[164:167], v[52:55]\n"
    "ds_read_b64_tr_b16 a[192:193], v179 offset:8192\n"
    "v_mul_f32 v154, s54, v154\n"
    "v_mfma_f32_16x16x32_bf16 v[56:59], a[184:187], v[164:167], v[56:59]\n"
    "v_exp_f32 v152, v152\n"
    "v_mfma_f32_16x16x32_bf16 v[60:63], a[188:191], v[164:167], v[60:63]\n"
    "s_waitcnt lgkmcnt(1)\n"
    "ds_read_b64_tr_b16 a[194:195], v179 offset:12288\n"
    "v_mul_f32 v155, s54, v155\n"
    "v_mfma_f32_16x16x32_bf16 a[228:231], a[240:243], v[164:167], a[228:231]\n"
    "v_exp_f32 v153, v153\n"
    "v_mfma_f32_16x16x32_bf16 v[128:131], a[128:131], a[0:3], v[248:251]\n"
    "v_exp_f32 v154, v154\n"
    "v_mfma_f32_16x16x32_bf16 v[128:131], a[132:135], a[4:7], v[128:131]\n"
    "v_exp_f32 v155, v155\n"
    "v_mfma_f32_16x16x32_bf16 v[128:131], a[136:139], a[8:11], v[128:131]\n"
    "ds_read_b64_tr_b16 a[196:197], v179 offset:8448\n"
    "v_cvt_pk_bf16_f32 v172, v152, v153\n"
    "v_mfma_f32_16x16x32_bf16 v[128:131], a[140:143], a[12:15], v[128:131]\n"
    "v_mul_f32 v156, s54, v156\n"
    "v_mul_f32 v157, s54, v157\n"
    "v_mfma_f32_16x16x32_bf16 v[132:135], a[144:147], a[0:3], v[248:251]\n"
    "v_exp_f32 v156, v156\n"
    "v_mfma_f32_16x16x32_bf16 v[132:135], a[148:151], a[4:7], v[132:135]\n"
    "v_exp_f32 v157, v157\n"
    "v_mfma_f32_16x16x32_bf16 v[132:135], a[152:155], a[8:11], v[132:135]\n"
    "ds_read_b64_tr_b16 a[198:199], v179 offset:12544\n"
    "v_cvt_pk_bf16_f32 v173, v154, v155\n"
    "v_mfma_f32_16x16x32_bf16 v[132:135], a[156:159], a[12:15], v[132:135]\n"
    "v_mul_f32 v158, s54, v158\n"
    "v_mul_f32 v159, s54, v159\n"
    "v_mfma_f32_16x16x32_bf16 v[136:139], a[128:131], a[16:19], v[252:255]\n"
    "v_exp_f32 v158, v158\n"
    "v_mfma_f32_16x16x32_bf16 v[136:139], a[132:135], a[20:23], v[136:139]\n"
    "v_exp_f32 v159, v159\n"
    "v_mfma_f32_16x16x32_bf16 v[136:139], a[136:139], a[24:27], v[136:139]\n"
    "v_cvt_pk_bf16_f32 v174, v156, v157\n"
    "v_cvt_pk_bf16_f32 v175, v158, v159\n"
    "v_mfma_f32_16x16x32_bf16 v[136:139], a[140:143], a[28:31], v[136:139]\n"
    "v_mul_f32 v128, s54, v128\n"
    "v_mul_f32 v129, s54, v129\n"
    "v_mfma_f32_16x16x32_bf16 v[140:143], a[144:147], a[16:19], v[252:255]\n"
    "ds_read_b64_tr_b16 a[200:201], v179 offset:8704\n"
    "v_mul_f32 v130, s54, v130\n"
    "v_mfma_f32_16x16x32_bf16 v[140:143], a[148:151], a[20:23], v[140:143]\n"
    "v_exp_f32 v128, v128\n"
    "v_mfma_f32_16x16x32_bf16 v[140:143], a[152:155], a[24:27], v[140:143]\n"
    "ds_read_b64_tr_b16 a[202:203], v179 offset:12800\n"
    "v_mul_f32 v131, s54, v131\n"
    "v_mfma_f32_16x16x32_bf16 v[140:143], a[156:159], a[28:31], v[140:143]\n"
    "v_exp_f32 v129, v129\n"
    "v_mul_f32 v132, s54, v132\n"
    "v_mfma_f32_16x16x32_bf16 v[64:67], a[160:163], v[168:171], v[64:67]\n"
    "ds_read_b64_tr_b16 a[204:205], v179 offset:8960\n"
    "v_cvt_pk_bf16_f32 v160, v128, v129\n"
    "v_mfma_f32_16x16x32_bf16 v[68:71], a[164:167], v[168:171], v[68:71]\n"
    "v_exp_f32 v130, v130\n"
    "v_mfma_f32_16x16x32_bf16 v[72:75], a[168:171], v[168:171], v[72:75]\n"
    "ds_read_b64_tr_b16 a[206:207], v179 offset:13056\n"
    "v_mul_f32 v133, s54, v133\n"
    "v_mfma_f32_16x16x32_bf16 v[76:79], a[172:175], v[168:171], v[76:79]\n"
    "v_exp_f32 v131, v131\n"
    "v_mfma_f32_16x16x32_bf16 v[80:83], a[176:179], v[168:171], v[80:83]\n"
    "ds_read_b64_tr_b16 a[208:209], v179 offset:9216\n"
    "v_cvt_pk_bf16_f32 v161, v130, v131\n"
    "v_mfma_f32_16x16x32_bf16 v[84:87], a[180:183], v[168:171], v[84:87]\n"
    "v_exp_f32 v132, v132\n"
    "v_mfma_f32_16x16x32_bf16 v[88:91], a[184:187], v[168:171], v[88:91]\n"
    "ds_read_b64_tr_b16 a[210:211], v179 offset:13312\n"
    "v_mul_f32 v134, s54, v134\n"
    "v_mfma_f32_16x16x32_bf16 v[92:95], a[188:191], v[168:171], v[92:95]\n"
    "v_exp_f32 v133, v133\n"
    "v_mul_f32 v135, s54, v135\n"
    "v_mfma_f32_16x16x32_bf16 a[232:235], a[240:243], v[168:171], a[232:235]\n"
    "ds_read_b64_tr_b16 a[212:213], v179 offset:9472\n"
    "v_cvt_pk_bf16_f32 v162, v132, v133\n"
    "v_mfma_f32_16x16x32_bf16 v[96:99], a[160:163], v[172:175], v[96:99]\n"
    "v_exp_f32 v134, v134\n"
    "v_mfma_f32_16x16x32_bf16 v[100:103], a[164:167], v[172:175], v[100:103]\n"
    "ds_read_b64_tr_b16 a[214:215], v179 offset:13568\n"
    "v_mfma_f32_16x16x32_bf16 v[104:107], a[168:171], v[172:175], v[104:107]\n"
    "v_exp_f32 v135, v135\n"
    "v_mfma_f32_16x16x32_bf16 v[108:111], a[172:175], v[172:175], v[108:111]\n"
    "v_cvt_pk_bf16_f32 v163, v134, v135\n"
    "v_mul_f32 v136, s54, v136\n"
    "v_mfma_f32_16x16x32_bf16 v[112:115], a[176:179], v[172:175], v[112:115]\n"
    "v_exp_f32 v136, v136\n"
    "v_mfma_f32_16x16x32_bf16 v[116:119], a[180:183], v[172:175], v[116:119]\n"
    "ds_read_b64_tr_b16 a[216:217], v179 offset:9728\n"
    "v_mul_f32 v137, s54, v137\n"
    "v_mfma_f32_16x16x32_bf16 v[120:123], a[184:187], v[172:175], v[120:123]\n"
    "v_exp_f32 v137, v137\n"
    "v_mfma_f32_16x16x32_bf16 v[124:127], a[188:191], v[172:175], v[124:127]\n"
    "v_mul_f32 v138, s54, v138\n"
    "v_mul_f32 v139, s54, v139\n"
    "v_mfma_f32_16x16x32_bf16 a[236:239], a[240:243], v[172:175], a[236:239]\n"
    "v_exp_f32 v138, v138\n"
    "v_mfma_f32_16x16x32_bf16 v[144:147], a[128:131], a[32:35], v[196:199]\n"
    "ds_read_b64_tr_b16 a[218:219], v179 offset:13824\n"
    "v_cvt_pk_bf16_f32 v164, v136, v137\n"
    "v_mfma_f32_16x16x32_bf16 v[144:147], a[132:135], a[36:39], v[144:147]\n"
    "v_exp_f32 v139, v139\n"
    "v_mfma_f32_16x16x32_bf16 v[144:147], a[136:139], a[40:43], v[144:147]\n"
    "ds_read_b64_tr_b16 a[220:221], v179 offset:9984\n"
    "v_mul_f32 v140, s54, v140\n"
    "v_mul_f32 v141, s54, v141\n"
    "v_mfma_f32_16x16x32_bf16 v[144:147], a[140:143], a[44:47], v[144:147]\n"
    "v_exp_f32 v140, v140\n"
    "v_mfma_f32_16x16x32_bf16 v[148:151], a[144:147], a[32:35], v[196:199]\n"
    "ds_read_b64_tr_b16 a[222:223], v179 offset:14080\n"
    "v_cvt_pk_bf16_f32 v165, v138, v139\n"
    "v_mfma_f32_16x16x32_bf16 v[148:151], a[148:151], a[36:39], v[148:151]\n"
    "v_exp_f32 v141, v141\n"
    "v_mfma_f32_16x16x32_bf16 v[148:151], a[152:155], a[40:43], v[148:151]\n"
    "v_mul_f32 v142, s54, v142\n"
    "v_mul_f32 v143, s54, v143\n"
    "v_mfma_f32_16x16x32_bf16 v[148:151], a[156:159], a[44:47], v[148:151]\n"
    "v_exp_f32 v142, v142\n"
    "v_mfma_f32_16x16x32_bf16 v[152:155], a[128:131], a[48:51], v[212:215]\n"
    "s_waitcnt vmcnt(16) lgkmcnt(0)\n"
    "s_barrier\n"
    "s_cmp_ge_u32 s57, s79\n"
    "s_cbranch_scc1 Lmha_PQ_T3\n"
    "s_cmp_eq_u32 s57, 0\n"
    "s_cbranch_scc0 Lmha_PX_T3\n"
    "s_memrealtime s[94:95]\n"
    "s_memtime s[90:91]\n"
    "s_waitcnt lgkmcnt(0)\n"
    "s_sub_u32 s94, s94, s77\n"
    "s_sub_u32 s90, s90, s69\n"
    "s_mul_i32 s94, s94, 152\n"
    "s_mul_i32 s90, s90, 10\n"
    "s_cmp_lt_u32 s90, s94\n"
    "s_cselect_b32 s79, 0, s79\n"
    "Lmha_PX_T3:\n"
    "Lmha_PL_T3:\n"
    "s_memrealtime s[94:95]\n"
    "s_waitcnt lgkmcnt(0)\n"
    "s_sub_u32 s94, s94, s78\n"
    "s_cmp_lt_i32 s94, 0\n"
    "s_cbranch_scc0 Lmha_PD_T3\n"
    "s_sleep 1\n"
    "s_branch Lmha_PL_T3\n"
    "Lmha_PD_T3:\n"
    "s_add_u32 s78, s78, s94\n"
    "s_lshr_b32 s94, s65, 2\n"
    "s_add_u32 s78, s78, s94\n"
    "s_sub_u32 s65, s65, 0\n"
    "Lmha_PQ_T3:\n"
    "v_cvt_pk_bf16_f32 v166, v140, v141\n"
    "v_mfma_f32_16x16x32_bf16 v[152:155], a[132:135], a[52:55], v[152:155]\n"
    "v_exp_f32 v143, v143\n"
    "v_mfma_f32_16x16x32_bf16 v[152:155], a[136:139], a[56:59], v[152:155]\n"
    "s_add_u32 m0, s52, 0x8000\n"
    "ds_read_b128 a[128:131], v177 offset:32768\n"
    "v_cvt_pk_bf16_f32 v167, v142, v143\n"
    "v_mfma_f32_16x16x32_bf16 v[152:155], a[140:143], a[60:63], v[152:155]\n"
    "buffer_load_dwordx4 v181, s[36:39], s74 offen offset:0 nt lds\n"
    "v_mul_f32 v144, s54, v144\n"
    "v_mfma_f32_16x16x32_bf16 v[156:159], a[144:147], a[48:51], v[212:215]\n"
    "ds_read_b128 a[132:135], v177 offset:33280\n"
    "v_mul_f32 v145, s54, v145\n"
    "v_mfma_f32_16x16x32_bf16 v[156:159], a[148:151], a[52:55], v[156:159]\n"
    "v_exp_f32 v144, v144\n"
    "v_mul_f32 v146, s54, v146\n"
    "v_mfma_f32_16x16x32_bf16 v[156:159], a[152:155], a[56:59], v[156:159]\n"
    "ds_read_b128 a[136:139], v177 offset:33792\n"
    "v_mul_f32 v147, s54, v147\n"
    "v_mfma_f32_16x16x32_bf16 v[156:159], a[156:159], a[60:63], v[156:159]\n"
    "v_exp_f32 v145, v145\n"
    "v_mfma_f32_16x16x32_bf16 v[0:3], a[192:195], v[160:163], v[0:3]\n"
    "ds_read_b128 a[140:143], v177 offset:34304\n"
    "v_cvt_pk_bf16_f32 v168, v144, v145\n"
    "v_mfma_f32_16x16x32_bf16 v[4:7], a[196:199], v[160:163], v[4:7]\n"
    "v_exp_f32 v146, v146\n"
    "v_mfma_f32_16x16x32_bf16 v[8:11], a[200:203], v[160:163], v[8:11]\n"
    "ds_read_b128 a[144:147], v177 offset:36864\n"
    "v_mul_f32 v148, s54, v148\n"
    "v_mfma_f32_16x16x32_bf16 v[12:15], a[204:207], v[160:163], v[12:15]\n"
    "v_exp_f32 v147, v147\n"
    "v_mul_f32 v149, s54, v149\n"
    "v_mfma_f32_16x16x32_bf16 v[16:19], a[208:211], v[160:163], v[16:19]\n"
    "ds_read_b128 a[148:151], v177 offset:37376\n"
    "v_cvt_pk_bf16_f32 v169, v146, v147\n"
    "v_mfma_f32_16x16x32_bf16 v[20:23], a[212:215], v[160:163], v[20:23]\n"
    "v_exp_f32 v148, v148\n"
    "v_mfma_f32_16x16x32_bf16 v[24:27], a[216:219], v[160:163], v[24:27]\n"
    "ds_read_b128 a[152:155], v177 offset:37888\n"
    "v_mul_f32 v150, s54, v150\n"
    "v_mfma_f32_16x16x32_bf16 v[28:31], a[220:223], v[160:163], v[28:31]\n"
    "buffer_load_dwordx4 v182, s[36:39], s74 offen offset:2048 nt lds\n"
    "v_mul_f32 v151, s54, v151\n"
    "v_mfma_f32_16x16x32_bf16 a[224:227], a[240:243], v[160:163], a[224:227]\n"
    "ds_read_b128 a[156:159], v177 offset:38400\n"
    "v_mfma_f32_16x16x32_bf16 v[32:35], a[192:195], v[164:167], v[32:35]\n"
    "v_exp_f32 v149, v149\n"
    "v_mfma_f32_16x16x32_bf16 v[36:39], a[196:199], v[164:167], v[36:39]\n"
    "v_exp_f32 v150, v150\n"
    "v_mfma_f32_16x16x32_bf16 v[40:43], a[200:203], v[164:167], v[40:43]\n"
    "v_exp_f32 v151, v151\n"
    "v_cvt_pk_bf16_f32 v170, v148, v149\n"
    "v_mfma_f32_16x16x32_bf16 v[44:47], a[204:207], v[164:167], v[44:47]\n"
    "v_cvt_pk_bf16_f32 v171, v150, v151\n"
    "v_mul_f32 v152, s54, v152\n"
    "v_mfma_f32_16x16x32_bf16 v[48:51], a[208:211], v[164:167], v[48:51]\n"
    "v_exp_f32 v152, v152\n"
    "v_mfma_f32_16x16x32_bf16 v[52:55], a[212:215], v[164:167], v[52:55]\n"
    "s_add_u32 m0, s52, 0x8380\n"
    "ds_read_b64_tr_b16 a[160:161], v179 offset:32768\n"
    "v_mul_f32 v153, s54, v153\n"
    "v_mfma_f32_16x16x32_bf16 v[56:59], a[216:219], v[164:167], v[56:59]\n"
    "buffer_load_dwordx4 v181, s[36:39], s74 offen offset:128 nt lds\n"
    "v_mul_f32 v154, s54, v154\n"
    "v_mfma_f32_16x16x32_bf16 v[60:63], a[220:223], v[164:167], v[60:63]\n"
    "s_waitcnt lgkmcnt(1)\n"
    "ds_read_b64_tr_b16 a[162:163], v179 offset:36864\n"
    "v_mul_f32 v155, s54, v155\n"
    "v_mfma_f32_16x16x32_bf16 a[228:231], a[240:243], v[164:167], a[228:231]\n"
    "v_exp_f32 v153, v153\n"
    "v_mfma_f32_16x16x32_bf16 v[128:131], a[128:131], a[0:3], v[248:251]\n"
    "v_exp_f32 v154, v154\n"
    "v_mfma_f32_16x16x32_bf16 v[128:131], a[132:135], a[4:7], v[128:131]\n"
    "v_exp_f32 v155, v155\n"
    "v_mfma_f32_16x16x32_bf16 v[128:131], a[136:139], a[8:11], v[128:131]\n"
    "ds_read_b64_tr_b16 a[164:165], v179 offset:33024\n"
    "v_cvt_pk_bf16_f32 v172, v152, v153\n"
    "v_mul_f32 v156, s54, v156\n"
    "v_mfma_f32_16x16x32_bf16 v[128:131], a[140:143], a[12:15], v[128:131]\n"
    "v_exp_f32 v156, v156\n"
    "v_mfma_f32_16x16x32_bf16 v[132:135], a[144:147], a[0:3], v[248:251]\n"
    "v_mul_f32 v157, s54, v157\n"
    "v_cvt_pk_bf16_f32 v173, v154, v155\n"
    "v_mfma_f32_16x16x32_bf16 v[132:135], a[148:151], a[4:7], v[132:135]\n"
    "v_exp_f32 v157, v157\n"
    "v_mfma_f32_16x16x32_bf16 v[132:135], a[152:155], a[8:11], v[132:135]\n"
    "ds_read_b64_tr_b16 a[166:167], v179 offset:37120\n"
    "v_mul_f32 v158, s54, v158\n"
    "v_mfma_f32_16x16x32_bf16 v[132:135], a[156:159], a[12:15], v[132:135]\n"
    "v_exp_f32 v158, v158\n"
    "v_mfma_f32_16x16x32_bf16 v[136:139], a[128:131], a[16:19], v[252:255]\n"
    "v_mul_f32 v159, s54, v159\n"
    "v_cvt_pk_bf16_f32 v174, v156, v157\n"
    "v_mfma_f32_16x16x32_bf16 v[136:139], a[132:135], a[20:23], v[136:139]\n"
    "buffer_load_dwordx4 v182, s[36:39], s74 offen offset:2176 nt lds\n"
    "v_mfma_f32_16x16x32_bf16 v[136:139], a[136:139], a[24:27], v[136:139]\n"
    "v_exp_f32 v159, v159\n"
    "v_mfma_f32_16x16x32_bf16 v[136:139], a[140:143], a[28:31], v[136:139]\n"
    "v_cvt_pk_bf16_f32 v175, v158, v159\n"
    "v_mul_f32 v128, s54, v128\n"
    "v_mfma_f32_16x16x32_bf16 v[140:143], a[144:147], a[16:19], v[252:255]\n"
    "ds_read_b64_tr_b16 a[168:169], v179 offset:33280\n"
    "v_mul_f32 v129, s54, v129\n"
    "v_mfma_f32_16x16x32_bf16 v[140:143], a[148:151], a[20:23], v[140:143]\n"
    "v_exp_f32 v128, v128\n"
    "v_mul_f32 v130, s54, v130\n"
    "v_mfma_f32_16x16x32_bf16 v[140:143], a[152:155], a[24:27], v[140:143]\n"
    "ds_read_b64_tr_b16 a[170:171], v179 offset:37376\n"
    "v_mul_f32 v131, s54, v131\n"
    "v_mfma_f32_16x16x32_bf16 v[140:143], a[156:159], a[28:31], v[140:143]\n"
    "v_exp_f32 v129, v129\n"
    "v_mfma_f32_16x16x32_bf16 v[64:67], a[192:195], v[168:171], v[64:67]\n"
    "ds_read_b64_tr_b16 a[172:173], v179 offset:33536\n"
    "v_cvt_pk_bf16_f32 v160, v128, v129\n"
    "v_mfma_f32_16x16x32_bf16 v[68:71], a[196:199], v[168:171], v[68:71]\n"
    "v_exp_f32 v130, v130\n"
    "v_mul_f32 v132, s54, v132\n"
    "v_mfma_f32_16x16x32_bf16 v[72:75], a[200:203], v[168:171], v[72:75]\n"
    "ds_read_b64_tr_b16 a[174:175], v179 offset:37632\n"
    "v_mul_f32 v133, s54, v133\n"
    "v_mfma_f32_16x16x32_bf16 v[76:79], a[204:207], v[168:171], v[76:79]\n"
    "v_exp_f32 v131, v131\n"
    "v_mfma_f32_16x16x32_bf16 v[80:83], a[208:211], v[168:171], v[80:83]\n"
    "ds_read_b64_tr_b16 a[176:177], v179 offset:33792\n"
    "v_cvt_pk_bf16_f32 v161, v130, v131\n"
    "v_mfma_f32_16x16x32_bf16 v[84:87], a[212:215], v[168:171], v[84:87]\n"
    "v_exp_f32 v132, v132\n"
    "v_mul_f32 v134, s54, v134\n"
    "v_mfma_f32_16x16x32_bf16 v[88:91], a[216:219], v[168:171], v[88:91]\n"
    "ds_read_b64_tr_b16 a[178:179], v179 offset:37888\n"
    "v_mul_f32 v135, s54, v135\n"
    "v_mfma_f32_16x16x32_bf16 v[92:95], a[220:223], v[168:171], v[92:95]\n"
    "v_exp_f32 v133, v133\n"
    "v_mfma_f32_16x16x32_bf16 a[232:235], a[240:243], v[168:171], a[232:235]\n"
    "s_add_u32 m0, s52, 0xc000\n"
    "ds_read_b64_tr_b16 a[180:181], v179 offset:34048\n"
    "v_cvt_pk_bf16_f32 v162, v132, v133\n"
    "v_mfma_f32_16x16x32_bf16 v[96:99], a[192:195], v[172:175], v[96:99]\n"
    "buffer_load_dwordx4 v183, s[40:43], s74 offen offset:0 nt lds\n"
    "v_exp_f32 v134, v134\n"
    "v_mfma_f32_16x16x32_bf16 v[100:103], a[196:199], v[172:175], v[100:103]\n"
    "ds_read_b64_tr_b16 a[182:183], v179 offset:38144\n"
    "v_mfma_f32_16x16x32_bf16 v[104:107], a[200:203], v[172:175], v[104:107]\n"
    "v_exp_f32 v135, v135\n"
    "v_mfma_f32_16x16x32_bf16 v[108:111], a[204:207], v[172:175], v[108:111]\n"
    "v_cvt_pk_bf16_f32 v163, v134, v135\n"
    "v_mul_f32 v136, s54, v136\n"
    "v_mfma_f32_16x16x32_bf16 v[112:115], a[208:211], v[172:175], v[112:115]\n"
    "v_exp_f32 v136, v136\n"
    "v_mfma_f32_16x16x32_bf16 v[116:119], a[212:215], v[172:175], v[116:119]\n"
    "ds_read_b64_tr_b16 a[184:185], v179 offset:34304\n"
    "v_mul_f32 v137, s54, v137\n"
    "v_mfma_f32_16x16x32_bf16 v[120:123], a[216:219], v[172:175], v[120:123]\n"
    "v_exp_f32 v137, v137\n"
    "v_mfma_f32_16x16x32_bf16 v[124:127], a[220:223], v[172:175], v[124:127]\n"
    "v_mul_f32 v138, s54, v138\n"
    "v_mul_f32 v139, s54, v139\n"
    "v_mfma_f32_16x16x32_bf16 a[236:239], a[240:243], v[172:175], a[236:239]\n"
    "buffer_load_dwordx4 v183, s[40:43], s74 offen offset:2048 nt lds\n"
    "v_cvt_pk_bf16_f32 v164, v136, v137\n"
    "v_mfma_f32_16x16x32_bf16 v[144:147], a[128:131], a[32:35], v[196:199]\n"
    "ds_read_b64_tr_b16 a[186:187], v179 offset:38400\n"
    "v_mul_f32 v140, s54, v140\n"
    "v_mfma_f32_16x16x32_bf16 v[144:147], a[132:135], a[36:39], v[144:147]\n"
    "v_exp_f32 v138, v138\n"
    "v_mfma_f32_16x16x32_bf16 v[144:147], a[136:139], a[40:43], v[144:147]\n"
    "ds_read_b64_tr_b16 a[188:189], v179 offset:34560\n"
    "v_exp_f32 v139, v139\n"
    "v_mfma_f32_16x16x32_bf16 v[144:147], a[140:143], a[44:47], v[144:147]\n"
    "v_exp_f32 v140, v140\n"
    "v_mfma_f32_16x16x32_bf16 v[148:151], a[144:147], a[32:35], v[196:199]\n"
    "ds_read_b64_tr_b16 a[190:191], v179 offset:38656\n"
    "v_mul_f32 v141, s54, v141\n"
    "v_mfma_f32_16x16x32_bf16 v[148:151], a[148:151], a[36:39], v[148:151]\n"
    "v_exp_f32 v141, v141\n"
    "v_mfma_f32_16x16x32_bf16 v[148:151], a[152:155], a[40:43], v[148:151]\n"
    "v_cvt_pk_bf16_f32 v165, v138, v139\n"
    "v_mul_f32 v142, s54, v142\n"
    "v_mfma_f32_16x16x32_bf16 v[148:151], a[156:159], a[44:47], v[148:151]\n"
    "v_exp_f32 v142, v142\n"
    "v_mfma_f32_16x16x32_bf16 v[152:155], a[128:131], a[48:51], v[212:215]\n"
    "s_waitcnt lgkmcnt(0)\n"
    "v_mul_f32 v143, s54, v143\n"
    "v_cvt_pk_bf16_f32 v166, v140, v141\n"
    "v_mfma_f32_16x16x32_bf16 v[152:155], a[132:135], a[52:55], v[152:155]\n"
    "v_exp_f32 v143, v143\n"
    "v_mfma_f32_16x16x32_bf16 v[152:155], a[136:139], a[56:59], v[152:155]\n"
    "s_add_u32 m0, s52, 0xc380\n"
    "ds_read_b128 a[128:131], v177 offset:40960\n"
    "v_cvt_pk_bf16_f32 v167, v142, v143\n"
    "v_mfma_f32_16x16x32_bf16 v[152:155], a[140:143], a[60:63], v[152:155]\n"
    "buffer_load_dwordx4 v183, s[40:43], s74 offen offset:128 nt lds\n"
    "v_mul_f32 v144, s54, v144\n"
    "v_mfma_f32_16x16x32_bf16 v[156:159], a[144:147], a[48:51], v[212:215]\n"
    "ds_read_b128 a[132:135], v177 offset:41472\n"
    "v_mul_f32 v145, s54, v145\n"
    "v_mfma_f32_16x16x32_bf16 v[156:159], a[148:151], a[52:55], v[156:159]\n"
    "v_exp_f32 v144, v144\n"
    "v_mul_f32 v146, s54, v146\n"
    "v_mfma_f32_16x16x32_bf16 v[156:159], a[152:155], a[56:59], v[156:159]\n"
    "ds_read_b128 a[136:139], v177 offset:41984\n"
    "v_mul_f32 v147, s54, v147\n"
    "v_mfma_f32_16x16x32_bf16 v[156:159], a[156:159], a[60:63], v[156:159]\n"
    "v_exp_f32 v145, v145\n"
    "v_mfma_f32_16x16x32_bf16 v[0:3], a[160:163], v[160:163], v[0:3]\n"
    "ds_read_b128 a[140:143], v177 offset:42496\n"
    "v_cvt_pk_bf16_f32 v168, v144, v145\n"
    "v_mfma_f32_16x16x32_bf16 v[4:7], a[164:167], v[160:163], v[4:7]\n"
    "v_exp_f32 v146, v146\n"
    "v_mfma_f32_16x16x32_bf16 v[8:11], a[168:171], v[160:163], v[8:11]\n"
    "ds_read_b128 a[144:147], v177 offset:45056\n"
    "v_mul_f32 v148, s54, v148\n"
    "v_mfma_f32_16x16x32_bf16 v[12:15], a[172:175], v[160:163], v[12:15]\n"
    "v_exp_f32 v147, v147\n"
    "v_mul_f32 v149, s54, v149\n"
    "v_mfma_f32_16x16x32_bf16 v[16:19], a[176:179], v[160:163], v[16:19]\n"
    "ds_read_b128 a[148:151], v177 offset:45568\n"
    "v_cvt_pk_bf16_f32 v169, v146, v147\n"
    "v_mfma_f32_16x16x32_bf16 v[20:23], a[180:183], v[160:163], v[20:23]\n"
    "v_exp_f32 v148, v148\n"
    "v_mfma_f32_16x16x32_bf16 v[24:27], a[184:187], v[160:163], v[24:27]\n"
    "ds_read_b128 a[152:155], v177 offset:46080\n"
    "v_mul_f32 v150, s54, v150\n"
    "v_mfma_f32_16x16x32_bf16 v[28:31], a[188:191], v[160:163], v[28:31]\n"
    "buffer_load_dwordx4 v183, s[40:43], s74 offen offset:2176 nt lds\n"
    "v_mul_f32 v151, s54, v151\n"
    "v_mfma_f32_16x16x32_bf16 a[224:227], a[240:243], v[160:163], a[224:227]\n"
    "ds_read_b128 a[156:159], v177 offset:46592\n"
    "v_mfma_f32_16x16x32_bf16 v[32:35], a[160:163], v[164:167], v[32:35]\n"
    "v_exp_f32 v149, v149\n"
    "v_mfma_f32_16x16x32_bf16 v[36:39], a[164:167], v[164:167], v[36:39]\n"
    "v_exp_f32 v150, v150\n"
    "v_mfma_f32_16x16x32_bf16 v[40:43], a[168:171], v[164:167], v[40:43]\n"
    "v_exp_f32 v151, v151\n"
    "v_cvt_pk_bf16_f32 v170, v148, v149\n"
    "v_mfma_f32_16x16x32_bf16 v[44:47], a[172:175], v[164:167], v[44:47]\n"
    "v_cvt_pk_bf16_f32 v171, v150, v151\n"
    "v_mul_f32 v152, s54, v152\n"
    "v_mfma_f32_16x16x32_bf16 v[48:51], a[176:179], v[164:167], v[48:51]\n"
    "v_exp_f32 v152, v152\n"
    "v_mfma_f32_16x16x32_bf16 v[52:55], a[180:183], v[164:167], v[52:55]\n"
    "ds_read_b64_tr_b16 a[192:193], v179 offset:40960\n"
    "v_mul_f32 v153, s54, v153\n"
    "v_mfma_f32_16x16x32_bf16 v[56:59], a[184:187], v[164:167], v[56:59]\n"
    "v_exp_f32 v153, v153\n"
    "v_mfma_f32_16x16x32_bf16 v[60:63], a[188:191], v[164:167], v[60:63]\n"
    "s_waitcnt lgkmcnt(1)\n"
    "ds_read_b64_tr_b16 a[194:195], v179 offset:45056\n"
    "v_mul_f32 v154, s54, v154\n"
    "v_mfma_f32_16x16x32_bf16 a[228:231], a[240:243], v[164:167], a[228:231]\n"
    "v_exp_f32 v154, v154\n"
    "v_mfma_f32_16x16x32_bf16 v[128:131], a[128:131], a[0:3], v[248:251]\n"
    "v_mul_f32 v155, s54, v155\n"
    "v_cvt_pk_bf16_f32 v172, v152, v153\n"
    "v_mfma_f32_16x16x32_bf16 v[128:131], a[132:135], a[4:7], v[128:131]\n"
    "v_exp_f32 v155, v155\n"
    "v_mfma_f32_16x16x32_bf16 v[128:131], a[136:139], a[8:11], v[128:131]\n"
    "ds_read_b64_tr_b16 a[196:197], v179 offset:41216\n"
    "v_mul_f32 v156, s54, v156\n"
    "v_mfma_f32_16x16x32_bf16 v[128:131], a[140:143], a[12:15], v[128:131]\n"
    "v_exp_f32 v156, v156\n"
    "v_mfma_f32_16x16x32_bf16 v[132:135], a[144:147], a[0:3], v[248:251]\n"
    "v_mul_f32 v157, s54, v157\n"
    "v_cvt_pk_bf16_f32 v173, v154, v155\n"
    "v_mfma_f32_16x16x32_bf16 v[132:135], a[148:151], a[4:7], v[132:135]\n"
    "v_exp_f32 v157, v157\n"
    "v_mfma_f32_16x16x32_bf16 v[132:135], a[152:155], a[8:11], v[132:135]\n"
    "ds_read_b64_tr_b16 a[198:199], v179 offset:45312\n"
    "v_mul_f32 v158, s54, v158\n"
    "v_mfma_f32_16x16x32_bf16 v[132:135], a[156:159], a[12:15], v[132:135]\n"
    "v_exp_f32 v158, v158\n"
    "v_mfma_f32_16x16x32_bf16 v[136:139], a[128:131], a[16:19], v[252:255]\n"
    "v_mul_f32 v159, s54, v159\n"
    "v_cvt_pk_bf16_f32 v174, v156, v157\n"
    "v_mfma_f32_16x16x32_bf16 v[136:139], a[132:135], a[20:23], v[136:139]\n"
    "v_exp_f32 v159, v159\n"
    "v_mfma_f32_16x16x32_bf16 v[136:139], a[136:139], a[24:27], v[136:139]\n"
    "v_cvt_pk_bf16_f32 v175, v158, v159\n"
    "v_mfma_f32_16x16x32_bf16 v[136:139], a[140:143], a[28:31], v[136:139]\n"
    "v_mul_f32 v128, s54, v128\n"
    "v_mul_f32 v129, s54, v129\n"
    "v_mfma_f32_16x16x32_bf16 v[140:143], a[144:147], a[16:19], v[252:255]\n"
    "ds_read_b64_tr_b16 a[200:201], v179 offset:41472\n"
    "v_mul_f32 v130, s54, v130\n"
    "v_mfma_f32_16x16x32_bf16 v[140:143], a[148:151], a[20:23], v[140:143]\n"
    "v_exp_f32 v128, v128\n"
    "v_mfma_f32_16x16x32_bf16 v[140:143], a[152:155], a[24:27], v[140:143]\n"
    "ds_read_b64_tr_b16 a[202:203], v179 offset:45568\n"
    "v_mul_f32 v131, s54, v131\n"
    "v_mfma_f32_16x16x32_bf16 v[140:143], a[156:159], a[28:31], v[140:143]\n"
    "v_exp_f32 v129, v129\n"
    "v_mul_f32 v132, s54, v132\n"
    "v_mfma_f32_16x16x32_bf16 v[64:67], a[160:163], v[168:171], v[64:67]\n"
    "ds_read_b64_tr_b16 a[204:205], v179 offset:41728\n"
    "v_cvt_pk_bf16_f32 v160, v128, v129\n"
    "v_mfma_f32_16x16x32_bf16 v[68:71], a[164:167], v[168:171], v[68:71]\n"
    "v_exp_f32 v130, v130\n"
    "v_mfma_f32_16x16x32_bf16 v[72:75], a[168:171], v[168:171], v[72:75]\n"
    "ds_read_b64_tr_b16 a[206:207], v179 offset:45824\n"
    "v_mul_f32 v133, s54, v133\n"
    "v_mfma_f32_16x16x32_bf16 v[76:79], a[172:175], v[168:171], v[76:79]\n"
    "v_exp_f32 v131, v131\n"
    "v_mfma_f32_16x16x32_bf16 v[80:83], a[176:179], v[168:171], v[80:83]\n"
    "ds_read_b64_tr_b16 a[208:209], v179 offset:41984\n"
    "v_cvt_pk_bf16_f32 v161, v130, v131\n"
    "v_mfma_f32_16x16x32_bf16 v[84:87], a[180:183], v[168:171], v[84:87]\n"
    "v_exp_f32 v132, v132\n"
    "v_mfma_f32_16x16x32_bf16 v[88:91], a[184:187], v[168:171], v[88:91]\n"
    "ds_read_b64_tr_b16 a[210:211], v179 offset:46080\n"
    "v_mul_f32 v134, s54, v134\n"
    "v_mfma_f32_16x16x32_bf16 v[92:95], a[188:191], v[168:171], v[92:95]\n"
    "v_exp_f32 v133, v133\n"
    "v_mul_f32 v135, s54, v135\n"
    "v_mfma_f32_16x16x32_bf16 a[232:235], a[240:243], v[168:171], a[232:235]\n"
    "ds_read_b64_tr_b16 a[212:213], v179 offset:42240\n"
    "v_cvt_pk_bf16_f32 v162, v132, v133\n"
    "v_mfma_f32_16x16x32_bf16 v[96:99], a[160:163], v[172:175], v[96:99]\n"
    "v_exp_f32 v134, v134\n"
    "v_mfma_f32_16x16x32_bf16 v[100:103], a[164:167], v[172:175], v[100:103]\n"
    "ds_read_b64_tr_b16 a[214:215], v179 offset:46336\n"
    "v_mfma_f32_16x16x32_bf16 v[104:107], a[168:171], v[172:175], v[104:107]\n"
    "v_exp_f32 v135, v135\n"
    "v_mfma_f32_16x16x32_bf16 v[108:111], a[172:175], v[172:175], v[108:111]\n"
    "v_cvt_pk_bf16_f32 v163, v134, v135\n"
    "v_mul_f32 v136, s54, v136\n"
    "v_mfma_f32_16x16x32_bf16 v[112:115], a[176:179], v[172:175], v[112:115]\n"
    "v_exp_f32 v136, v136\n"
    "v_mfma_f32_16x16x32_bf16 v[116:119], a[180:183], v[172:175], v[116:119]\n"
    "ds_read_b64_tr_b16 a[216:217], v179 offset:42496\n"
    "v_mul_f32 v137, s54, v137\n"
    "v_mfma_f32_16x16x32_bf16 v[120:123], a[184:187], v[172:175], v[120:123]\n"
    "v_exp_f32 v137, v137\n"
    "v_mfma_f32_16x16x32_bf16 v[124:127], a[188:191], v[172:175], v[124:127]\n"
    "v_mul_f32 v138, s54, v138\n"
    "v_mul_f32 v139, s54, v139\n"
    "v_mfma_f32_16x16x32_bf16 a[236:239], a[240:243], v[172:175], a[236:239]\n"
    "v_exp_f32 v138, v138\n"
    "v_mfma_f32_16x16x32_bf16 v[144:147], a[128:131], a[32:35], v[196:199]\n"
    "ds_read_b64_tr_b16 a[218:219], v179 offset:46592\n"
    "v_cvt_pk_bf16_f32 v164, v136, v137\n"
    "v_mfma_f32_16x16x32_bf16 v[144:147], a[132:135], a[36:39], v[144:147]\n"
    "v_exp_f32 v139, v139\n"
    "v_mfma_f32_16x16x32_bf16 v[144:147], a[136:139], a[40:43], v[144:147]\n"
    "ds_read_b64_tr_b16 a[220:221], v179 offset:42752\n"
    "v_mul_f32 v140, s54, v140\n"
    "v_mul_f32 v141, s54, v141\n"
    "v_mfma_f32_16x16x32_bf16 v[144:147], a[140:143], a[44:47], v[144:147]\n"
    "v_exp_f32 v140, v140\n"
    "v_mfma_f32_16x16x32_bf16 v[148:151], a[144:147], a[32:35], v[196:199]\n"
    "ds_read_b64_tr_b16 a[222:223], v179 offset:46848\n"
    "v_cvt_pk_bf16_f32 v165, v138, v139\n"
    "v_mfma_f32_16x16x32_bf16 v[148:151], a[148:151], a[36:39], v[148:151]\n"
    "v_exp_f32 v141, v141\n"
    "v_mfma_f32_16x16x32_bf16 v[148:151], a[152:155], a[40:43], v[148:151]\n"
    "v_mul_f32 v142, s54, v142\n"
    "v_mul_f32 v143, s54, v143\n"
    "v_mfma_f32_16x16x32_bf16 v[148:151], a[156:159], a[44:47], v[148:151]\n"
    "v_exp_f32 v142, v142\n"
    "v_mfma_f32_16x16x32_bf16 v[152:155], a[128:131], a[48:51], v[212:215]\n"
    "s_waitcnt vmcnt(8) lgkmcnt(0)\n"
    "s_barrier\n"
    "s_cmp_ge_u32 s57, s79\n"
    "s_cbranch_scc1 Lmha_PQ_T4\n"
    "Lmha_PL_T4:\n"
    "s_memrealtime s[94:95]\n"
    "s_waitcnt lgkmcnt(0)\n"
    "s_sub_u32 s94, s94, s78\n"
    "s_cmp_lt_i32 s94, 0\n"
    "s_cbranch_scc0 Lmha_PD_T4\n"
    "s_sleep 1\n"
    "s_branch Lmha_PL_T4\n"
    "Lmha_PD_T4:\n"
    "s_add_u32 s78, s78, s94\n"
    "s_lshr_b32 s94, s65, 2\n"
    "s_add_u32 s78, s78, s94\n"
    "s_sub_u32 s65, s65, 0\n"
    "Lmha_PQ_T4:\n"
    "v_cvt_pk_bf16_f32 v166, v140, v141\n"
    "v_mfma_f32_16x16x32_bf16 v[152:155], a[132:135], a[52:55], v[152:155]\n"
    "v_exp_f32 v143, v143\n"
    "v_mfma_f32_16x16x32_bf16 v[152:155], a[136:139], a[56:59], v[152:155]\n"
    "s_add_u32 m0, s52, 0x10000\n"
    "ds_read_b128 a[128:131], v176 offset:0\n"
    "v_cvt_pk_bf16_f32 v167, v142, v143\n"
    "v_mfma_f32_16x16x32_bf16 v[152:155], a[140:143], a[60:63], v[152:155]\n"
    "buffer_load_dwordx4 v181, s[36:39], s75 offen offset:0 nt lds\n"
    "v_mul_f32 v144, s54, v144\n"
    "v_mfma_f32_16x16x32_bf16 v[156:159], a[144:147], a[48:51], v[212:215]\n"
    "ds_read_b128 a[132:135], v176 offset:512\n"
    "v_mul_f32 v145, s54, v145\n"
    "v_mfma_f32_16x16x32_bf16 v[156:159], a[148:151], a[52:55], v[156:159]\n"
    "v_exp_f32 v144, v144\n"
    "v_mul_f32 v146, s54, v146\n"
    "v_mfma_f32_16x16x32_bf16 v[156:159], a[152:155], a[56:59], v[156:159]\n"
    "ds_read_b128 a[136:139], v176 offset:1024\n"
    "v_mul_f32 v147, s54, v147\n"
    "v_mfma_f32_16x16x32_bf16 v[156:159], a[156:159], a[60:63], v[156:159]\n"
    "v_exp_f32 v145, v145\n"
    "v_mfma_f32_16x16x32_bf16 v[0:3], a[192:195], v[160:163], v[0:3]\n"
    "ds_read_b128 a[140:143], v176 offset:1536\n"
    "v_cvt_pk_bf16_f32 v168, v144, v145\n"
    "v_mfma_f32_16x16x32_bf16 v[4:7], a[196:199], v[160:163], v[4:7]\n"
    "v_exp_f32 v146, v146\n"
    "v_mfma_f32_16x16x32_bf16 v[8:11], a[200:203], v[160:163], v[8:11]\n"
    "ds_read_b128 a[144:147], v176 offset:4096\n"
    "v_mul_f32 v148, s54, v148\n"
    "v_mfma_f32_16x16x32_bf16 v[12:15], a[204:207], v[160:163], v[12:15]\n"
    "v_exp_f32 v147, v147\n"
    "v_mul_f32 v149, s54, v149\n"
    "v_mfma_f32_16x16x32_bf16 v[16:19], a[208:211], v[160:163], v[16:19]\n"
    "ds_read_b128 a[148:151], v176 offset:4608\n"
    "v_cvt_pk_bf16_f32 v169, v146, v147\n"
    "v_mfma_f32_16x16x32_bf16 v[20:23], a[212:215], v[160:163], v[20:23]\n"
    "v_exp_f32 v148, v148\n"
    "v_mfma_f32_16x16x32_bf16 v[24:27], a[216:219], v[160:163], v[24:27]\n"
    "ds_read_b128 a[152:155], v176 offset:5120\n"
    "v_mul_f32 v150, s54, v150\n"
    "v_mfma_f32_16x16x32_bf16 v[28:31], a[220:223], v[160:163], v[28:31]\n"
    "buffer_load_dwordx4 v182, s[36:39], s75 offen offset:2048 nt lds\n"
    "v_mul_f32 v151, s54, v151\n"
    "v_mfma_f32_16x16x32_bf16 a[224:227], a[240:243], v[160:163], a[224:227]\n"
    "ds_read_b128 a[156:159], v176 offset:5632\n"
    "v_mfma_f32_16x16x32_bf16 v[32:35], a[192:195], v[164:167], v[32:35]\n"
    "v_exp_f32 v149, v149\n"
    "v_mfma_f32_16x16x32_bf16 v[36:39], a[196:199], v[164:167], v[36:39]\n"
    "v_exp_f32 v150, v150\n"
    "v_mfma_f32_16x16x32_bf16 v[40:43], a[200:203], v[164:167], v[40:43]\n"
    "v_exp_f32 v151, v151\n"
    "v_cvt_pk_bf16_f32 v170, v148, v149\n"
    "v_mfma_f32_16x16x32_bf16 v[44:47], a[204:207], v[164:167], v[44:47]\n"
    "v_cvt_pk_bf16_f32 v171, v150, v151\n"
    "v_mul_f32 v152, s54, v152\n"
    "v_mfma_f32_16x16x32_bf16 v[48:51], a[208:211], v[164:167], v[48:51]\n"
    "v_exp_f32 v152, v152\n"
    "v_mfma_f32_16x16x32_bf16 v[52:55], a[212:215], v[164:167], v[52:55]\n"
    "s_add_u32 m0, s52, 0x10380\n"
    "ds_read_b64_tr_b16 a[160:161], v178 offset:0\n"
    "v_mul_f32 v153, s54, v153\n"
    "v_mfma_f32_16x16x32_bf16 v[56:59], a[216:219], v[164:167], v[56:59]\n"
    "buffer_load_dwordx4 v181, s[36:39], s75 offen offset:128 nt lds\n"
    "v_mul_f32 v154, s54, v154\n"
    "v_mfma_f32_16x16x32_bf16 v[60:63], a[220:223], v[164:167], v[60:63]\n"
    "s_waitcnt lgkmcnt(1)\n"
    "ds_read_b64_tr_b16 a[162:163], v178 offset:4096\n"
    "v_mul_f32 v155, s54, v155\n"
    "v_mfma_f32_16x16x32_bf16 a[228:231], a[240:243], v[164:167], a[228:231]\n"
    "v_exp_f32 v153, v153\n"
    "v_mfma_f32_16x16x32_bf16 v[128:131], a[128:131], a[0:3], v[248:251]\n"
    "v_exp_f32 v154, v154\n"
    "v_mfma_f32_16x16x32_bf16 v[128:131], a[132:135], a[4:7], v[128:131]\n"
    "v_exp_f32 v155, v155\n"
    "v_mfma_f32_16x16x32_bf16 v[128:131], a[136:139], a[8:11], v[128:131]\n"
    "ds_read_b64_tr_b16 a[164:165], v178 offset:256\n"
    "v_cvt_pk_bf16_f32 v172, v152, v153\n"
    "v_mul_f32 v156, s54, v156\n"
    "v_mfma_f32_16x16x32_bf16 v[128:131], a[140:143], a[12:15], v[128:131]\n"
    "v_exp_f32 v156, v156\n"
    "v_mfma_f32_16x16x32_bf16 v[132:135], a[144:147], a[0:3], v[248:251]\n"
    "v_mul_f32 v157, s54, v157\n"
    "v_cvt_pk_bf16_f32 v173, v154, v155\n"
    "v_mfma_f32_16x16x32_bf16 v[132:135], a[148:151], a[4:7], v[132:135]\n"
    "v_exp_f32 v157, v157\n"
    "v_mfma_f32_16x16x32_bf16 v[132:135], a[152:155], a[8:11], v[132:135]\n"
    "ds_read_b64_tr_b16 a[166:167], v178 offset:4352\n"
    "v_mul_f32 v158, s54, v158\n"
    "v_mfma_f32_16x16x32_bf16 v[132:135], a[156:159], a[12:15], v[132:135]\n"
    "v_exp_f32 v158, v158\n"
    "v_mfma_f32_16x16x32_bf16 v[136:139], a[128:131], a[16:19], v[252:255]\n"
    "v_mul_f32 v159, s54, v159\n"
    "v_cvt_pk_bf16_f32 v174, v156, v157\n"
    "v_mfma_f32_16x16x32_bf16 v[136:139], a[132:135], a[20:23], v[136:139]\n"
    "buffer_load_dwordx4 v182, s[36:39], s75 offen offset:2176 nt lds\n"
    "v_mfma_f32_16x16x32_bf16 v[136:139], a[136:139], a[24:27], v[136:139]\n"
    "v_exp_f32 v159, v159\n"
    "v_mfma_f32_16x16x32_bf16 v[136:139], a[140:143], a[28:31], v[136:139]\n"
    "v_cvt_pk_bf16_f32 v175, v158, v159\n"
    "v_mul_f32 v128, s54, v128\n"
    "v_mfma_f32_16x16x32_bf16 v[140:143], a[144:147], a[16:19], v[252:255]\n"
    "ds_read_b64_tr_b16 a[168:169], v178 offset:512\n"
    "v_exp_f32 v128, v128\n"
    "v_mfma_f32_16x16x32_bf16 v[140:143], a[148:151], a[20:23], v[140:143]\n"
    "v_mul_f32 v129, s54, v129\n"
    "v_mul_f32 v130, s54, v130\n"
    "v_mfma_f32_16x16x32_bf16 v[140:143], a[152:155], a[24:27], v[140:143]\n"
    "ds_read_b64_tr_b16 a[170:171], v178 offset:4608\n"
    "v_mul_f32 v131, s54, v131\n"
    "v_mfma_f32_16x16x32_bf16 v[140:143], a[156:159], a[28:31], v[140:143]\n"
    "v_exp_f32 v129, v129\n"
    "v_mfma_f32_16x16x32_bf16 v[64:67], a[192:195], v[168:171], v[64:67]\n"
    "ds_read_b64_tr_b16 a[172:173], v178 offset:768\n"
    "v_exp_f32 v130, v130\n"
    "v_mfma_f32_16x16x32_bf16 v[68:71], a[196:199], v[168:171], v[68:71]\n"
    "v_exp_f32 v131, v131\n"
    "v_mfma_f32_16x16x32_bf16 v[72:75], a[200:203], v[168:171], v[72:75]\n"
    "ds_read_b64_tr_b16 a[174:175], v178 offset:4864\n"
    "v_cvt_pk_bf16_f32 v160, v128, v129\n"
    "v_mfma_f32_16x16x32_bf16 v[76:79], a[204:207], v[168:171], v[76:79]\n"
    "v_mul_f32 v132, s54, v132\n"
    "v_exp_f32 v132, v132\n"
    "v_mfma_f32_16x16x32_bf16 v[80:83], a[208:211], v[168:171], v[80:83]\n"
    "ds_read_b64_tr_b16 a[176:177], v178 offset:1024\n"
    "v_mul_f32 v133, s54, v133\n"
    "v_mfma_f32_16x16x32_bf16 v[84:87], a[212:215], v[168:171], v[84:87]\n"
    "v_exp_f32 v133, v133\n"
    "v_mfma_f32_16x16x32_bf16 v[88:91], a[216:219], v[168:171], v[88:91]\n"
    "ds_read_b64_tr_b16 a[178:179], v178 offset:5120\n"
    "v_cvt_pk_bf16_f32 v161, v130, v131\n"
    "v_mul_f32 v134, s54, v134\n"
    "v_mfma_f32_16x16x32_bf16 v[92:95], a[220:223], v[168:171], v[92:95]\n"
    "v_exp_f32 v134, v134\n"
    "v_mfma_f32_16x16x32_bf16 a[232:235], a[240:243], v[168:171], a[232:235]\n"
    "s_add_u32 m0, s52, 0x14000\n"
    "ds_read_b64_tr_b16 a[180:181], v178 offset:1280\n"
    "v_mul_f32 v135, s54, v135\n"
    "v_mfma_f32_16x16x32_bf16 v[96:99], a[192:195], v[172:175], v[96:99]\n"
    "buffer_load_dwordx4 v183, s[40:43], s75 offen offset:0 nt lds\n"
    "v_cvt_pk_bf16_f32 v162, v132, v133\n"
    "v_mfma_f32_16x16x32_bf16 v[100:103], a[196:199], v[172:175], v[100:103]\n"
    "ds_read_b64_tr_b16 a[182:183], v178 offset:5376\n"
    "v_exp_f32 v135, v135\n"
    "v_mfma_f32_16x16x32_bf16 v[104:107], a[200:203], v[172:175], v[104:107]\n"
    "ds_read_b128 a[64:67], v180 offset:0\n"
    "v_cvt_pk_bf16_f32 v163, v134, v135\n"
    "v_mfma_f32_16x16x32_bf16 v[108:111], a[204:207], v[172:175], v[108:111]\n"
    "v_mul_f32 v136, s54, v136\n"
    "v_mul_f32 v137, s54, v137\n"
    "v_mfma_f32_16x16x32_bf16 v[112:115], a[208:211], v[172:175], v[112:115]\n"
    "ds_read_b128 a[68:71], v180 offset:512\n"
    "v_exp_f32 v136, v136\n"
    "v_mfma_f32_16x16x32_bf16 v[116:119], a[212:215], v[172:175], v[116:119]\n"
    "ds_read_b64_tr_b16 a[184:185], v178 offset:1536\n"
    "v_mul_f32 v138, s54, v138\n"
    "v_mfma_f32_16x16x32_bf16 v[120:123], a[216:219], v[172:175], v[120:123]\n"
    "ds_read_b128 a[80:83], v180 offset:2048\n"
    "v_exp_f32 v137, v137\n"
    "v_mfma_f32_16x16x32_bf16 v[124:127], a[220:223], v[172:175], v[124:127]\n"
    "v_exp_f32 v138, v138\n"
    "v_mfma_f32_16x16x32_bf16 a[236:239], a[240:243], v[172:175], a[236:239]\n"
    "buffer_load_dwordx4 v183, s[40:43], s75 offen offset:2048 nt lds\n"
    "v_mul_f32 v139, s54, v139\n"
    "v_mfma_f32_16x16x32_bf16 v[144:147], a[128:131], a[32:35], v[196:199]\n"
    "ds_read_b64_tr_b16 a[186:187], v178 offset:5632\n"
    "v_exp_f32 v139, v139\n"
    "v_mfma_f32_16x16x32_bf16 v[144:147], a[132:135], a[36:39], v[144:147]\n"
    "ds_read_b128 a[84:87], v180 offset:2560\n"
    "v_cvt_pk_bf16_f32 v164, v136, v137\n"
    "v_mfma_f32_16x16x32_bf16 v[144:147], a[136:139], a[40:43], v[144:147]\n"
    "ds_read_b64_tr_b16 a[188:189], v178 offset:1792\n"
    "v_mul_f32 v140, s54, v140\n"
    "v_mul_f32 v141, s54, v141\n"
    "v_mfma_f32_16x16x32_bf16 v[144:147], a[140:143], a[44:47], v[144:147]\n"
    "ds_read_b128 a[72:75], v180 offset:16384\n"
    "v_cvt_pk_bf16_f32 v165, v138, v139\n"
    "v_mfma_f32_16x16x32_bf16 v[148:151], a[144:147], a[32:35], v[196:199]\n"
    "ds_read_b64_tr_b16 a[190:191], v178 offset:5888\n"
    "v_exp_f32 v140, v140\n"
    "v_mfma_f32_16x16x32_bf16 v[148:151], a[148:151], a[36:39], v[148:151]\n"
    "ds_read_b128 a[76:79], v180 offset:16896\n"
    "v_mul_f32 v142, s54, v142\n"
    "v_mfma_f32_16x16x32_bf16 v[148:151], a[152:155], a[40:43], v[148:151]\n"
    "v_exp_f32 v141, v141\n"
    "v_mfma_f32_16x16x32_bf16 v[148:151], a[156:159], a[44:47], v[148:151]\n"
    "ds_read_b128 a[88:91], v180 offset:18432\n"
    "v_exp_f32 v142, v142\n"
    "v_mfma_f32_16x16x32_bf16 v[152:155], a[128:131], a[48:51], v[212:215]\n"
    "s_waitcnt lgkmcnt(0)\n"
    "v_mul_f32 v143, s54, v143\n"
    "v_cvt_pk_bf16_f32 v166, v140, v141\n"
    "v_mfma_f32_16x16x32_bf16 v[152:155], a[132:135], a[52:55], v[152:155]\n"
    "ds_read_b128 a[92:95], v180 offset:18944\n"
    "v_exp_f32 v143, v143\n"
    "v_mfma_f32_16x16x32_bf16 v[152:155], a[136:139], a[56:59], v[152:155]\n"
    "s_add_u32 m0, s52, 0x14380\n"
    "ds_read_b128 a[128:131], v176 offset:8192\n"
    "v_cvt_pk_bf16_f32 v167, v142, v143\n"
    "v_mfma_f32_16x16x32_bf16 v[152:155], a[140:143], a[60:63], v[152:155]\n"
    "buffer_load_dwordx4 v183, s[40:43], s75 offen offset:128 nt lds\n"
    "v_mul_f32 v144, s54, v144\n"
    "v_mfma_f32_16x16x32_bf16 v[156:159], a[144:147], a[48:51], v[212:215]\n"
    "ds_read_b128 a[132:135], v176 offset:8704\n"
    "v_mul_f32 v145, s54, v145\n"
    "v_mfma_f32_16x16x32_bf16 v[156:159], a[148:151], a[52:55], v[156:159]\n"
    "v_exp_f32 v144, v144\n"
    "v_mul_f32 v146, s54, v146\n"
    "v_mfma_f32_16x16x32_bf16 v[156:159], a[152:155], a[56:59], v[156:159]\n"
    "ds_read_b128 a[136:139], v176 offset:9216\n"
    "v_mul_f32 v147, s54, v147\n"
    "v_mfma_f32_16x16x32_bf16 v[156:159], a[156:159], a[60:63], v[156:159]\n"
    "v_exp_f32 v145, v145\n"
    "v_mfma_f32_16x16x32_bf16 v[0:3], a[160:163], v[160:163], v[0:3]\n"
    "ds_read_b128 a[140:143], v176 offset:9728\n"
    "v_cvt_pk_bf16_f32 v168, v144, v145\n"
    "v_mfma_f32_16x16x32_bf16 v[4:7], a[164:167], v[160:163], v[4:7]\n"
    "v_exp_f32 v146, v146\n"
    "v_mfma_f32_16x16x32_bf16 v[8:11], a[168:171], v[160:163], v[8:11]\n"
    "ds_read_b128 a[144:147], v176 offset:12288\n"
    "v_mul_f32 v148, s54, v148\n"
    "v_mfma_f32_16x16x32_bf16 v[12:15], a[172:175], v[160:163], v[12:15]\n"
    "v_exp_f32 v147, v147\n"
    "v_mul_f32 v149, s54, v149\n"
    "v_mfma_f32_16x16x32_bf16 v[16:19], a[176:179], v[160:163], v[16:19]\n"
    "ds_read_b128 a[148:151], v176 offset:12800\n"
    "v_cvt_pk_bf16_f32 v169, v146, v147\n"
    "v_mfma_f32_16x16x32_bf16 v[20:23], a[180:183], v[160:163], v[20:23]\n"
    "v_exp_f32 v148, v148\n"
    "v_mfma_f32_16x16x32_bf16 v[24:27], a[184:187], v[160:163], v[24:27]\n"
    "ds_read_b128 a[152:155], v176 offset:13312\n"
    "v_mul_f32 v150, s54, v150\n"
    "v_mfma_f32_16x16x32_bf16 v[28:31], a[188:191], v[160:163], v[28:31]\n"
    "buffer_load_dwordx4 v183, s[40:43], s75 offen offset:2176 nt lds\n"
    "v_mul_f32 v151, s54, v151\n"
    "v_mfma_f32_16x16x32_bf16 a[224:227], a[240:243], v[160:163], a[224:227]\n"
    "ds_read_b128 a[156:159], v176 offset:13824\n"
    "v_mfma_f32_16x16x32_bf16 v[32:35], a[160:163], v[164:167], v[32:35]\n"
    "v_exp_f32 v149, v149\n"
    "v_mfma_f32_16x16x32_bf16 v[36:39], a[164:167], v[164:167], v[36:39]\n"
    "v_exp_f32 v150, v150\n"
    "v_mfma_f32_16x16x32_bf16 v[40:43], a[168:171], v[164:167], v[40:43]\n"
    "v_exp_f32 v151, v151\n"
    "v_cvt_pk_bf16_f32 v170, v148, v149\n"
    "v_mfma_f32_16x16x32_bf16 v[44:47], a[172:175], v[164:167], v[44:47]\n"
    "v_cvt_pk_bf16_f32 v171, v150, v151\n"
    "v_mul_f32 v152, s54, v152\n"
    "v_mfma_f32_16x16x32_bf16 v[48:51], a[176:179], v[164:167], v[48:51]\n"
    "v_exp_f32 v152, v152\n"
    "v_mfma_f32_16x16x32_bf16 v[52:55], a[180:183], v[164:167], v[52:55]\n"
    "ds_read_b64_tr_b16 a[192:193], v178 offset:8192\n"
    "v_mul_f32 v153, s54, v153\n"
    "v_mfma_f32_16x16x32_bf16 v[56:59], a[184:187], v[164:167], v[56:59]\n"
    "v_exp_f32 v153, v153\n"
    "v_mfma_f32_16x16x32_bf16 v[60:63], a[188:191], v[164:167], v[60:63]\n"
    "s_waitcnt lgkmcnt(1)\n"
    "ds_read_b64_tr_b16 a[194:195], v178 offset:12288\n"
    "v_mul_f32 v154, s54, v154\n"
    "v_mfma_f32_16x16x32_bf16 a[228:231], a[240:243], v[164:167], a[228:231]\n"
    "v_exp_f32 v154, v154\n"
    "v_mfma_f32_16x16x32_bf16 v[128:131], a[128:131], a[0:3], v[248:251]\n"
    "v_mul_f32 v155, s54, v155\n"
    "v_cvt_pk_bf16_f32 v172, v152, v153\n"
    "v_mfma_f32_16x16x32_bf16 v[128:131], a[132:135], a[4:7], v[128:131]\n"
    "v_exp_f32 v155, v155\n"
    "v_mfma_f32_16x16x32_bf16 v[128:131], a[136:139], a[8:11], v[128:131]\n"
    "ds_read_b64_tr_b16 a[196:197], v178 offset:8448\n"
    "v_mul_f32 v156, s54, v156\n"
    "v_mfma_f32_16x16x32_bf16 v[128:131], a[140:143], a[12:15], v[128:131]\n"
    "v_exp_f32 v156, v156\n"
    "v_mfma_f32_16x16x32_bf16 v[132:135], a[144:147], a[0:3], v[248:251]\n"
    "v_mul_f32 v157, s54, v157\n"
    "v_cvt_pk_bf16_f32 v173, v154, v155\n"
    "v_mfma_f32_16x16x32_bf16 v[132:135], a[148:151], a[4:7], v[132:135]\n"
    "v_exp_f32 v157, v157\n"
    "v_mfma_f32_16x16x32_bf16 v[132:135], a[152:155], a[8:11], v[132:135]\n"
    "ds_read_b64_tr_b16 a[198:199], v178 offset:12544\n"
    "v_mul_f32 v158, s54, v158\n"
    "v_mfma_f32_16x16x32_bf16 v[132:135], a[156:159], a[12:15], v[132:135]\n"
    "v_exp_f32 v158, v158\n"
    "v_mfma_f32_16x16x32_bf16 v[136:139], a[128:131], a[16:19], v[252:255]\n"
    "v_mul_f32 v159, s54, v159\n"
    "v_cvt_pk_bf16_f32 v174, v156, v157\n"
    "v_mfma_f32_16x16x32_bf16 v[136:139], a[132:135], a[20:23], v[136:139]\n"
    "v_exp_f32 v159, v159\n"
    "v_mfma_f32_16x16x32_bf16 v[136:139], a[136:139], a[24:27], v[136:139]\n"
    "v_cvt_pk_bf16_f32 v175, v158, v159\n"
    "v_mfma_f32_16x16x32_bf16 v[136:139], a[140:143], a[28:31], v[136:139]\n"
    "v_mul_f32 v128, s54, v128\n"
    "v_mul_f32 v129, s54, v129\n"
    "v_mfma_f32_16x16x32_bf16 v[140:143], a[144:147], a[16:19], v[252:255]\n"
    "ds_read_b64_tr_b16 a[200:201], v178 offset:8704\n"
    "v_mul_f32 v130, s54, v130\n"
    "v_mfma_f32_16x16x32_bf16 v[140:143], a[148:151], a[20:23], v[140:143]\n"
    "v_exp_f32 v128, v128\n"
    "v_mfma_f32_16x16x32_bf16 v[140:143], a[152:155], a[24:27], v[140:143]\n"
    "ds_read_b64_tr_b16 a[202:203], v178 offset:12800\n"
    "v_mul_f32 v131, s54, v131\n"
    "v_mfma_f32_16x16x32_bf16 v[140:143], a[156:159], a[28:31], v[140:143]\n"
    "v_exp_f32 v129, v129\n"
    "v_mul_f32 v132, s54, v132\n"
    "v_mfma_f32_16x16x32_bf16 v[64:67], a[160:163], v[168:171], v[64:67]\n"
    "ds_read_b64_tr_b16 a[204:205], v178 offset:8960\n"
    "v_cvt_pk_bf16_f32 v160, v128, v129\n"
    "v_mfma_f32_16x16x32_bf16 v[68:71], a[164:167], v[168:171], v[68:71]\n"
    "v_exp_f32 v130, v130\n"
    "v_mfma_f32_16x16x32_bf16 v[72:75], a[168:171], v[168:171], v[72:75]\n"
    "ds_read_b64_tr_b16 a[206:207], v178 offset:13056\n"
    "v_mul_f32 v133, s54, v133\n"
    "v_mfma_f32_16x16x32_bf16 v[76:79], a[172:175], v[168:171], v[76:79]\n"
    "v_exp_f32 v131, v131\n"
    "v_mfma_f32_16x16x32_bf16 v[80:83], a[176:179], v[168:171], v[80:83]\n"
    "ds_read_b64_tr_b16 a[208:209], v178 offset:9216\n"
    "v_cvt_pk_bf16_f32 v161, v130, v131\n"
    "v_mfma_f32_16x16x32_bf16 v[84:87], a[180:183], v[168:171], v[84:87]\n"
    "v_exp_f32 v132, v132\n"
    "v_mfma_f32_16x16x32_bf16 v[88:91], a[184:187], v[168:171], v[88:91]\n"
    "ds_read_b64_tr_b16 a[210:211], v178 offset:13312\n"
    "v_mul_f32 v134, s54, v134\n"
    "v_mfma_f32_16x16x32_bf16 v[92:95], a[188:191], v[168:171], v[92:95]\n"
    "v_exp_f32 v133, v133\n"
    "v_mul_f32 v135, s54, v135\n"
    "v_mfma_f32_16x16x32_bf16 a[232:235], a[240:243], v[168:171], a[232:235]\n"
    "ds_read_b64_tr_b16 a[212:213], v178 offset:9472\n"
    "v_cvt_pk_bf16_f32 v162, v132, v133\n"
    "v_mfma_f32_16x16x32_bf16 v[96:99], a[160:163], v[172:175], v[96:99]\n"
    "v_exp_f32 v134, v134\n"
    "v_mfma_f32_16x16x32_bf16 v[100:103], a[164:167], v[172:175], v[100:103]\n"
    "ds_read_b64_tr_b16 a[214:215], v178 offset:13568\n"
    "v_mfma_f32_16x16x32_bf16 v[104:107], a[168:171], v[172:175], v[104:107]\n"
    "v_exp_f32 v135, v135\n"
    "v_mfma_f32_16x16x32_bf16 v[108:111], a[172:175], v[172:175], v[108:111]\n"
    "v_cvt_pk_bf16_f32 v163, v134, v135\n"
    "v_mul_f32 v136, s54, v136\n"
    "v_mfma_f32_16x16x32_bf16 v[112:115], a[176:179], v[172:175], v[112:115]\n"
    "v_exp_f32 v136, v136\n"
    "v_mfma_f32_16x16x32_bf16 v[116:119], a[180:183], v[172:175], v[116:119]\n"
    "ds_read_b64_tr_b16 a[216:217], v178 offset:9728\n"
    "v_mul_f32 v137, s54, v137\n"
    "v_mfma_f32_16x16x32_bf16 v[120:123], a[184:187], v[172:175], v[120:123]\n"
    "v_exp_f32 v137, v137\n"
    "v_mfma_f32_16x16x32_bf16 v[124:127], a[188:191], v[172:175], v[124:127]\n"
    "v_mul_f32 v138, s54, v138\n"
    "v_mul_f32 v139, s54, v139\n"
    "v_mfma_f32_16x16x32_bf16 a[236:239], a[240:243], v[172:175], a[236:239]\n"
    "v_exp_f32 v138, v138\n"
    "v_mfma_f32_16x16x32_bf16 v[144:147], a[128:131], a[32:35], v[196:199]\n"
    "ds_read_b64_tr_b16 a[218:219], v178 offset:13824\n"
    "v_cvt_pk_bf16_f32 v164, v136, v137\n"
    "v_mfma_f32_16x16x32_bf16 v[144:147], a[132:135], a[36:39], v[144:147]\n"
    "v_exp_f32 v139, v139\n"
    "v_mfma_f32_16x16x32_bf16 v[144:147], a[136:139], a[40:43], v[144:147]\n"
    "ds_read_b64_tr_b16 a[220:221], v178 offset:9984\n"
    "v_mul_f32 v140, s54, v140\n"
    "v_mul_f32 v141, s54, v141\n"
    "v_mfma_f32_16x16x32_bf16 v[144:147], a[140:143], a[44:47], v[144:147]\n"
    "v_exp_f32 v140, v140\n"
    "v_mfma_f32_16x16x32_bf16 v[148:151], a[144:147], a[32:35], v[196:199]\n"
    "ds_read_b64_tr_b16 a[222:223], v178 offset:14080\n"
    "v_cvt_pk_bf16_f32 v165, v138, v139\n"
    "v_mfma_f32_16x16x32_bf16 v[148:151], a[148:151], a[36:39], v[148:151]\n"
    "v_exp_f32 v141, v141\n"
    "v_mfma_f32_16x16x32_bf16 v[148:151], a[152:155], a[40:43], v[148:151]\n"
    "v_mul_f32 v142, s54, v142\n"
    "v_mul_f32 v143, s54, v143\n"
    "v_mfma_f32_16x16x32_bf16 v[148:151], a[156:159], a[44:47], v[148:151]\n"
    "v_exp_f32 v142, v142\n"
    "v_mfma_f32_16x16x32_bf16 v[152:155], a[128:131], a[48:51], v[212:215]\n"
    "s_waitcnt vmcnt(8) lgkmcnt(0)\n"
    "s_barrier\n"
    "s_cmp_ge_u32 s57, s79\n"
    "s_cbranch_scc1 Lmha_PQ_T5\n"
    "Lmha_PL_T5:\n"
    "s_memrealtime s[94:95]\n"
    "s_waitcnt lgkmcnt(0)\n"
    "s_sub_u32 s94, s94, s78\n"
    "s_cmp_lt_i32 s94, 0\n"
    "s_cbranch_scc0 Lmha_PD_T5\n"
    "s_sleep 1\n"
    "s_branch Lmha_PL_T5\n"
    "Lmha_PD_T5:\n"
    "s_add_u32 s78, s78, s94\n"
    "s_lshr_b32 s94, s65, 2\n"
    "s_add_u32 s78, s78, s94\n"
    "s_sub_u32 s65, s65, 0\n"
    "Lmha_PQ_T5:\n"
    "v_cvt_pk_bf16_f32 v166, v140, v141\n"
    "v_mfma_f32_16x16x32_bf16 v[152:155], a[132:135], a[52:55], v[152:155]\n"
    "v_exp_f32 v143, v143\n"
    "v_mfma_f32_16x16x32_bf16 v[152:155], a[136:139], a[56:59], v[152:155]\n"
    "s_add_u32 m0, s52, 0x18000\n"
    "ds_read_b128 a[128:131], v176 offset:32768\n"
    "v_cvt_pk_bf16_f32 v167, v142, v143\n"
    "v_mfma_f32_16x16x32_bf16 v[152:155], a[140:143], a[60:63], v[152:155]\n"
    "buffer_load_dwordx4 v181, s[36:39], s76 offen offset:0 nt lds\n"
    "v_mul_f32 v144, s54, v144\n"
    "v_mfma_f32_16x16x32_bf16 v[156:159], a[144:147], a[48:51], v[212:215]\n"
    "ds_read_b128 a[132:135], v176 offset:33280\n"
    "v_mul_f32 v145, s54, v145\n"
    "v_mfma_f32_16x16x32_bf16 v[156:159], a[148:151], a[52:55], v[156:159]\n"
    "v_exp_f32 v144, v144\n"
    "v_mul_f32 v146, s54, v146\n"
    "v_mfma_f32_16x16x32_bf16 v[156:159], a[152:155], a[56:59], v[156:159]\n"
    "ds_read_b128 a[136:139], v176 offset:33792\n"
    "v_mul_f32 v147, s54, v147\n"
    "v_mfma_f32_16x16x32_bf16 v[156:159], a[156:159], a[60:63], v[156:159]\n"
    "v_exp_f32 v145, v145\n"
    "v_mfma_f32_16x16x32_bf16 v[0:3], a[192:195], v[160:163], v[0:3]\n"
    "ds_read_b128 a[140:143], v176 offset:34304\n"
    "v_cvt_pk_bf16_f32 v168, v144, v145\n"
    "v_mfma_f32_16x16x32_bf16 v[4:7], a[196:199], v[160:163], v[4:7]\n"
    "v_exp_f32 v146, v146\n"
    "v_mfma_f32_16x16x32_bf16 v[8:11], a[200:203], v[160:163], v[8:11]\n"
    "ds_read_b128 a[144:147], v176 offset:36864\n"
    "v_mul_f32 v148, s54, v148\n"
    "v_mfma_f32_16x16x32_bf16 v[12:15], a[204:207], v[160:163], v[12:15]\n"
    "v_exp_f32 v147, v147\n"
    "v_mul_f32 v149, s54, v149\n"
    "v_mfma_f32_16x16x32_bf16 v[16:19], a[208:211], v[160:163], v[16:19]\n"
    "ds_read_b128 a[148:151], v176 offset:37376\n"
    "v_cvt_pk_bf16_f32 v169, v146, v147\n"
    "v_mfma_f32_16x16x32_bf16 v[20:23], a[212:215], v[160:163], v[20:23]\n"
    "v_exp_f32 v148, v148\n"
    "v_mfma_f32_16x16x32_bf16 v[24:27], a[216:219], v[160:163], v[24:27]\n"
    "ds_read_b128 a[152:155], v176 offset:37888\n"
    "v_mul_f32 v150, s54, v150\n"
    "v_mfma_f32_16x16x32_bf16 v[28:31], a[220:223], v[160:163], v[28:31]\n"
    "buffer_load_dwordx4 v182, s[36:39], s76 offen offset:2048 nt lds\n"
    "v_mul_f32 v151, s54, v151\n"
    "v_mfma_f32_16x16x32_bf16 a[224:227], a[240:243], v[160:163], a[224:227]\n"
    "ds_read_b128 a[156:159], v176 offset:38400\n"
    "v_mfma_f32_16x16x32_bf16 v[32:35], a[192:195], v[164:167], v[32:35]\n"
    "v_exp_f32 v149, v149\n"
    "v_mfma_f32_16x16x32_bf16 v[36:39], a[196:199], v[164:167], v[36:39]\n"
    "v_exp_f32 v150, v150\n"
    "v_mfma_f32_16x16x32_bf16 v[40:43], a[200:203], v[164:167], v[40:43]\n"
    "v_exp_f32 v151, v151\n"
    "v_cvt_pk_bf16_f32 v170, v148, v149\n"
    "v_mfma_f32_16x16x32_bf16 v[44:47], a[204:207], v[164:167], v[44:47]\n"
    "v_cvt_pk_bf16_f32 v171, v150, v151\n"
    "v_mul_f32 v152, s54, v152\n"
    "v_mfma_f32_16x16x32_bf16 v[48:51], a[208:211], v[164:167], v[48:51]\n"
    "v_exp_f32 v152, v152\n"
    "v_mfma_f32_16x16x32_bf16 v[52:55], a[212:215], v[164:167], v[52:55]\n"
    "s_add_u32 m0, s52, 0x18380\n"
    "ds_read_b64_tr_b16 a[160:161], v178 offset:32768\n"
    "v_mul_f32 v153, s54, v153\n"
    "v_mfma_f32_16x16x32_bf16 v[56:59], a[216:219], v[164:167], v[56:59]\n"
    "buffer_load_dwordx4 v181, s[36:39], s76 offen offset:128 nt lds\n"
    "v_mul_f32 v154, s54, v154\n"
    "v_mfma_f32_16x16x32_bf16 v[60:63], a[220:223], v[164:167], v[60:63]\n"
    "s_waitcnt lgkmcnt(1)\n"
    "ds_read_b64_tr_b16 a[162:163], v178 offset:36864\n"
    "v_mul_f32 v155, s54, v155\n"
    "v_mfma_f32_16x16x32_bf16 a[228:231], a[240:243], v[164:167], a[228:231]\n"
    "v_exp_f32 v153, v153\n"
    "v_mfma_f32_16x16x32_bf16 v[128:131], a[128:131], a[0:3], v[248:251]\n"
    "v_exp_f32 v154, v154\n"
    "v_mfma_f32_16x16x32_bf16 v[128:131], a[132:135], a[4:7], v[128:131]\n"
    "v_exp_f32 v155, v155\n"
    "v_mfma_f32_16x16x32_bf16 v[128:131], a[136:139], a[8:11], v[128:131]\n"
    "ds_read_b64_tr_b16 a[164:165], v178 offset:33024\n"
    "v_cvt_pk_bf16_f32 v172, v152, v153\n"
    "v_mul_f32 v156, s54, v156\n"
    "v_mfma_f32_16x16x32_bf16 v[128:131], a[140:143], a[12:15], v[128:131]\n"
    "v_exp_f32 v156, v156\n"
    "v_mfma_f32_16x16x32_bf16 v[132:135], a[144:147], a[0:3], v[248:251]\n"
    "v_mul_f32 v157, s54, v157\n"
    "v_cvt_pk_bf16_f32 v173, v154, v155\n"
    "v_mfma_f32_16x16x32_bf16 v[132:135], a[148:151], a[4:7], v[132:135]\n"
    "v_exp_f32 v157, v157\n"
    "v_mfma_f32_16x16x32_bf16 v[132:135], a[152:155], a[8:11], v[132:135]\n"
    "ds_read_b64_tr_b16 a[166:167], v178 offset:37120\n"
    "v_mul_f32 v158, s54, v158\n"
    "v_mfma_f32_16x16x32_bf16 v[132:135], a[156:159], a[12:15], v[132:135]\n"
    "v_exp_f32 v158, v158\n"
    "v_mfma_f32_16x16x32_bf16 v[136:139], a[128:131], a[16:19], v[252:255]\n"
    "v_mul_f32 v159, s54, v159\n"
    "v_cvt_pk_bf16_f32 v174, v156, v157\n"
    "v_mfma_f32_16x16x32_bf16 v[136:139], a[132:135], a[20:23], v[136:139]\n"
    "buffer_load_dwordx4 v182, s[36:39], s76 offen offset:2176 nt lds\n"
    "v_mfma_f32_16x16x32_bf16 v[136:139], a[136:139], a[24:27], v[136:139]\n"
    "v_exp_f32 v159, v159\n"
    "v_mfma_f32_16x16x32_bf16 v[136:139], a[140:143], a[28:31], v[136:139]\n"
    "v_cvt_pk_bf16_f32 v175, v158, v159\n"
    "v_mul_f32 v128, s54, v128\n"
    "v_mfma_f32_16x16x32_bf16 v[140:143], a[144:147], a[16:19], v[252:255]\n"
    "ds_read_b64_tr_b16 a[168:169], v178 offset:33280\n"
    "v_mul_f32 v129, s54, v129\n"
    "v_mfma_f32_16x16x32_bf16 v[140:143], a[148:151], a[20:23], v[140:143]\n"
    "v_exp_f32 v128, v128\n"
    "v_mul_f32 v130, s54, v130\n"
    "v_mfma_f32_16x16x32_bf16 v[140:143], a[152:155], a[24:27], v[140:143]\n"
    "ds_read_b64_tr_b16 a[170:171], v178 offset:37376\n"
    "v_mul_f32 v131, s54, v131\n"
    "v_mfma_f32_16x16x32_bf16 v[140:143], a[156:159], a[28:31], v[140:143]\n"
    "v_exp_f32 v129, v129\n"
    "v_mfma_f32_16x16x32_bf16 v[64:67], a[192:195], v[168:171], v[64:67]\n"
    "ds_read_b64_tr_b16 a[172:173], v178 offset:33536\n"
    "v_cvt_pk_bf16_f32 v160, v128, v129\n"
    "v_mfma_f32_16x16x32_bf16 v[68:71], a[196:199], v[168:171], v[68:71]\n"
    "v_exp_f32 v130, v130\n"
    "v_mul_f32 v132, s54, v132\n"
    "v_mfma_f32_16x16x32_bf16 v[72:75], a[200:203], v[168:171], v[72:75]\n"
    "ds_read_b64_tr_b16 a[174:175], v178 offset:37632\n"
    "v_mul_f32 v133, s54, v133\n"
    "v_mfma_f32_16x16x32_bf16 v[76:79], a[204:207], v[168:171], v[76:79]\n"
    "v_exp_f32 v131, v131\n"
    "v_mfma_f32_16x16x32_bf16 v[80:83], a[208:211], v[168:171], v[80:83]\n"
    "ds_read_b64_tr_b16 a[176:177], v178 offset:33792\n"
    "v_cvt_pk_bf16_f32 v161, v130, v131\n"
    "v_mfma_f32_16x16x32_bf16 v[84:87], a[212:215], v[168:171], v[84:87]\n"
    "v_exp_f32 v132, v132\n"
    "v_mul_f32 v134, s54, v134\n"
    "v_mfma_f32_16x16x32_bf16 v[88:91], a[216:219], v[168:171], v[88:91]\n"
    "ds_read_b64_tr_b16 a[178:179], v178 offset:37888\n"
    "v_mul_f32 v135, s54, v135\n"
    "v_mfma_f32_16x16x32_bf16 v[92:95], a[220:223], v[168:171], v[92:95]\n"
    "v_exp_f32 v133, v133\n"
    "v_mfma_f32_16x16x32_bf16 a[232:235], a[240:243], v[168:171], a[232:235]\n"
    "s_add_u32 m0, s52, 0x1c000\n"
    "ds_read_b64_tr_b16 a[180:181], v178 offset:34048\n"
    "v_cvt_pk_bf16_f32 v162, v132, v133\n"
    "v_mfma_f32_16x16x32_bf16 v[96:99], a[192:195], v[172:175], v[96:99]\n"
    "buffer_load_dwordx4 v183, s[40:43], s76 offen offset:0 nt lds\n"
    "v_exp_f32 v134, v134\n"
    "v_mfma_f32_16x16x32_bf16 v[100:103], a[196:199], v[172:175], v[100:103]\n"
    "ds_read_b64_tr_b16 a[182:183], v178 offset:38144\n"
    "v_mfma_f32_16x16x32_bf16 v[104:107], a[200:203], v[172:175], v[104:107]\n"
    "v_exp_f32 v135, v135\n"
    "v_mfma_f32_16x16x32_bf16 v[108:111], a[204:207], v[172:175], v[108:111]\n"
    "v_cvt_pk_bf16_f32 v163, v134, v135\n"
    "v_mul_f32 v136, s54, v136\n"
    "v_mfma_f32_16x16x32_bf16 v[112:115], a[208:211], v[172:175], v[112:115]\n"
    "v_exp_f32 v136, v136\n"
    "v_mfma_f32_16x16x32_bf16 v[116:119], a[212:215], v[172:175], v[116:119]\n"
    "ds_read_b64_tr_b16 a[184:185], v178 offset:34304\n"
    "v_mul_f32 v137, s54, v137\n"
    "v_mfma_f32_16x16x32_bf16 v[120:123], a[216:219], v[172:175], v[120:123]\n"
    "v_exp_f32 v137, v137\n"
    "v_mfma_f32_16x16x32_bf16 v[124:127], a[220:223], v[172:175], v[124:127]\n"
    "v_mul_f32 v138, s54, v138\n"
    "v_mul_f32 v139, s54, v139\n"
    "v_mfma_f32_16x16x32_bf16 a[236:239], a[240:243], v[172:175], a[236:239]\n"
    "buffer_load_dwordx4 v183, s[40:43], s76 offen offset:2048 nt lds\n"
    "v_cvt_pk_bf16_f32 v164, v136, v137\n"
    "v_mfma_f32_16x16x32_bf16 v[144:147], a[128:131], a[32:35], v[196:199]\n"
    "ds_read_b64_tr_b16 a[186:187], v178 offset:38400\n"
    "v_mul_f32 v140, s54, v140\n"
    "v_mfma_f32_16x16x32_bf16 v[144:147], a[132:135], a[36:39], v[144:147]\n"
    "v_exp_f32 v138, v138\n"
    "v_mfma_f32_16x16x32_bf16 v[144:147], a[136:139], a[40:43], v[144:147]\n"
    "ds_read_b64_tr_b16 a[188:189], v178 offset:34560\n"
    "v_exp_f32 v139, v139\n"
    "v_mfma_f32_16x16x32_bf16 v[144:147], a[140:143], a[44:47], v[144:147]\n"
    "v_exp_f32 v140, v140\n"
    "v_mfma_f32_16x16x32_bf16 v[148:151], a[144:147], a[32:35], v[196:199]\n"
    "ds_read_b64_tr_b16 a[190:191], v178 offset:38656\n"
    "v_mul_f32 v141, s54, v141\n"
    "v_mfma_f32_16x16x32_bf16 v[148:151], a[148:151], a[36:39], v[148:151]\n"
    "v_exp_f32 v141, v141\n"
    "v_mfma_f32_16x16x32_bf16 v[148:151], a[152:155], a[40:43], v[148:151]\n"
    "v_cvt_pk_bf16_f32 v165, v138, v139\n"
    "v_mul_f32 v142, s54, v142\n"
    "v_mfma_f32_16x16x32_bf16 v[148:151], a[156:159], a[44:47], v[148:151]\n"
    "v_exp_f32 v142, v142\n"
    "v_mfma_f32_16x16x32_bf16 v[152:155], a[128:131], a[48:51], v[212:215]\n"
    "s_waitcnt lgkmcnt(0)\n"
    "v_mul_f32 v143, s54, v143\n"
    "v_cvt_pk_bf16_f32 v166, v140, v141\n"
    "v_mfma_f32_16x16x32_bf16 v[152:155], a[132:135], a[52:55], v[152:155]\n"
    "v_exp_f32 v143, v143\n"
    "v_mfma_f32_16x16x32_bf16 v[152:155], a[136:139], a[56:59], v[152:155]\n"
    "s_add_u32 m0, s52, 0x1c380\n"
    "ds_read_b128 a[128:131], v176 offset:40960\n"
    "v_cvt_pk_bf16_f32 v167, v142, v143\n"
    "v_mfma_f32_16x16x32_bf16 v[152:155], a[140:143], a[60:63], v[152:155]\n"
    "buffer_load_dwordx4 v183, s[40:43], s76 offen offset:128 nt lds\n"
    "v_mul_f32 v144, s54, v144\n"
    "v_mfma_f32_16x16x32_bf16 v[156:159], a[144:147], a[48:51], v[212:215]\n"
    "ds_read_b128 a[132:135], v176 offset:41472\n"
    "v_mul_f32 v145, s54, v145\n"
    "v_mfma_f32_16x16x32_bf16 v[156:159], a[148:151], a[52:55], v[156:159]\n"
    "v_exp_f32 v144, v144\n"
    "v_mul_f32 v146, s54, v146\n"
    "v_mfma_f32_16x16x32_bf16 v[156:159], a[152:155], a[56:59], v[156:159]\n"
    "ds_read_b128 a[136:139], v176 offset:41984\n"
    "v_mul_f32 v147, s54, v147\n"
    "v_mfma_f32_16x16x32_bf16 v[156:159], a[156:159], a[60:63], v[156:159]\n"
    "v_exp_f32 v145, v145\n"
    "v_mfma_f32_16x16x32_bf16 v[0:3], a[160:163], v[160:163], v[0:3]\n"
    "ds_read_b128 a[140:143], v176 offset:42496\n"
    "v_cvt_pk_bf16_f32 v168, v144, v145\n"
    "v_mfma_f32_16x16x32_bf16 v[4:7], a[164:167], v[160:163], v[4:7]\n"
    "v_exp_f32 v146, v146\n"
    "v_mfma_f32_16x16x32_bf16 v[8:11], a[168:171], v[160:163], v[8:11]\n"
    "ds_read_b128 a[144:147], v176 offset:45056\n"
    "v_mul_f32 v148, s54, v148\n"
    "v_mfma_f32_16x16x32_bf16 v[12:15], a[172:175], v[160:163], v[12:15]\n"
    "v_exp_f32 v147, v147\n"
    "v_mul_f32 v149, s54, v149\n"
    "v_mfma_f32_16x16x32_bf16 v[16:19], a[176:179], v[160:163], v[16:19]\n"
    "ds_read_b128 a[148:151], v176 offset:45568\n"
    "v_cvt_pk_bf16_f32 v169, v146, v147\n"
    "v_mfma_f32_16x16x32_bf16 v[20:23], a[180:183], v[160:163], v[20:23]\n"
    "v_exp_f32 v148, v148\n"
    "v_mfma_f32_16x16x32_bf16 v[24:27], a[184:187], v[160:163], v[24:27]\n"
    "ds_read_b128 a[152:155], v176 offset:46080\n"
    "v_mul_f32 v150, s54, v150\n"
    "v_mfma_f32_16x16x32_bf16 v[28:31], a[188:191], v[160:163], v[28:31]\n"
    "buffer_load_dwordx4 v183, s[40:43], s76 offen offset:2176 nt lds\n"
    "v_mul_f32 v151, s54, v151\n"
    "v_mfma_f32_16x16x32_bf16 a[224:227], a[240:243], v[160:163], a[224:227]\n"
    "ds_read_b128 a[156:159], v176 offset:46592\n"
    "v_mfma_f32_16x16x32_bf16 v[32:35], a[160:163], v[164:167], v[32:35]\n"
    "v_exp_f32 v149, v149\n"
    "v_mfma_f32_16x16x32_bf16 v[36:39], a[164:167], v[164:167], v[36:39]\n"
    "v_exp_f32 v150, v150\n"
    "v_mfma_f32_16x16x32_bf16 v[40:43], a[168:171], v[164:167], v[40:43]\n"
    "v_exp_f32 v151, v151\n"
    "v_cvt_pk_bf16_f32 v170, v148, v149\n"
    "v_mfma_f32_16x16x32_bf16 v[44:47], a[172:175], v[164:167], v[44:47]\n"
    "v_cvt_pk_bf16_f32 v171, v150, v151\n"
    "v_mul_f32 v152, s54, v152\n"
    "v_mfma_f32_16x16x32_bf16 v[48:51], a[176:179], v[164:167], v[48:51]\n"
    "v_exp_f32 v152, v152\n"
    "v_mfma_f32_16x16x32_bf16 v[52:55], a[180:183], v[164:167], v[52:55]\n"
    "ds_read_b64_tr_b16 a[192:193], v178 offset:40960\n"
    "v_mul_f32 v153, s54, v153\n"
    "v_mfma_f32_16x16x32_bf16 v[56:59], a[184:187], v[164:167], v[56:59]\n"
    "v_exp_f32 v153, v153\n"
    "v_mfma_f32_16x16x32_bf16 v[60:63], a[188:191], v[164:167], v[60:63]\n"
    "s_waitcnt lgkmcnt(1)\n"
    "ds_read_b64_tr_b16 a[194:195], v178 offset:45056\n"
    "v_mul_f32 v154, s54, v154\n"
    "v_mfma_f32_16x16x32_bf16 a[228:231], a[240:243], v[164:167], a[228:231]\n"
    "v_exp_f32 v154, v154\n"
    "v_mfma_f32_16x16x32_bf16 v[128:131], a[128:131], a[0:3], v[248:251]\n"
    "v_mul_f32 v155, s54, v155\n"
    "v_cvt_pk_bf16_f32 v172, v152, v153\n"
    "v_mfma_f32_16x16x32_bf16 v[128:131], a[132:135], a[4:7], v[128:131]\n"
    "v_exp_f32 v155, v155\n"
    "v_mfma_f32_16x16x32_bf16 v[128:131], a[136:139], a[8:11], v[128:131]\n"
    "ds_read_b64_tr_b16 a[196:197], v178 offset:41216\n"
    "v_mul_f32 v156, s54, v156\n"
    "v_mfma_f32_16x16x32_bf16 v[128:131], a[140:143], a[12:15], v[128:131]\n"
    "v_exp_f32 v156, v156\n"
    "v_mfma_f32_16x16x32_bf16 v[132:135], a[144:147], a[0:3], v[248:251]\n"
    "v_mul_f32 v157, s54, v157\n"
    "v_cvt_pk_bf16_f32 v173, v154, v155\n"
    "v_mfma_f32_16x16x32_bf16 v[132:135], a[148:151], a[4:7], v[132:135]\n"
    "v_exp_f32 v157, v157\n"
    "v_mfma_f32_16x16x32_bf16 v[132:135], a[152:155], a[8:11], v[132:135]\n"
    "ds_read_b64_tr_b16 a[198:199], v178 offset:45312\n"
    "v_mul_f32 v158, s54, v158\n"
    "v_mfma_f32_16x16x32_bf16 v[132:135], a[156:159], a[12:15], v[132:135]\n"
    "v_exp_f32 v158, v158\n"
    "v_mfma_f32_16x16x32_bf16 v[136:139], a[128:131], a[16:19], v[252:255]\n"
    "v_mul_f32 v159, s54, v159\n"
    "v_cvt_pk_bf16_f32 v174, v156, v157\n"
    "v_mfma_f32_16x16x32_bf16 v[136:139], a[132:135], a[20:23], v[136:139]\n"
    "v_exp_f32 v159, v159\n"
    "v_mfma_f32_16x16x32_bf16 v[136:139], a[136:139], a[24:27], v[136:139]\n"
    "v_cvt_pk_bf16_f32 v175, v158, v159\n"
    "v_mfma_f32_16x16x32_bf16 v[136:139], a[140:143], a[28:31], v[136:139]\n"
    "v_mul_f32 v128, s54, v128\n"
    "v_mul_f32 v129, s54, v129\n"
    "v_mfma_f32_16x16x32_bf16 v[140:143], a[144:147], a[16:19], v[252:255]\n"
    "ds_read_b64_tr_b16 a[200:201], v178 offset:41472\n"
    "v_mul_f32 v130, s54, v130\n"
    "v_mfma_f32_16x16x32_bf16 v[140:143], a[148:151], a[20:23], v[140:143]\n"
    "v_exp_f32 v128, v128\n"
    "v_mfma_f32_16x16x32_bf16 v[140:143], a[152:155], a[24:27], v[140:143]\n"
    "ds_read_b64_tr_b16 a[202:203], v178 offset:45568\n"
    "v_mul_f32 v131, s54, v131\n"
    "v_mfma_f32_16x16x32_bf16 v[140:143], a[156:159], a[28:31], v[140:143]\n"
    "v_exp_f32 v129, v129\n"
    "v_mul_f32 v132, s54, v132\n"
    "v_mfma_f32_16x16x32_bf16 v[64:67], a[160:163], v[168:171], v[64:67]\n"
    "ds_read_b64_tr_b16 a[204:205], v178 offset:41728\n"
    "v_cvt_pk_bf16_f32 v160, v128, v129\n"
    "v_mfma_f32_16x16x32_bf16 v[68:71], a[164:167], v[168:171], v[68:71]\n"
    "v_exp_f32 v130, v130\n"
    "v_mfma_f32_16x16x32_bf16 v[72:75], a[168:171], v[168:171], v[72:75]\n"
    "ds_read_b64_tr_b16 a[206:207], v178 offset:45824\n"
    "v_mul_f32 v133, s54, v133\n"
    "v_mfma_f32_16x16x32_bf16 v[76:79], a[172:175], v[168:171], v[76:79]\n"
    "v_exp_f32 v131, v131\n"
    "v_mfma_f32_16x16x32_bf16 v[80:83], a[176:179], v[168:171], v[80:83]\n"
    "ds_read_b64_tr_b16 a[208:209], v178 offset:41984\n"
    "v_cvt_pk_bf16_f32 v161, v130, v131\n"
    "v_mfma_f32_16x16x32_bf16 v[84:87], a[180:183], v[168:171], v[84:87]\n"
    "v_exp_f32 v132, v132\n"
    "v_mfma_f32_16x16x32_bf16 v[88:91], a[184:187], v[168:171], v[88:91]\n"
    "ds_read_b64_tr_b16 a[210:211], v178 offset:46080\n"
    "v_mul_f32 v134, s54, v134\n"
    "v_mfma_f32_16x16x32_bf16 v[92:95], a[188:191], v[168:171], v[92:95]\n"
    "v_exp_f32 v133, v133\n"
    "v_mul_f32 v135, s54, v135\n"
    "v_mfma_f32_16x16x32_bf16 a[232:235], a[240:243], v[168:171], a[232:235]\n"
    "ds_read_b64_tr_b16 a[212:213], v178 offset:42240\n"
    "v_cvt_pk_bf16_f32 v162, v132, v133\n"
    "v_mfma_f32_16x16x32_bf16 v[96:99], a[160:163], v[172:175], v[96:99]\n"
    "v_exp_f32 v134, v134\n"
    "v_mfma_f32_16x16x32_bf16 v[100:103], a[164:167], v[172:175], v[100:103]\n"
    "ds_read_b64_tr_b16 a[214:215], v178 offset:46336\n"
    "v_mfma_f32_16x16x32_bf16 v[104:107], a[168:171], v[172:175], v[104:107]\n"
    "v_exp_f32 v135, v135\n"
    "v_mfma_f32_16x16x32_bf16 v[108:111], a[172:175], v[172:175], v[108:111]\n"
    "v_cvt_pk_bf16_f32 v163, v134, v135\n"
    "v_mul_f32 v136, s54, v136\n"
    "v_mfma_f32_16x16x32_bf16 v[112:115], a[176:179], v[172:175], v[112:115]\n"
    "v_exp_f32 v136, v136\n"
    "v_mul_f32 v137, s54, v137\n"
    "v_mfma_f32_16x16x32_bf16 v[116:119], a[180:183], v[172:175], v[116:119]\n"
    "ds_read_b64_tr_b16 a[216:217], v178 offset:42496\n"
    "v_mul_f32 v138, s54, v138\n"
    "v_mfma_f32_16x16x32_bf16 v[120:123], a[184:187], v[172:175], v[120:123]\n"
    "v_exp_f32 v137, v137\n"
    "v_mfma_f32_16x16x32_bf16 v[124:127], a[188:191], v[172:175], v[124:127]\n"
    "v_exp_f32 v138, v138\n"
    "v_mfma_f32_16x16x32_bf16 a[236:239], a[240:243], v[172:175], a[236:239]\n"
    "v_mul_f32 v139, s54, v139\n"
    "v_exp_f32 v139, v139\n"
    "v_mfma_f32_16x16x32_bf16 v[144:147], a[128:131], a[32:35], v[196:199]\n"
    "ds_read_b64_tr_b16 a[218:219], v178 offset:46592\n"
    "v_cvt_pk_bf16_f32 v164, v136, v137\n"
    "v_mfma_f32_16x16x32_bf16 v[144:147], a[132:135], a[36:39], v[144:147]\n"
    "v_mul_f32 v140, s54, v140\n"
    "v_mul_f32 v141, s54, v141\n"
    "v_mfma_f32_16x16x32_bf16 v[144:147], a[136:139], a[40:43], v[144:147]\n"
    "ds_read_b64_tr_b16 a[220:221], v178 offset:42752\n"
    "v_exp_f32 v140, v140\n"
    "v_mfma_f32_16x16x32_bf16 v[144:147], a[140:143], a[44:47], v[144:147]\n"
    "v_exp_f32 v141, v141\n"
    "v_mfma_f32_16x16x32_bf16 v[148:151], a[144:147], a[32:35], v[196:199]\n"
    "ds_read_b64_tr_b16 a[222:223], v178 offset:46848\n"
    "v_cvt_pk_bf16_f32 v165, v138, v139\n"
    "v_mfma_f32_16x16x32_bf16 v[148:151], a[148:151], a[36:39], v[148:151]\n"
    "s_add_u32 s36, s36, 0x2000000\n"
    "s_addc_u32 s37, s37, 0\n"
    "s_cmp_lt_u32 s57, 3\n"
    "s_cselect_b32 s38, 0x20000, 0\n"
    "v_mul_f32 v142, s54, v142\n"
    "v_mfma_f32_16x16x32_bf16 v[148:151], a[152:155], a[40:43], v[148:151]\n"
    "s_add_u32 s40, s40, 0x2000000\n"
    "s_addc_u32 s41, s41, 0\n"
    "s_cmp_lt_u32 s57, 3\n"
    "s_cselect_b32 s42, 0x20000, 0\n"
    "v_mfma_f32_16x16x32_bf16 v[148:151], a[156:159], a[44:47], v[148:151]\n"
    "v_exp_f32 v142, v142\n"
    "v_mfma_f32_16x16x32_bf16 v[152:155], a[128:131], a[48:51], v[212:215]\n"
    "s_waitcnt vmcnt(8) lgkmcnt(0)\n"
    "s_barrier\n"
    "s_cmp_ge_u32 s57, s79\n"
    "s_cbranch_scc1 Lmha_PQ_T6\n"
    "Lmha_PL_T6:\n"
    "s_memrealtime s[94:95]\n"
    "s_waitcnt lgkmcnt(0)\n"
    "s_sub_u32 s94, s94, s78\n"
    "s_cmp_lt_i32 s94, 0\n"
    "s_cbranch_scc0 Lmha_PD_T6\n"
    "s_sleep 1\n"
    "s_branch Lmha_PL_T6\n"
    "Lmha_PD_T6:\n"
    "s_add_u32 s78, s78, s94\n"
    "s_lshr_b32 s94, s65, 2\n"
    "s_add_u32 s78, s78, s94\n"
    "s_sub_u32 s65, s65, 0\n"
    "Lmha_PQ_T6:\n"
    "v_mul_f32 v143, s54, v143\n"
    "v_mfma_f32_16x16x32_bf16 v[152:155], a[132:135], a[52:55], v[152:155]\n"
    "v_exp_f32 v143, v143\n"
    "v_cvt_pk_bf16_f32 v166, v140, v141\n"
    "v_mfma_f32_16x16x32_bf16 v[152:155], a[136:139], a[56:59], v[152:155]\n"
    "s_add_u32 m0, s52, 0x0\n"
    "ds_read_b128 a[128:131], v177 offset:0\n"
    "v_cvt_pk_bf16_f32 v167, v142, v143\n"
    "v_mfma_f32_16x16x32_bf16 v[152:155], a[140:143], a[60:63], v[152:155]\n"
    "buffer_load_dwordx4 v181, s[36:39], 0 offen offset:0 nt lds\n"
    "v_mul_f32 v144, s54, v144\n"
    "v_mfma_f32_16x16x32_bf16 v[156:159], a[144:147], a[48:51], v[212:215]\n"
    "ds_read_b128 a[132:135], v177 offset:512\n"
    "v_exp_f32 v144, v144\n"
    "v_mfma_f32_16x16x32_bf16 v[156:159], a[148:151], a[52:55], v[156:159]\n"
    "v_mul_f32 v145, s54, v145\n"
    "v_mul_f32 v146, s54, v146\n"
    "v_mfma_f32_16x16x32_bf16 v[156:159], a[152:155], a[56:59], v[156:159]\n"
    "ds_read_b128 a[136:139], v177 offset:1024\n"
    "v_mul_f32 v147, s54, v147\n"
    "v_mfma_f32_16x16x32_bf16 v[156:159], a[156:159], a[60:63], v[156:159]\n"
    "v_exp_f32 v145, v145\n"
    "v_mfma_f32_16x16x32_bf16 v[0:3], a[192:195], v[160:163], v[0:3]\n"
    "s_add_u32 s58, s53, 0x2000\n"
    "s_add_u32 m0, s52, 0x20000\n"
    "ds_read_b128 a[140:143], v177 offset:1536\n"
    "v_cvt_pk_bf16_f32 v168, v144, v145\n"
    "v_mfma_f32_16x16x32_bf16 v[4:7], a[196:199], v[160:163], v[4:7]\n"
    "buffer_load_dwordx4 v181, s[44:47], s58 offen offset:0 nt lds\n"
    "v_mul_f32 v148, s54, v148\n"
    "v_mfma_f32_16x16x32_bf16 v[8:11], a[200:203], v[160:163], v[8:11]\n"
    "ds_read_b128 a[144:147], v177 offset:4096\n"
    "v_mul_f32 v149, s54, v149\n"
    "v_mfma_f32_16x16x32_bf16 v[12:15], a[204:207], v[160:163], v[12:15]\n"
    "v_exp_f32 v146, v146\n"
    "v_mul_f32 v150, s54, v150\n"
    "v_mfma_f32_16x16x32_bf16 v[16:19], a[208:211], v[160:163], v[16:19]\n"
    "ds_read_b128 a[148:151], v177 offset:4608\n"
    "v_mul_f32 v151, s54, v151\n"
    "v_mfma_f32_16x16x32_bf16 v[20:23], a[212:215], v[160:163], v[20:23]\n"
    "v_exp_f32 v147, v147\n"
    "v_mfma_f32_16x16x32_bf16 v[24:27], a[216:219], v[160:163], v[24:27]\n"
    "s_add_u32 m0, s52, 0x0\n"
    "ds_read_b128 a[152:155], v177 offset:5120\n"
    "v_exp_f32 v148, v148\n"
    "v_mfma_f32_16x16x32_bf16 v[28:31], a[220:223], v[160:163], v[28:31]\n"
    "buffer_load_dwordx4 v182, s[36:39], 0 offen offset:2048 nt lds\n"
    "v_cvt_pk_bf16_f32 v169, v146, v147\n"
    "v_mfma_f32_16x16x32_bf16 a[224:227], a[240:243], v[160:163], a[224:227]\n"
    "ds_read_b128 a[156:159], v177 offset:5632\n"
    "v_mfma_f32_16x16x32_bf16 v[32:35], a[192:195], v[164:167], v[32:35]\n"
    "v_exp_f32 v149, v149\n"
    "v_mfma_f32_16x16x32_bf16 v[36:39], a[196:199], v[164:167], v[36:39]\n"
    "v_exp_f32 v150, v150\n"
    "v_cvt_pk_bf16_f32 v170, v148, v149\n"
    "v_mfma_f32_16x16x32_bf16 v[40:43], a[200:203], v[164:167], v[40:43]\n"
    "v_exp_f32 v151, v151\n"
    "v_mfma_f32_16x16x32_bf16 v[44:47], a[204:207], v[164:167], v[44:47]\n"
    "v_cvt_pk_bf16_f32 v171, v150, v151\n"
    "v_mul_f32 v152, s54, v152\n"
    "v_mfma_f32_16x16x32_bf16 v[48:51], a[208:211], v[164:167], v[48:51]\n"
    "v_exp_f32 v152, v152\n"
    "v_mfma_f32_16x16x32_bf16 v[52:55], a[212:215], v[164:167], v[52:55]\n"
    "s_add_u32 m0, s52, 0x380\n"
    "ds_read_b64_tr_b16 a[160:161], v179 offset:0\n"
    "v_mul_f32 v153, s54, v153\n"
    "v_mfma_f32_16x16x32_bf16 v[56:59], a[216:219], v[164:167], v[56:59]\n"
    "buffer_load_dwordx4 v181, s[36:39], 0 offen offset:128 nt lds\n"
    "v_mul_f32 v154, s54, v154\n"
    "v_mfma_f32_16x16x32_bf16 v[60:63], a[220:223], v[164:167], v[60:63]\n"
    "s_waitcnt lgkmcnt(1)\n"
    "ds_read_b64_tr_b16 a[162:163], v179 offset:4096\n"
    "v_exp_f32 v153, v153\n"
    "v_mfma_f32_16x16x32_bf16 a[228:231], a[240:243], v[164:167], a[228:231]\n"
    "v_exp_f32 v154, v154\n"
    "v_mfma_f32_16x16x32_bf16 v[128:131], a[128:131], a[0:3], v[248:251]\n"
    "s_add_u32 m0, s52, 0x1fc00\n"
    "v_mul_f32 v155, s54, v155\n"
    "v_cvt_pk_bf16_f32 v172, v152, v153\n"
    "v_mfma_f32_16x16x32_bf16 v[128:131], a[132:135], a[4:7], v[128:131]\n"
    "buffer_load_dwordx4 v182, s[44:47], s58 offen offset:2048 nt lds\n"
    "v_mul_f32 v156, s54, v156\n"
    "v_mfma_f32_16x16x32_bf16 v[128:131], a[136:139], a[8:11], v[128:131]\n"
    "ds_read_b64_tr_b16 a[164:165], v179 offset:256\n"
    "v_mul_f32 v157, s54, v157\n"
    "v_mfma_f32_16x16x32_bf16 v[128:131], a[140:143], a[12:15], v[128:131]\n"
    "v_exp_f32 v155, v155\n"
    "v_mfma_f32_16x16x32_bf16 v[132:135], a[144:147], a[0:3], v[248:251]\n"
    "v_exp_f32 v156, v156\n"
    "v_mfma_f32_16x16x32_bf16 v[132:135], a[148:151], a[4:7], v[132:135]\n"
    "v_exp_f32 v157, v157\n"
    "v_mfma_f32_16x16x32_bf16 v[132:135], a[152:155], a[8:11], v[132:135]\n"
    "ds_read_b64_tr_b16 a[166:167], v179 offset:4352\n"
    "v_cvt_pk_bf16_f32 v173, v154, v155\n"
    "v_mul_f32 v158, s54, v158\n"
    "v_mfma_f32_16x16x32_bf16 v[132:135], a[156:159], a[12:15], v[132:135]\n"
    "v_exp_f32 v158, v158\n"
    "v_mfma_f32_16x16x32_bf16 v[136:139], a[128:131], a[16:19], v[252:255]\n"
    "s_add_u32 m0, s52, 0x380\n"
    "v_mul_f32 v159, s54, v159\n"
    "v_cvt_pk_bf16_f32 v174, v156, v157\n"
    "v_mfma_f32_16x16x32_bf16 v[136:139], a[132:135], a[20:23], v[136:139]\n"
    "buffer_load_dwordx4 v182, s[36:39], 0 offen offset:2176 nt lds\n"
    "v_mfma_f32_16x16x32_bf16 v[136:139], a[136:139], a[24:27], v[136:139]\n"
    "v_exp_f32 v159, v159\n"
    "v_mfma_f32_16x16x32_bf16 v[136:139], a[140:143], a[28:31], v[136:139]\n"
    "v_cvt_pk_bf16_f32 v175, v158, v159\n"
    "v_mul_f32 v128, s54, v128\n"
    "v_mfma_f32_16x16x32_bf16 v[140:143], a[144:147], a[16:19], v[252:255]\n"
    "s_add_u32 s58, s53, 0x3000\n"
    "s_add_u32 m0, s52, 0x20800\n"
    "ds_read_b64_tr_b16 a[168:169], v179 offset:512\n"
    "v_mul_f32 v129, s54, v129\n"
    "v_mfma_f32_16x16x32_bf16 v[140:143], a[148:151], a[20:23], v[140:143]\n"
    "buffer_load_dwordx4 v181, s[44:47], s58 offen offset:0 nt lds\n"
    "v_mul_f32 v130, s54, v130\n"
    "v_mfma_f32_16x16x32_bf16 v[140:143], a[152:155], a[24:27], v[140:143]\n"
    "ds_read_b64_tr_b16 a[170:171], v179 offset:4608\n"
    "v_exp_f32 v128, v128\n"
    "v_mfma_f32_16x16x32_bf16 v[140:143], a[156:159], a[28:31], v[140:143]\n"
    "v_exp_f32 v129, v129\n"
    "v_mfma_f32_16x16x32_bf16 v[64:67], a[192:195], v[168:171], v[64:67]\n"
    "ds_read_b64_tr_b16 a[172:173], v179 offset:768\n"
    "v_mul_f32 v131, s54, v131\n"
    "v_mfma_f32_16x16x32_bf16 v[68:71], a[196:199], v[168:171], v[68:71]\n"
    "v_exp_f32 v130, v130\n"
    "v_cvt_pk_bf16_f32 v160, v128, v129\n"
    "v_mfma_f32_16x16x32_bf16 v[72:75], a[200:203], v[168:171], v[72:75]\n"
    "s_add_u32 m0, s52, 0x20400\n"
    "ds_read_b64_tr_b16 a[174:175], v179 offset:4864\n"
    "v_mul_f32 v132, s54, v132\n"
    "v_mfma_f32_16x16x32_bf16 v[76:79], a[204:207], v[168:171], v[76:79]\n"
    "buffer_load_dwordx4 v182, s[44:47], s58 offen offset:2048 nt lds\n"
    "v_exp_f32 v131, v131\n"
    "v_mfma_f32_16x16x32_bf16 v[80:83], a[208:211], v[168:171], v[80:83]\n"
    "ds_read_b64_tr_b16 a[176:177], v179 offset:1024\n"
    "v_mul_f32 v133, s54, v133\n"
    "v_mfma_f32_16x16x32_bf16 v[84:87], a[212:215], v[168:171], v[84:87]\n"
    "v_exp_f32 v132, v132\n"
    "v_cvt_pk_bf16_f32 v161, v130, v131\n"
    "v_mfma_f32_16x16x32_bf16 v[88:91], a[216:219], v[168:171], v[88:91]\n"
    "ds_read_b64_tr_b16 a[178:179], v179 offset:5120\n"
    "v_mul_f32 v134, s54, v134\n"
    "v_mfma_f32_16x16x32_bf16 v[92:95], a[220:223], v[168:171], v[92:95]\n"
    "v_exp_f32 v133, v133\n"
    "v_mfma_f32_16x16x32_bf16 a[232:235], a[240:243], v[168:171], a[232:235]\n"
    "s_add_u32 m0, s52, 0x4000\n"
    "ds_read_b64_tr_b16 a[180:181], v179 offset:1280\n"
    "v_exp_f32 v134, v134\n"
    "v_mfma_f32_16x16x32_bf16 v[96:99], a[192:195], v[172:175], v[96:99]\n"
    "buffer_load_dwordx4 v183, s[40:43], 0 offen offset:0 nt lds\n"
    "v_mul_f32 v135, s54, v135\n"
    "v_mfma_f32_16x16x32_bf16 v[100:103], a[196:199], v[172:175], v[100:103]\n"
    "ds_read_b64_tr_b16 a[182:183], v179 offset:5376\n"
    "v_exp_f32 v135, v135\n"
    "v_mfma_f32_16x16x32_bf16 v[104:107], a[200:203], v[172:175], v[104:107]\n"
    "v_cvt_pk_bf16_f32 v162, v132, v133\n"
    "v_cvt_pk_bf16_f32 v163, v134, v135\n"
    "v_mfma_f32_16x16x32_bf16 v[108:111], a[204:207], v[172:175], v[108:111]\n"
    "s_add_u32 s58, s53, 0x2080\n"
    "s_add_u32 m0, s52, 0x24000\n"
    "v_mul_f32 v136, s54, v136\n"
    "v_mfma_f32_16x16x32_bf16 v[112:115], a[208:211], v[172:175], v[112:115]\n"
    "buffer_load_dwordx4 v181, s[44:47], s58 offen offset:0 nt lds\n"
    "v_mul_f32 v137, s54, v137\n"
    "v_mfma_f32_16x16x32_bf16 v[116:119], a[212:215], v[172:175], v[116:119]\n"
    "ds_read_b64_tr_b16 a[184:185], v179 offset:1536\n"
    "v_exp_f32 v136, v136\n"
    "v_mfma_f32_16x16x32_bf16 v[120:123], a[216:219], v[172:175], v[120:123]\n"
    "v_exp_f32 v137, v137\n"
    "v_mfma_f32_16x16x32_bf16 v[124:127], a[220:223], v[172:175], v[124:127]\n"
    "s_add_u32 m0, s52, 0x4000\n"
    "v_mul_f32 v138, s54, v138\n"
    "v_mul_f32 v139, s54, v139\n"
    "v_mfma_f32_16x16x32_bf16 a[236:239], a[240:243], v[172:175], a[236:239]\n"
    "buffer_load_dwordx4 v183, s[40:43], 0 offen offset:2048 nt lds\n"
    "v_cvt_pk_bf16_f32 v164, v136, v137\n"
    "v_mfma_f32_16x16x32_bf16 v[144:147], a[128:131], a[32:35], v[196:199]\n"
    "ds_read_b64_tr_b16 a[186:187], v179 offset:5632\n"
    "v_mul_f32 v140, s54, v140\n"
    "v_mfma_f32_16x16x32_bf16 v[144:147], a[132:135], a[36:39], v[144:147]\n"
    "v_exp_f32 v138, v138\n"
    "v_mfma_f32_16x16x32_bf16 v[144:147], a[136:139], a[40:43], v[144:147]\n"
    "ds_read_b64_tr_b16 a[188:189], v179 offset:1792\n"
    "v_exp_f32 v139, v139\n"
    "v_mfma_f32_16x16x32_bf16 v[144:147], a[140:143], a[44:47], v[144:147]\n"
    "v_exp_f32 v140, v140\n"
    "v_mfma_f32_16x16x32_bf16 v[148:151], a[144:147], a[32:35], v[196:199]\n"
    "s_add_u32 m0, s52, 0x23c00\n"
    "ds_read_b64_tr_b16 a[190:191], v179 offset:5888\n"
    "v_mul_f32 v141, s54, v141\n"
    "v_mfma_f32_16x16x32_bf16 v[148:151], a[148:151], a[36:39], v[148:151]\n"
    "buffer_load_dwordx4 v182, s[44:47], s58 offen offset:2048 nt lds\n"
    "v_cvt_pk_bf16_f32 v165, v138, v139\n"
    "v_mfma_f32_16x16x32_bf16 v[148:151], a[152:155], a[40:43], v[148:151]\n"
    "v_exp_f32 v141, v141\n"
    "v_mfma_f32_16x16x32_bf16 v[148:151], a[156:159], a[44:47], v[148:151]\n"
    "v_mul_f32 v142, s54, v142\n"
    "v_mul_f32 v143, s54, v143\n"
    "v_mfma_f32_16x16x32_bf16 v[152:155], a[128:131], a[48:51], v[212:215]\n"
    "s_waitcnt lgkmcnt(0)\n"
    "v_exp_f32 v142, v142\n"
    "v_cvt_pk_bf16_f32 v166, v140, v141\n"
    "v_mfma_f32_16x16x32_bf16 v[152:155], a[132:135], a[52:55], v[152:155]\n"
    "v_exp_f32 v143, v143\n"
    "v_mfma_f32_16x16x32_bf16 v[152:155], a[136:139], a[56:59], v[152:155]\n"
    "s_add_u32 m0, s52, 0x4380\n"
    "ds_read_b128 a[128:131], v177 offset:8192\n"
    "v_cvt_pk_bf16_f32 v167, v142, v143\n"
    "v_mfma_f32_16x16x32_bf16 v[152:155], a[140:143], a[60:63], v[152:155]\n"
    "buffer_load_dwordx4 v183, s[40:43], 0 offen offset:128 nt lds\n"
    "v_mul_f32 v144, s54, v144\n"
    "v_mfma_f32_16x16x32_bf16 v[156:159], a[144:147], a[48:51], v[212:215]\n"
    "ds_read_b128 a[132:135], v177 offset:8704\n"
    "v_exp_f32 v144, v144\n"
    "v_mfma_f32_16x16x32_bf16 v[156:159], a[148:151], a[52:55], v[156:159]\n"
    "v_mul_f32 v145, s54, v145\n"
    "v_mul_f32 v146, s54, v146\n"
    "v_mfma_f32_16x16x32_bf16 v[156:159], a[152:155], a[56:59], v[156:159]\n"
    "ds_read_b128 a[136:139], v177 offset:9216\n"
    "v_mul_f32 v147, s54, v147\n"
    "v_mfma_f32_16x16x32_bf16 v[156:159], a[156:159], a[60:63], v[156:159]\n"
    "v_exp_f32 v145, v145\n"
    "v_mfma_f32_16x16x32_bf16 v[0:3], a[160:163], v[160:163], v[0:3]\n"
    "s_add_u32 s58, s53, 0x3080\n"
    "s_add_u32 m0, s52, 0x24800\n"
    "ds_read_b128 a[140:143], v177 offset:9728\n"
    "v_cvt_pk_bf16_f32 v168, v144, v145\n"
    "v_mfma_f32_16x16x32_bf16 v[4:7], a[164:167], v[160:163], v[4:7]\n"
    "buffer_load_dwordx4 v181, s[44:47], s58 offen offset:0 nt lds\n"
    "v_mul_f32 v148, s54, v148\n"
    "v_mfma_f32_16x16x32_bf16 v[8:11], a[168:171], v[160:163], v[8:11]\n"
    "ds_read_b128 a[144:147], v177 offset:12288\n"
    "v_mul_f32 v149, s54, v149\n"
    "v_mfma_f32_16x16x32_bf16 v[12:15], a[172:175], v[160:163], v[12:15]\n"
    "v_exp_f32 v146, v146\n"
    "v_mul_f32 v150, s54, v150\n"
    "v_mfma_f32_16x16x32_bf16 v[16:19], a[176:179], v[160:163], v[16:19]\n"
    "ds_read_b128 a[148:151], v177 offset:12800\n"
    "v_mul_f32 v151, s54, v151\n"
    "v_mfma_f32_16x16x32_bf16 v[20:23], a[180:183], v[160:163], v[20:23]\n"
    "v_exp_f32 v147, v147\n"
    "v_mfma_f32_16x16x32_bf16 v[24:27], a[184:187], v[160:163], v[24:27]\n"
    "s_add_u32 m0, s52, 0x4380\n"
    "ds_read_b128 a[152:155], v177 offset:13312\n"
    "v_exp_f32 v148, v148\n"
    "v_mfma_f32_16x16x32_bf16 v[28:31], a[188:191], v[160:163], v[28:31]\n"
    "buffer_load_dwordx4 v183, s[40:43], 0 offen offset:2176 nt lds\n"
    "v_cvt_pk_bf16_f32 v169, v146, v147\n"
    "v_mfma_f32_16x16x32_bf16 a[224:227], a[240:243], v[160:163], a[224:227]\n"
    "ds_read_b128 a[156:159], v177 offset:13824\n"
    "v_mfma_f32_16x16x32_bf16 v[32:35], a[160:163], v[164:167], v[32:35]\n"
    "v_exp_f32 v149, v149\n"
    "v_mfma_f32_16x16x32_bf16 v[36:39], a[164:167], v[164:167], v[36:39]\n"
    "v_exp_f32 v150, v150\n"
    "v_cvt_pk_bf16_f32 v170, v148, v149\n"
    "v_mfma_f32_16x16x32_bf16 v[40:43], a[168:171], v[164:167], v[40:43]\n"
    "v_exp_f32 v151, v151\n"
    "v_mfma_f32_16x16x32_bf16 v[44:47], a[172:175], v[164:167], v[44:47]\n"
    "s_add_u32 m0, s52, 0x24400\n"
    "v_cvt_pk_bf16_f32 v171, v150, v151\n"
    "v_mul_f32 v152, s54, v152\n"
    "v_mfma_f32_16x16x32_bf16 v[48:51], a[176:179], v[164:167], v[48:51]\n"
    "buffer_load_dwordx4 v182, s[44:47], s58 offen offset:2048 nt lds\n"
    "v_mul_f32 v153, s54, v153\n"
    "v_mfma_f32_16x16x32_bf16 v[52:55], a[180:183], v[164:167], v[52:55]\n"
    "ds_read_b64_tr_b16 a[192:193], v179 offset:8192\n"
    "v_mul_f32 v154, s54, v154\n"
    "v_mfma_f32_16x16x32_bf16 v[56:59], a[184:187], v[164:167], v[56:59]\n"
    "v_exp_f32 v152, v152\n"
    "v_mfma_f32_16x16x32_bf16 v[60:63], a[188:191], v[164:167], v[60:63]\n"
    "s_waitcnt lgkmcnt(1)\n"
    "ds_read_b64_tr_b16 a[194:195], v179 offset:12288\n"
    "v_mul_f32 v155, s54, v155\n"
    "v_mfma_f32_16x16x32_bf16 a[228:231], a[240:243], v[164:167], a[228:231]\n"
    "v_exp_f32 v153, v153\n"
    "v_mfma_f32_16x16x32_bf16 v[128:131], a[128:131], a[0:3], v[248:251]\n"
    "v_exp_f32 v154, v154\n"
    "v_mfma_f32_16x16x32_bf16 v[128:131], a[132:135], a[4:7], v[128:131]\n"
    "v_exp_f32 v155, v155\n"
    "v_mfma_f32_16x16x32_bf16 v[128:131], a[136:139], a[8:11], v[128:131]\n"
    "ds_read_b64_tr_b16 a[196:197], v179 offset:8448\n"
    "v_cvt_pk_bf16_f32 v172, v152, v153\n"
    "v_mfma_f32_16x16x32_bf16 v[128:131], a[140:143], a[12:15], v[128:131]\n"
    "v_mul_f32 v156, s54, v156\n"
    "v_mul_f32 v157, s54, v157\n"
    "v_mfma_f32_16x16x32_bf16 v[132:135], a[144:147], a[0:3], v[248:251]\n"
    "v_exp_f32 v156, v156\n"
    "v_mfma_f32_16x16x32_bf16 v[132:135], a[148:151], a[4:7], v[132:135]\n"
    "v_exp_f32 v157, v157\n"
    "v_mfma_f32_16x16x32_bf16 v[132:135], a[152:155], a[8:11], v[132:135]\n"
    "ds_read_b64_tr_b16 a[198:199], v179 offset:12544\n"
    "v_cvt_pk_bf16_f32 v173, v154, v155\n"
    "v_mfma_f32_16x16x32_bf16 v[132:135], a[156:159], a[12:15], v[132:135]\n"
    "v_mul_f32 v158, s54, v158\n"
    "v_mul_f32 v159, s54, v159\n"
    "v_mfma_f32_16x16x32_bf16 v[136:139], a[128:131], a[16:19], v[252:255]\n"
    "v_exp_f32 v158, v158\n"
    "v_mfma_f32_16x16x32_bf16 v[136:139], a[132:135], a[20:23], v[136:139]\n"
    "v_exp_f32 v159, v159\n"
    "v_mfma_f32_16x16x32_bf16 v[136:139], a[136:139], a[24:27], v[136:139]\n"
    "v_cvt_pk_bf16_f32 v174, v156, v157\n"
    "v_cvt_pk_bf16_f32 v175, v158, v159\n"
    "v_mfma_f32_16x16x32_bf16 v[136:139], a[140:143], a[28:31], v[136:139]\n"
    "v_mul_f32 v128, s54, v128\n"
    "v_mul_f32 v129, s54, v129\n"
    "v_mfma_f32_16x16x32_bf16 v[140:143], a[144:147], a[16:19], v[252:255]\n"
    "ds_read_b64_tr_b16 a[200:201], v179 offset:8704\n"
    "v_mul_f32 v130, s54, v130\n"
    "v_mfma_f32_16x16x32_bf16 v[140:143], a[148:151], a[20:23], v[140:143]\n"
    "v_exp_f32 v128, v128\n"
    "v_mfma_f32_16x16x32_bf16 v[140:143], a[152:155], a[24:27], v[140:143]\n"
    "ds_read_b64_tr_b16 a[202:203], v179 offset:12800\n"
    "v_mul_f32 v131, s54, v131\n"
    "v_mfma_f32_16x16x32_bf16 v[140:143], a[156:159], a[28:31], v[140:143]\n"
    "v_exp_f32 v129, v129\n"
    "v_mul_f32 v132, s54, v132\n"
    "v_mfma_f32_16x16x32_bf16 v[64:67], a[160:163], v[168:171], v[64:67]\n"
    "ds_read_b64_tr_b16 a[204:205], v179 offset:8960\n"
    "v_cvt_pk_bf16_f32 v160, v128, v129\n"
    "v_mfma_f32_16x16x32_bf16 v[68:71], a[164:167], v[168:171], v[68:71]\n"
    "v_exp_f32 v130, v130\n"
    "v_mfma_f32_16x16x32_bf16 v[72:75], a[168:171], v[168:171], v[72:75]\n"
    "ds_read_b64_tr_b16 a[206:207], v179 offset:13056\n"
    "v_mul_f32 v133, s54, v133\n"
    "v_mfma_f32_16x16x32_bf16 v[76:79], a[172:175], v[168:171], v[76:79]\n"
    "v_exp_f32 v131, v131\n"
    "v_mfma_f32_16x16x32_bf16 v[80:83], a[176:179], v[168:171], v[80:83]\n"
    "ds_read_b64_tr_b16 a[208:209], v179 offset:9216\n"
    "v_cvt_pk_bf16_f32 v161, v130, v131\n"
    "v_mfma_f32_16x16x32_bf16 v[84:87], a[180:183], v[168:171], v[84:87]\n"
    "v_exp_f32 v132, v132\n"
    "v_mfma_f32_16x16x32_bf16 v[88:91], a[184:187], v[168:171], v[88:91]\n"
    "ds_read_b64_tr_b16 a[210:211], v179 offset:13312\n"
    "v_mul_f32 v134, s54, v134\n"
    "v_mfma_f32_16x16x32_bf16 v[92:95], a[188:191], v[168:171], v[92:95]\n"
    "v_exp_f32 v133, v133\n"
    "v_mul_f32 v135, s54, v135\n"
    "v_mfma_f32_16x16x32_bf16 a[232:235], a[240:243], v[168:171], a[232:235]\n"
    "ds_read_b64_tr_b16 a[212:213], v179 offset:9472\n"
    "v_cvt_pk_bf16_f32 v162, v132, v133\n"
    "v_mfma_f32_16x16x32_bf16 v[96:99], a[160:163], v[172:175], v[96:99]\n"
    "v_exp_f32 v134, v134\n"
    "v_mfma_f32_16x16x32_bf16 v[100:103], a[164:167], v[172:175], v[100:103]\n"
    "ds_read_b64_tr_b16 a[214:215], v179 offset:13568\n"
    "v_mfma_f32_16x16x32_bf16 v[104:107], a[168:171], v[172:175], v[104:107]\n"
    "v_exp_f32 v135, v135\n"
    "v_mfma_f32_16x16x32_bf16 v[108:111], a[172:175], v[172:175], v[108:111]\n"
    "v_cvt_pk_bf16_f32 v163, v134, v135\n"
    "v_mul_f32 v136, s54, v136\n"
    "v_mfma_f32_16x16x32_bf16 v[112:115], a[176:179], v[172:175], v[112:115]\n"
    "v_exp_f32 v136, v136\n"
    "v_mfma_f32_16x16x32_bf16 v[116:119], a[180:183], v[172:175], v[116:119]\n"
    "ds_read_b64_tr_b16 a[216:217], v179 offset:9728\n"
    "v_mul_f32 v137, s54, v137\n"
    "v_mfma_f32_16x16x32_bf16 v[120:123], a[184:187], v[172:175], v[120:123]\n"
    "v_exp_f32 v137, v137\n"
    "v_mfma_f32_16x16x32_bf16 v[124:127], a[188:191], v[172:175], v[124:127]\n"
    "v_mul_f32 v138, s54, v138\n"
    "v_mul_f32 v139, s54, v139\n"
    "v_mfma_f32_16x16x32_bf16 a[236:239], a[240:243], v[172:175], a[236:239]\n"
    "v_exp_f32 v138, v138\n"
    "v_mfma_f32_16x16x32_bf16 v[144:147], a[128:131], a[32:35], v[196:199]\n"
    "ds_read_b64_tr_b16 a[218:219], v179 offset:13824\n"
    "v_cvt_pk_bf16_f32 v164, v136, v137\n"
    "v_mfma_f32_16x16x32_bf16 v[144:147], a[132:135], a[36:39], v[144:147]\n"
    "v_exp_f32 v139, v139\n"
    "v_mfma_f32_16x16x32_bf16 v[144:147], a[136:139], a[40:43], v[144:147]\n"
    "ds_read_b64_tr_b16 a[220:221], v179 offset:9984\n"
    "v_mul_f32 v140, s54, v140\n"
    "v_mul_f32 v141, s54, v141\n"
    "v_mfma_f32_16x16x32_bf16 v[144:147], a[140:143], a[44:47], v[144:147]\n"
    "v_exp_f32 v140, v140\n"
    "v_mfma_f32_16x16x32_bf16 v[148:151], a[144:147], a[32:35], v[196:199]\n"
    "ds_read_b64_tr_b16 a[222:223], v179 offset:14080\n"
    "v_cvt_pk_bf16_f32 v165, v138, v139\n"
    "v_mfma_f32_16x16x32_bf16 v[148:151], a[148:151], a[36:39], v[148:151]\n"
    "v_exp_f32 v141, v141\n"
    "v_mfma_f32_16x16x32_bf16 v[148:151], a[152:155], a[40:43], v[148:151]\n"
    "v_mul_f32 v142, s54, v142\n"
    "v_mul_f32 v143, s54, v143\n"
    "v_mfma_f32_16x16x32_bf16 v[148:151], a[156:159], a[44:47], v[148:151]\n"
    "v_exp_f32 v142, v142\n"
    "v_mfma_f32_16x16x32_bf16 v[152:155], a[128:131], a[48:51], v[212:215]\n"
    "s_waitcnt vmcnt(16) lgkmcnt(0)\n"
    "s_barrier\n"
    "s_cmp_ge_u32 s57, s79\n"
    "s_cbranch_scc1 Lmha_PQ_T7\n"
    "Lmha_PL_T7:\n"
    "s_memrealtime s[94:95]\n"
    "s_waitcnt lgkmcnt(0)\n"
    "s_sub_u32 s94, s94, s78\n"
    "s_cmp_lt_i32 s94, 0\n"
    "s_cbranch_scc0 Lmha_PD_T7\n"
    "s_sleep 1\n"
    "s_branch Lmha_PL_T7\n"
    "Lmha_PD_T7:\n"
    "s_add_u32 s78, s78, s94\n"
    "s_lshr_b32 s94, s65, 2\n"
    "s_add_u32 s78, s78, s94\n"
    "s_sub_u32 s65, s65, 0\n"
    "Lmha_PQ_T7:\n"
    "v_cvt_pk_bf16_f32 v166, v140, v141\n"
    "v_mfma_f32_16x16x32_bf16 v[152:155], a[132:135], a[52:55], v[152:155]\n"
    "v_exp_f32 v143, v143\n"
    "v_mfma_f32_16x16x32_bf16 v[152:155], a[136:139], a[56:59], v[152:155]\n"
    "s_add_u32 m0, s52, 0x8000\n"
    "ds_read_b128 a[128:131], v177 offset:32768\n"
    "v_cvt_pk_bf16_f32 v167, v142, v143\n"
    "v_mfma_f32_16x16x32_bf16 v[152:155], a[140:143], a[60:63], v[152:155]\n"
    "buffer_load_dwordx4 v181, s[36:39], s70 offen offset:0 nt lds\n"
    "v_mul_f32 v144, s54, v144\n"
    "v_mfma_f32_16x16x32_bf16 v[156:159], a[144:147], a[48:51], v[212:215]\n"
    "ds_read_b128 a[132:135], v177 offset:33280\n"
    "v_mul_f32 v145, s54, v145\n"
    "v_mfma_f32_16x16x32_bf16 v[156:159], a[148:151], a[52:55], v[156:159]\n"
    "v_exp_f32 v144, v144\n"
    "v_mul_f32 v146, s54, v146\n"
    "v_mfma_f32_16x16x32_bf16 v[156:159], a[152:155], a[56:59], v[156:159]\n"
    "ds_read_b128 a[136:139], v177 offset:33792\n"
    "v_mul_f32 v147, s54, v147\n"
    "v_mfma_f32_16x16x32_bf16 v[156:159], a[156:159], a[60:63], v[156:159]\n"
    "v_exp_f32 v145, v145\n"
    "v_mfma_f32_16x16x32_bf16 v[0:3], a[192:195], v[160:163], v[0:3]\n"
    "ds_read_b128 a[140:143], v177 offset:34304\n"
    "v_cvt_pk_bf16_f32 v168, v144, v145\n"
    "v_mfma_f32_16x16x32_bf16 v[4:7], a[196:199], v[160:163], v[4:7]\n"
    "buffer_load_dwordx4 v182, s[36:39], s70 offen offset:2048 nt lds\n"
    "v_exp_f32 v146, v146\n"
    "v_mfma_f32_16x16x32_bf16 v[8:11], a[200:203], v[160:163], v[8:11]\n"
    "ds_read_b128 a[144:147], v177 offset:36864\n"
    "v_mul_f32 v148, s54, v148\n"
    "v_mfma_f32_16x16x32_bf16 v[12:15], a[204:207], v[160:163], v[12:15]\n"
    "v_exp_f32 v147, v147\n"
    "v_mfma_f32_16x16x32_bf16 v[16:19], a[208:211], v[160:163], v[16:19]\n"
    "ds_read_b128 a[148:151], v177 offset:37376\n"
    "v_mul_f32 v149, s54, v149\n"
    "v_mfma_f32_16x16x32_bf16 v[20:23], a[212:215], v[160:163], v[20:23]\n"
    "v_exp_f32 v148, v148\n"
    "v_cvt_pk_bf16_f32 v169, v146, v147\n"
    "v_mfma_f32_16x16x32_bf16 v[24:27], a[216:219], v[160:163], v[24:27]\n"
    "s_add_u32 m0, s52, 0x8380\n"
    "ds_read_b128 a[152:155], v177 offset:37888\n"
    "v_mul_f32 v150, s54, v150\n"
    "v_mfma_f32_16x16x32_bf16 v[28:31], a[220:223], v[160:163], v[28:31]\n"
    "buffer_load_dwordx4 v181, s[36:39], s70 offen offset:128 nt lds\n"
    "v_mul_f32 v151, s54, v151\n"
    "v_mfma_f32_16x16x32_bf16 a[224:227], a[240:243], v[160:163], a[224:227]\n"
    "ds_read_b128 a[156:159], v177 offset:38400\n"
    "v_mfma_f32_16x16x32_bf16 v[32:35], a[192:195], v[164:167], v[32:35]\n"
    "v_exp_f32 v149, v149\n"
    "v_mfma_f32_16x16x32_bf16 v[36:39], a[196:199], v[164:167], v[36:39]\n"
    "v_exp_f32 v150, v150\n"
    "v_mfma_f32_16x16x32_bf16 v[40:43], a[200:203], v[164:167], v[40:43]\n"
    "v_exp_f32 v151, v151\n"
    "v_cvt_pk_bf16_f32 v170, v148, v149\n"
    "v_mfma_f32_16x16x32_bf16 v[44:47], a[204:207], v[164:167], v[44:47]\n"
    "v_cvt_pk_bf16_f32 v171, v150, v151\n"
    "v_mul_f32 v152, s54, v152\n"
    "v_mfma_f32_16x16x32_bf16 v[48:51], a[208:211], v[164:167], v[48:51]\n"
    "buffer_load_dwordx4 v182, s[36:39], s70 offen offset:2176 nt lds\n"
    "v_mul_f32 v153, s54, v153\n"
    "v_mfma_f32_16x16x32_bf16 v[52:55], a[212:215], v[164:167], v[52:55]\n"
    "ds_read_b64_tr_b16 a[160:161], v179 offset:32768\n"
    "v_exp_f32 v152, v152\n"
    "v_mfma_f32_16x16x32_bf16 v[56:59], a[216:219], v[164:167], v[56:59]\n"
    "v_exp_f32 v153, v153\n"
    "v_mfma_f32_16x16x32_bf16 v[60:63], a[220:223], v[164:167], v[60:63]\n"
    "s_waitcnt lgkmcnt(1)\n"
    "s_add_u32 m0, s52, 0xc000\n"
    "ds_read_b64_tr_b16 a[162:163], v179 offset:36864\n"
    "v_mfma_f32_16x16x32_bf16 a[228:231], a[240:243], v[164:167], a[228:231]\n"
    "buffer_load_dwordx4 v183, s[40:43], s70 offen offset:0 nt lds\n"
    "v_mul_f32 v154, s54, v154\n"
    "v_mfma_f32_16x16x32_bf16 v[128:131], a[128:131], a[0:3], v[248:251]\n"
    "v_exp_f32 v154, v154\n"
    "v_mul_f32 v155, s54, v155\n"
    "v_mfma_f32_16x16x32_bf16 v[128:131], a[132:135], a[4:7], v[128:131]\n"
    "v_exp_f32 v155, v155\n"
    "v_mfma_f32_16x16x32_bf16 v[128:131], a[136:139], a[8:11], v[128:131]\n"
    "ds_read_b64_tr_b16 a[164:165], v179 offset:33024\n"
    "v_cvt_pk_bf16_f32 v172, v152, v153\n"
    "v_mfma_f32_16x16x32_bf16 v[128:131], a[140:143], a[12:15], v[128:131]\n"
    "buffer_load_dwordx4 v183, s[40:43], s70 offen offset:2048 nt lds\n"
    "v_mul_f32 v156, s54, v156\n"
    "v_mfma_f32_16x16x32_bf16 v[132:135], a[144:147], a[0:3], v[248:251]\n"
    "v_exp_f32 v156, v156\n"
    "v_mul_f32 v157, s54, v157\n"
    "v_mfma_f32_16x16x32_bf16 v[132:135], a[148:151], a[4:7], v[132:135]\n"
    "v_exp_f32 v157, v157\n"
    "v_mfma_f32_16x16x32_bf16 v[132:135], a[152:155], a[8:11], v[132:135]\n"
    "s_add_u32 m0, s52, 0xc380\n"
    "ds_read_b64_tr_b16 a[166:167], v179 offset:37120\n"
    "v_cvt_pk_bf16_f32 v173, v154, v155\n"
    "v_mfma_f32_16x16x32_bf16 v[132:135], a[156:159], a[12:15], v[132:135]\n"
    "buffer_load_dwordx4 v183, s[40:43], s70 offen offset:128 nt lds\n"
    "v_mul_f32 v158, s54, v158\n"
    "v_mfma_f32_16x16x32_bf16 v[136:139], a[128:131], a[16:19], v[252:255]\n"
    "v_exp_f32 v158, v158\n"
    "v_mul_f32 v159, s54, v159\n"
    "v_mfma_f32_16x16x32_bf16 v[136:139], a[132:135], a[20:23], v[136:139]\n"
    "v_exp_f32 v159, v159\n"
    "v_mfma_f32_16x16x32_bf16 v[136:139], a[136:139], a[24:27], v[136:139]\n"
    "v_cvt_pk_bf16_f32 v174, v156, v157\n"
    "v_cvt_pk_bf16_f32 v175, v158, v159\n"
    "v_mfma_f32_16x16x32_bf16 v[136:139], a[140:143], a[28:31], v[136:139]\n"
    "buffer_load_dwordx4 v183, s[40:43], s70 offen offset:2176 nt lds\n"
    "v_mul_f32 v128, s54, v128\n"
    "v_mfma_f32_16x16x32_bf16 v[140:143], a[144:147], a[16:19], v[252:255]\n"
    "ds_read_b64_tr_b16 a[168:169], v179 offset:33280\n"
    "v_exp_f32 v128, v128\n"
    "v_mfma_f32_16x16x32_bf16 v[140:143], a[148:151], a[20:23], v[140:143]\n"
    "v_mul_f32 v129, s54, v129\n"
    "v_mul_f32 v130, s54, v130\n"
    "v_mfma_f32_16x16x32_bf16 v[140:143], a[152:155], a[24:27], v[140:143]\n"
    "s_add_u32 m0, s52, 0x10000\n"
    "ds_read_b64_tr_b16 a[170:171], v179 offset:37376\n"
    "v_exp_f32 v129, v129\n"
    "v_mfma_f32_16x16x32_bf16 v[140:143], a[156:159], a[28:31], v[140:143]\n"
    "buffer_load_dwordx4 v181, s[36:39], s71 offen offset:0 nt lds\n"
    "v_mul_f32 v131, s54, v131\n"
    "v_mfma_f32_16x16x32_bf16 v[64:67], a[192:195], v[168:171], v[64:67]\n"
    "ds_read_b64_tr_b16 a[172:173], v179 offset:33536\n"
    "v_cvt_pk_bf16_f32 v160, v128, v129\n"
    "v_mfma_f32_16x16x32_bf16 v[68:71], a[196:199], v[168:171], v[68:71]\n"
    "v_exp_f32 v130, v130\n"
    "v_mul_f32 v132, s54, v132\n"
    "v_mfma_f32_16x16x32_bf16 v[72:75], a[200:203], v[168:171], v[72:75]\n"
    "ds_read_b64_tr_b16 a[174:175], v179 offset:37632\n"
    "v_mul_f32 v133, s54, v133\n"
    "v_mfma_f32_16x16x32_bf16 v[76:79], a[204:207], v[168:171], v[76:79]\n"
    "buffer_load_dwordx4 v182, s[36:39], s71 offen offset:2048 nt lds\n"
    "v_exp_f32 v131, v131\n"
    "v_mfma_f32_16x16x32_bf16 v[80:83], a[208:211], v[168:171], v[80:83]\n"
    "ds_read_b64_tr_b16 a[176:177], v179 offset:33792\n"
    "v_cvt_pk_bf16_f32 v161, v130, v131\n"
    "v_mfma_f32_16x16x32_bf16 v[84:87], a[212:215], v[168:171], v[84:87]\n"
    "v_exp_f32 v132, v132\n"
    "v_mul_f32 v134, s54, v134\n"
    "v_mfma_f32_16x16x32_bf16 v[88:91], a[216:219], v[168:171], v[88:91]\n"
    "s_add_u32 m0, s52, 0x10380\n"
    "ds_read_b64_tr_b16 a[178:179], v179 offset:37888\n"
    "v_mul_f32 v135, s54, v135\n"
    "v_mfma_f32_16x16x32_bf16 v[92:95], a[220:223], v[168:171], v[92:95]\n"
    "buffer_load_dwordx4 v181, s[36:39], s71 offen offset:128 nt lds\n"
    "v_mfma_f32_16x16x32_bf16 a[232:235], a[240:243], v[168:171], a[232:235]\n"
    "ds_read_b64_tr_b16 a[180:181], v179 offset:34048\n"
    "v_exp_f32 v133, v133\n"
    "v_mfma_f32_16x16x32_bf16 v[96:99], a[192:195], v[172:175], v[96:99]\n"
    "v_exp_f32 v134, v134\n"
    "v_mfma_f32_16x16x32_bf16 v[100:103], a[196:199], v[172:175], v[100:103]\n"
    "ds_read_b64_tr_b16 a[182:183], v179 offset:38144\n"
    "v_exp_f32 v135, v135\n"
    "v_mfma_f32_16x16x32_bf16 v[104:107], a[200:203], v[172:175], v[104:107]\n"
    "buffer_load_dwordx4 v182, s[36:39], s71 offen offset:2176 nt lds\n"
    "v_cvt_pk_bf16_f32 v162, v132, v133\n"
    "v_cvt_pk_bf16_f32 v163, v134, v135\n"
    "v_mfma_f32_16x16x32_bf16 v[108:111], a[204:207], v[172:175], v[108:111]\n"
    "v_mul_f32 v136, s54, v136\n"
    "v_mul_f32 v137, s54, v137\n"
    "v_mfma_f32_16x16x32_bf16 v[112:115], a[208:211], v[172:175], v[112:115]\n"
    "v_exp_f32 v136, v136\n"
    "v_mfma_f32_16x16x32_bf16 v[116:119], a[212:215], v[172:175], v[116:119]\n"
    "s_add_u32 m0, s52, 0x14000\n"
    "ds_read_b64_tr_b16 a[184:185], v179 offset:34304\n"
    "v_exp_f32 v137, v137\n"
    "v_mfma_f32_16x16x32_bf16 v[120:123], a[216:219], v[172:175], v[120:123]\n"
    "buffer_load_dwordx4 v183, s[40:43], s71 offen offset:0 nt lds\n"
    "v_mul_f32 v138, s54, v138\n"
    "v_mfma_f32_16x16x32_bf16 v[124:127], a[220:223], v[172:175], v[124:127]\n"
    "v_exp_f32 v138, v138\n"
    "v_mfma_f32_16x16x32_bf16 a[236:239], a[240:243], v[172:175], a[236:239]\n"
    "v_mul_f32 v139, s54, v139\n"
    "v_cvt_pk_bf16_f32 v164, v136, v137\n"
    "v_mfma_f32_16x16x32_bf16 v[144:147], a[128:131], a[32:35], v[196:199]\n"
    "ds_read_b64_tr_b16 a[186:187], v179 offset:38400\n"
    "v_mul_f32 v140, s54, v140\n"
    "v_mfma_f32_16x16x32_bf16 v[144:147], a[132:135], a[36:39], v[144:147]\n"
    "buffer_load_dwordx4 v183, s[40:43], s71 offen offset:2048 nt lds\n"
    "v_mul_f32 v141, s54, v141\n"
    "v_mfma_f32_16x16x32_bf16 v[144:147], a[136:139], a[40:43], v[144:147]\n"
    "ds_read_b64_tr_b16 a[188:189], v179 offset:34560\n"
    "v_exp_f32 v139, v139\n"
    "v_mfma_f32_16x16x32_bf16 v[144:147], a[140:143], a[44:47], v[144:147]\n"
    "v_exp_f32 v140, v140\n"
    "v_mfma_f32_16x16x32_bf16 v[148:151], a[144:147], a[32:35], v[196:199]\n"
    "s_add_u32 m0, s52, 0x14380\n"
    "ds_read_b64_tr_b16 a[190:191], v179 offset:38656\n"
    "v_cvt_pk_bf16_f32 v165, v138, v139\n"
    "v_mfma_f32_16x16x32_bf16 v[148:151], a[148:151], a[36:39], v[148:151]\n"
    "buffer_load_dwordx4 v183, s[40:43], s71 offen offset:128 nt lds\n"
    "v_mul_f32 v142, s54, v142\n"
    "v_mfma_f32_16x16x32_bf16 v[148:151], a[152:155], a[40:43], v[148:151]\n"
    "v_exp_f32 v141, v141\n"
    "v_mfma_f32_16x16x32_bf16 v[148:151], a[156:159], a[44:47], v[148:151]\n"
    "v_exp_f32 v142, v142\n"
    "v_mfma_f32_16x16x32_bf16 v[152:155], a[128:131], a[48:51], v[212:215]\n"
    "s_waitcnt lgkmcnt(0)\n"
    "v_mul_f32 v143, s54, v143\n"
    "v_exp_f32 v143, v143\n"
    "v_mfma_f32_16x16x32_bf16 v[152:155], a[132:135], a[52:55], v[152:155]\n"
    "buffer_load_dwordx4 v183, s[40:43], s71 offen offset:2176 nt lds\n"
    "v_cvt_pk_bf16_f32 v166, v140, v141\n"
    "v_mfma_f32_16x16x32_bf16 v[152:155], a[136:139], a[56:59], v[152:155]\n"
    "ds_read_b128 a[128:131], v177 offset:40960\n"
    "v_cvt_pk_bf16_f32 v167, v142, v143\n"
    "v_mfma_f32_16x16x32_bf16 v[152:155], a[140:143], a[60:63], v[152:155]\n"
    "v_mul_f32 v144, s54, v144\n"
    "v_mul_f32 v145, s54, v145\n"
    "v_mfma_f32_16x16x32_bf16 v[156:159], a[144:147], a[48:51], v[212:215]\n"
    "ds_read_b128 a[132:135], v177 offset:41472\n"
    "v_mul_f32 v146, s54, v146\n"
    "v_mfma_f32_16x16x32_bf16 v[156:159], a[148:151], a[52:55], v[156:159]\n"
    "v_exp_f32 v144, v144\n"
    "v_mfma_f32_16x16x32_bf16 v[156:159], a[152:155], a[56:59], v[156:159]\n"
    "ds_read_b128 a[136:139], v177 offset:41984\n"
    "v_mul_f32 v147, s54, v147\n"
    "v_mfma_f32_16x16x32_bf16 v[156:159], a[156:159], a[60:63], v[156:159]\n"
    "v_exp_f32 v145, v145\n"
    "v_mfma_f32_16x16x32_bf16 v[0:3], a[160:163], v[160:163], v[0:3]\n"
    "ds_read_b128 a[140:143], v177 offset:42496\n"
    "v_cvt_pk_bf16_f32 v168, v144, v145\n"
    "v_mfma_f32_16x16x32_bf16 v[4:7], a[164:167], v[160:163], v[4:7]\n"
    "v_exp_f32 v146, v146\n"
    "v_mfma_f32_16x16x32_bf16 v[8:11], a[168:171], v[160:163], v[8:11]\n"
    "ds_read_b128 a[144:147], v177 offset:45056\n"
    "v_mul_f32 v148, s54, v148\n"
    "v_mfma_f32_16x16x32_bf16 v[12:15], a[172:175], v[160:163], v[12:15]\n"
    "v_exp_f32 v147, v147\n"
    "v_mul_f32 v149, s54, v149\n"
    "v_mfma_f32_16x16x32_bf16 v[16:19], a[176:179], v[160:163], v[16:19]\n"
    "ds_read_b128 a[148:151], v177 offset:45568\n"
    "v_cvt_pk_bf16_f32 v169, v146, v147\n"
    "v_mfma_f32_16x16x32_bf16 v[20:23], a[180:183], v[160:163], v[20:23]\n"
    "v_exp_f32 v148, v148\n"
    "v_mfma_f32_16x16x32_bf16 v[24:27], a[184:187], v[160:163], v[24:27]\n"
    "ds_read_b128 a[152:155], v177 offset:46080\n"
    "v_mul_f32 v150, s54, v150\n"
    "v_mfma_f32_16x16x32_bf16 v[28:31], a[188:191], v[160:163], v[28:31]\n"
    "v_exp_f32 v149, v149\n"
    "v_mfma_f32_16x16x32_bf16 a[224:227], a[240:243], v[160:163], a[224:227]\n"
    "ds_read_b128 a[156:159], v177 offset:46592\n"
    "v_mul_f32 v151, s54, v151\n"
    "v_mfma_f32_16x16x32_bf16 v[32:35], a[160:163], v[164:167], v[32:35]\n"
    "v_exp_f32 v150, v150\n"
    "v_mfma_f32_16x16x32_bf16 v[36:39], a[164:167], v[164:167], v[36:39]\n"
    "v_exp_f32 v151, v151\n"
    "v_mfma_f32_16x16x32_bf16 v[40:43], a[168:171], v[164:167], v[40:43]\n"
    "v_cvt_pk_bf16_f32 v170, v148, v149\n"
    "v_cvt_pk_bf16_f32 v171, v150, v151\n"
    "v_mfma_f32_16x16x32_bf16 v[44:47], a[172:175], v[164:167], v[44:47]\n"
    "v_mul_f32 v152, s54, v152\n"
    "v_mul_f32 v153, s54, v153\n"
    "v_mfma_f32_16x16x32_bf16 v[48:51], a[176:179], v[164:167], v[48:51]\n"
    "v_exp_f32 v152, v152\n"
    "v_mfma_f32_16x16x32_bf16 v[52:55], a[180:183], v[164:167], v[52:55]\n"
    "ds_read_b64_tr_b16 a[192:193], v179 offset:40960\n"
    "v_mul_f32 v154, s54, v154\n"
    "v_mfma_f32_16x16x32_bf16 v[56:59], a[184:187], v[164:167], v[56:59]\n"
    "v_exp_f32 v153, v153\n"
    "v_mfma_f32_16x16x32_bf16 v[60:63], a[188:191], v[164:167], v[60:63]\n"
    "s_waitcnt lgkmcnt(1)\n"
    "ds_read_b64_tr_b16 a[194:195], v179 offset:45056\n"
    "v_mul_f32 v155, s54, v155\n"
    "v_mfma_f32_16x16x32_bf16 a[228:231], a[240:243], v[164:167], a[228:231]\n"
    "v_exp_f32 v154, v154\n"
    "v_mfma_f32_16x16x32_bf16 v[128:131], a[128:131], a[0:3], v[248:251]\n"
    "v_exp_f32 v155, v155\n"
    "v_mfma_f32_16x16x32_bf16 v[128:131], a[132:135], a[4:7], v[128:131]\n"
    "v_cvt_pk_bf16_f32 v172, v152, v153\n"
    "v_mul_f32 v156, s54, v156\n"
    "v_mfma_f32_16x16x32_bf16 v[128:131], a[136:139], a[8:11], v[128:131]\n"
    "ds_read_b64_tr_b16 a[196:197], v179 offset:41216\n"
    "v_mul_f32 v157, s54, v157\n"
    "v_mfma_f32_16x16x32_bf16 v[128:131], a[140:143], a[12:15], v[128:131]\n"
    "v_exp_f32 v156, v156\n"
    "v_mfma_f32_16x16x32_bf16 v[132:135], a[144:147], a[0:3], v[248:251]\n"
    "v_exp_f32 v157, v157\n"
    "v_mfma_f32_16x16x32_bf16 v[132:135], a[148:151], a[4:7], v[132:135]\n"
    "v_cvt_pk_bf16_f32 v173, v154, v155\n"
    "v_mul_f32 v158, s54, v158\n"
    "v_mfma_f32_16x16x32_bf16 v[132:135], a[152:155], a[8:11], v[132:135]\n"
    "ds_read_b64_tr_b16 a[198:199], v179 offset:45312\n"
    "v_mul_f32 v159, s54, v159\n"
    "v_mfma_f32_16x16x32_bf16 v[132:135], a[156:159], a[12:15], v[132:135]\n"
    "v_exp_f32 v158, v158\n"
    "v_mfma_f32_16x16x32_bf16 v[136:139], a[128:131], a[16:19], v[252:255]\n"
    "v_exp_f32 v159, v159\n"
    "v_mfma_f32_16x16x32_bf16 v[136:139], a[132:135], a[20:23], v[136:139]\n"
    "v_cvt_pk_bf16_f32 v174, v156, v157\n"
    "v_cvt_pk_bf16_f32 v175, v158, v159\n"
    "v_mfma_f32_16x16x32_bf16 v[136:139], a[136:139], a[24:27], v[136:139]\n"
    "v_mfma_f32_16x16x32_bf16 v[136:139], a[140:143], a[28:31], v[136:139]\n"
    "v_mul_f32 v128, s54, v128\n"
    "v_mul_f32 v129, s54, v129\n"
    "v_mfma_f32_16x16x32_bf16 v[140:143], a[144:147], a[16:19], v[252:255]\n"
    "ds_read_b64_tr_b16 a[200:201], v179 offset:41472\n"
    "v_mul_f32 v130, s54, v130\n"
    "v_mfma_f32_16x16x32_bf16 v[140:143], a[148:151], a[20:23], v[140:143]\n"
    "v_exp_f32 v128, v128\n"
    "v_mfma_f32_16x16x32_bf16 v[140:143], a[152:155], a[24:27], v[140:143]\n"
    "ds_read_b64_tr_b16 a[202:203], v179 offset:45568\n"
    "v_mul_f32 v131, s54, v131\n"
    "v_mfma_f32_16x16x32_bf16 v[140:143], a[156:159], a[28:31], v[140:143]\n"
    "v_exp_f32 v129, v129\n"
    "v_mul_f32 v132, s54, v132\n"
    "v_mfma_f32_16x16x32_bf16 v[64:67], a[160:163], v[168:171], v[64:67]\n"
    "ds_read_b64_tr_b16 a[204:205], v179 offset:41728\n"
    "v_cvt_pk_bf16_f32 v160, v128, v129\n"
    "v_mfma_f32_16x16x32_bf16 v[68:71], a[164:167], v[168:171], v[68:71]\n"
    "v_exp_f32 v130, v130\n"
    "v_accvgpr_mov_b32 a0, a64\n"
    "v_mfma_f32_16x16x32_bf16 v[72:75], a[168:171], v[168:171], v[72:75]\n"
    "ds_read_b64_tr_b16 a[206:207], v179 offset:45824\n"
    "v_mul_f32 v133, s54, v133\n"
    "v_mfma_f32_16x16x32_bf16 v[76:79], a[172:175], v[168:171], v[76:79]\n"
    "v_exp_f32 v131, v131\n"
    "v_accvgpr_mov_b32 a1, a65\n"
    "v_mfma_f32_16x16x32_bf16 v[80:83], a[176:179], v[168:171], v[80:83]\n"
    "ds_read_b64_tr_b16 a[208:209], v179 offset:41984\n"
    "v_cvt_pk_bf16_f32 v161, v130, v131\n"
    "v_accvgpr_mov_b32 a2, a66\n"
    "v_mfma_f32_16x16x32_bf16 v[84:87], a[180:183], v[168:171], v[84:87]\n"
    "v_exp_f32 v132, v132\n"
    "v_mfma_f32_16x16x32_bf16 v[88:91], a[184:187], v[168:171], v[88:91]\n"
    "ds_read_b64_tr_b16 a[210:211], v179 offset:46080\n"
    "v_mul_f32 v134, s54, v134\n"
    "v_accvgpr_mov_b32 a3, a67\n"
    "v_mfma_f32_16x16x32_bf16 v[92:95], a[188:191], v[168:171], v[92:95]\n"
    "v_exp_f32 v133, v133\n"
    "v_mul_f32 v135, s54, v135\n"
    "v_accvgpr_mov_b32 a4, a68\n"
    "v_mfma_f32_16x16x32_bf16 a[232:235], a[240:243], v[168:171], a[232:235]\n"
    "ds_read_b64_tr_b16 a[212:213], v179 offset:42240\n"
    "v_cvt_pk_bf16_f32 v162, v132, v133\n"
    "v_mfma_f32_16x16x32_bf16 v[96:99], a[160:163], v[172:175], v[96:99]\n"
    "v_exp_f32 v134, v134\n"
    "v_accvgpr_mov_b32 a5, a69\n"
    "v_mfma_f32_16x16x32_bf16 v[100:103], a[164:167], v[172:175], v[100:103]\n"
    "ds_read_b64_tr_b16 a[214:215], v179 offset:46336\n"
    "v_accvgpr_mov_b32 a6, a70\n"
    "v_mfma_f32_16x16x32_bf16 v[104:107], a[168:171], v[172:175], v[104:107]\n"
    "v_exp_f32 v135, v135\n"
    "v_accvgpr_mov_b32 a7, a71\n"
    "v_mfma_f32_16x16x32_bf16 v[108:111], a[172:175], v[172:175], v[108:111]\n"
    "v_cvt_pk_bf16_f32 v163, v134, v135\n"
    "v_mul_f32 v136, s54, v136\n"
    "v_accvgpr_mov_b32 a8, a72\n"
    "v_mfma_f32_16x16x32_bf16 v[112:115], a[176:179], v[172:175], v[112:115]\n"
    "v_exp_f32 v136, v136\n"
    "v_mfma_f32_16x16x32_bf16 v[116:119], a[180:183], v[172:175], v[116:119]\n"
    "ds_read_b64_tr_b16 a[216:217], v179 offset:42496\n"
    "v_mul_f32 v137, s54, v137\n"
    "v_accvgpr_mov_b32 a9, a73\n"
    "v_mfma_f32_16x16x32_bf16 v[120:123], a[184:187], v[172:175], v[120:123]\n"
    "v_exp_f32 v137, v137\n"
    "v_accvgpr_mov_b32 a10, a74\n"
    "v_mfma_f32_16x16x32_bf16 v[124:127], a[188:191], v[172:175], v[124:127]\n"
    "v_mul_f32 v138, s54, v138\n"
    "v_mul_f32 v139, s54, v139\n"
    "v_mfma_f32_16x16x32_bf16 a[236:239], a[240:243], v[172:175], a[236:239]\n"
    "v_exp_f32 v138, v138\n"
    "v_accvgpr_mov_b32 a11, a75\n"
    "v_mfma_f32_16x16x32_bf16 v[144:147], a[128:131], a[32:35], v[196:199]\n"
    "ds_read_b64_tr_b16 a[218:219], v179 offset:46592\n"
    "v_cvt_pk_bf16_f32 v164, v136, v137\n"
    "v_accvgpr_mov_b32 a12, a76\n"
    "v_mfma_f32_16x16x32_bf16 v[144:147], a[132:135], a[36:39], v[144:147]\n"
    "v_exp_f32 v139, v139\n"
    "v_mfma_f32_16x16x32_bf16 v[144:147], a[136:139], a[40:43], v[144:147]\n"
    "ds_read_b64_tr_b16 a[220:221], v179 offset:42752\n"
    "v_mul_f32 v140, s54, v140\n"
    "v_mul_f32 v141, s54, v141\n"
    "v_accvgpr_mov_b32 a13, a77\n"
    "v_mfma_f32_16x16x32_bf16 v[144:147], a[140:143], a[44:47], v[144:147]\n"
    "v_exp_f32 v140, v140\n"
    "v_accvgpr_mov_b32 a14, a78\n"
    "v_mfma_f32_16x16x32_bf16 v[148:151], a[144:147], a[32:35], v[196:199]\n"
    "ds_read_b64_tr_b16 a[222:223], v179 offset:46848\n"
    "v_cvt_pk_bf16_f32 v165, v138, v139\n"
    "v_mfma_f32_16x16x32_bf16 v[148:151], a[148:151], a[36:39], v[148:151]\n"
    "v_exp_f32 v141, v141\n"
    "v_accvgpr_mov_b32 a15, a79\n"
    "v_mfma_f32_16x16x32_bf16 v[148:151], a[152:155], a[40:43], v[148:151]\n"
    "v_mul_f32 v142, s54, v142\n"
    "v_mul_f32 v143, s54, v143\n"
    "v_accvgpr_mov_b32 a16, a80\n"
    "v_mfma_f32_16x16x32_bf16 v[148:151], a[156:159], a[44:47], v[148:151]\n"
    "v_exp_f32 v142, v142\n"
    "v_mfma_f32_16x16x32_bf16 v[152:155], a[128:131], a[48:51], v[212:215]\n"
    "s_waitcnt vmcnt(16) lgkmcnt(0)\n"
    "s_barrier\n"
    "s_cmp_ge_u32 s57, s79\n"
    "s_cbranch_scc1 Lmha_PQ_T0\n"
    "Lmha_PL_T0:\n"
    "s_memrealtime s[94:95]\n"
    "s_waitcnt lgkmcnt(0)\n"
    "s_sub_u32 s94, s94, s78\n"
    "s_cmp_lt_i32 s94, 0\n"
    "s_cbranch_scc0 Lmha_PD_T0\n"
    "s_sleep 1\n"
    "s_branch Lmha_PL_T0\n"
    "Lmha_PD_T0:\n"
    "s_add_u32 s78, s78, s94\n"
    "s_lshr_b32 s94, s65, 2\n"
    "s_add_u32 s78, s78, s94\n"
    "s_sub_u32 s65, s65, 0\n"
    "Lmha_PQ_T0:\n"
    "v_cvt_pk_bf16_f32 v166, v140, v141\n"
    "v_accvgpr_mov_b32 a17, a81\n"
    "v_mfma_f32_16x16x32_bf16 v[152:155], a[132:135], a[52:55], v[152:155]\n"
    "v_exp_f32 v143, v143\n"
    "v_accvgpr_mov_b32 a18, a82\n"
    "v_mfma_f32_16x16x32_bf16 v[152:155], a[136:139], a[56:59], v[152:155]\n"
    "ds_read_b128 a[128:131], v176 offset:0\n"
    "v_cvt_pk_bf16_f32 v167, v142, v143\n"
    "v_mfma_f32_16x16x32_bf16 v[152:155], a[140:143], a[60:63], v[152:155]\n"
    "v_mul_f32 v144, s54, v144\n"
    "v_mul_f32 v145, s54, v145\n"
    "v_accvgpr_mov_b32 a19, a83\n"
    "v_mfma_f32_16x16x32_bf16 v[156:159], a[144:147], a[48:51], v[212:215]\n"
    "ds_read_b128 a[132:135], v176 offset:512\n"
    "v_exp_f32 v144, v144\n"
    "v_accvgpr_mov_b32 a20, a84\n"
    "v_mfma_f32_16x16x32_bf16 v[156:159], a[148:151], a[52:55], v[156:159]\n"
    "v_exp_f32 v145, v145\n"
    "v_mfma_f32_16x16x32_bf16 v[156:159], a[152:155], a[56:59], v[156:159]\n"
    "ds_read_b128 a[136:139], v176 offset:1024\n"
    "v_mul_f32 v146, s54, v146\n"
    "v_mul_f32 v147, s54, v147\n"
    "v_accvgpr_mov_b32 a21, a85\n"
    "v_mfma_f32_16x16x32_bf16 v[156:159], a[156:159], a[60:63], v[156:159]\n"
    "v_exp_f32 v146, v146\n"
    "v_accvgpr_mov_b32 a22, a86\n"
    "v_mfma_f32_16x16x32_bf16 v[0:3], a[192:195], v[160:163], v[0:3]\n"
    "ds_read_b128 a[140:143], v176 offset:1536\n"
    "v_cvt_pk_bf16_f32 v168, v144, v145\n"
    "v_mfma_f32_16x16x32_bf16 v[4:7], a[196:199], v[160:163], v[4:7]\n"
    "ds_read_b128 a[32:35], v180 offset:0\n"
    "v_exp_f32 v147, v147\n"
    "v_accvgpr_mov_b32 a23, a87\n"
    "v_mfma_f32_16x16x32_bf16 v[8:11], a[200:203], v[160:163], v[8:11]\n"
    "ds_read_b128 a[144:147], v176 offset:4096\n"
    "v_mul_f32 v148, s54, v148\n"
    "v_accvgpr_mov_b32 a24, a88\n"
    "v_mfma_f32_16x16x32_bf16 v[12:15], a[204:207], v[160:163], v[12:15]\n"
    "ds_read_b128 a[36:39], v180 offset:512\n"
    "v_exp_f32 v148, v148\n"
    "v_mfma_f32_16x16x32_bf16 v[16:19], a[208:211], v[160:163], v[16:19]\n"
    "ds_read_b128 a[148:151], v176 offset:4608\n"
    "v_mul_f32 v149, s54, v149\n"
    "v_accvgpr_mov_b32 a25, a89\n"
    "v_mfma_f32_16x16x32_bf16 v[20:23], a[212:215], v[160:163], v[20:23]\n"
    "ds_read_b128 a[48:51], v180 offset:2048\n"
    "v_exp_f32 v149, v149\n"
    "v_accvgpr_mov_b32 a26, a90\n"
    "v_mfma_f32_16x16x32_bf16 v[24:27], a[216:219], v[160:163], v[24:27]\n"
    "ds_read_b128 a[152:155], v176 offset:5120\n"
    "v_cvt_pk_bf16_f32 v169, v146, v147\n"
    "v_mfma_f32_16x16x32_bf16 v[28:31], a[220:223], v[160:163], v[28:31]\n"
    "v_mul_f32 v150, s54, v150\n"
    "v_mul_f32 v151, s54, v151\n"
    "v_accvgpr_mov_b32 a27, a91\n"
    "v_mfma_f32_16x16x32_bf16 a[224:227], a[240:243], v[160:163], a[224:227]\n"
    "ds_read_b128 a[156:159], v176 offset:5632\n"
    "v_exp_f32 v150, v150\n"
    "v_accvgpr_mov_b32 a28, a92\n"
    "v_mfma_f32_16x16x32_bf16 v[32:35], a[192:195], v[164:167], v[32:35]\n"
    "ds_read_b128 a[52:55], v180 offset:2560\n"
    "v_cvt_pk_bf16_f32 v170, v148, v149\n"
    "v_mfma_f32_16x16x32_bf16 v[36:39], a[196:199], v[164:167], v[36:39]\n"
    "ds_read_b128 a[40:43], v180 offset:16384\n"
    "v_exp_f32 v151, v151\n"
    "v_accvgpr_mov_b32 a29, a93\n"
    "v_mfma_f32_16x16x32_bf16 v[40:43], a[200:203], v[164:167], v[40:43]\n"
    "ds_read_b128 a[44:47], v180 offset:16896\n"
    "v_cvt_pk_bf16_f32 v171, v150, v151\n"
    "v_accvgpr_mov_b32 a30, a94\n"
    "v_mfma_f32_16x16x32_bf16 v[44:47], a[204:207], v[164:167], v[44:47]\n"
    "ds_read_b128 a[56:59], v180 offset:18432\n"
    "v_mul_f32 v152, s54, v152\n"
    "v_mul_f32 v153, s54, v153\n"
    "v_mfma_f32_16x16x32_bf16 v[48:51], a[208:211], v[164:167], v[48:51]\n"
    "ds_read_b128 a[60:63], v180 offset:18944\n"
    "v_exp_f32 v152, v152\n"
    "v_accvgpr_mov_b32 a31, a95\n"
    "v_mfma_f32_16x16x32_bf16 v[52:55], a[212:215], v[164:167], v[52:55]\n"
    "ds_read_b64_tr_b16 a[160:161], v178 offset:0\n"
    "v_exp_f32 v153, v153\n"
    "v_mfma_f32_16x16x32_bf16 v[56:59], a[216:219], v[164:167], v[56:59]\n"
    "v_mul_f32 v154, s54, v154\n"
    "v_exp_f32 v154, v154\n"
    "v_mfma_f32_16x16x32_bf16 v[60:63], a[220:223], v[164:167], v[60:63]\n"
    "s_waitcnt lgkmcnt(6)\n"
    "ds_read_b64_tr_b16 a[162:163], v178 offset:4096\n"
    "v_mul_f32 v155, s54, v155\n"
    "v_exp_f32 v155, v155\n"
    "v_mfma_f32_16x16x32_bf16 a[228:231], a[240:243], v[164:167], a[228:231]\n"
    "v_cvt_pk_bf16_f32 v172, v152, v153\n"
    "v_mul_f32 v156, s54, v156\n"
    "v_mul_f32 v157, s54, v157\n"
    "s_cmp_eq_u32 s57, 3\n"
    "s_cbranch_scc1 Lmha_TAIL\n"
    "Lmha_P0:\n"
    "v_mfma_f32_16x16x32_bf16 v[128:131], a[128:131], a[0:3], 0\n"
    "v_accvgpr_read_b32 v200, a224\n"
    "v_exp_f32 v156, v156\n"
    "v_mfma_f32_16x16x32_bf16 v[128:131], a[132:135], a[4:7], v[128:131]\n"
    "s_add_u32 s48, s48, 0x1000000\n"
    "s_addc_u32 s49, s49, 0\n"
    "v_cmp_nle_f32 vcc, v200, v206\n"
    "v_rcp_f32 v200, v200\n"
    "v_cvt_pk_bf16_f32 v173, v154, v155\n"
    "v_mfma_f32_16x16x32_bf16 v[128:131], a[136:139], a[8:11], v[128:131]\n"
    "s_or_b64 s[60:61], s[60:61], vcc\n"
    "ds_read_b64_tr_b16 a[164:165], v178 offset:256\n"
    "v_mul_f32 v0, v0, v200\n"
    "v_mul_f32 v1, v1, v200\n"
    "v_mul_f32 v2, v2, v200\n"
    "v_mul_f32 v3, v3, v200\n"
    "v_cvt_pk_bf16_f32 v208, v0, v1\n"
    "v_cvt_pk_bf16_f32 v209, v2, v3\n"
    "v_mul_f32 v158, s54, v158\n"
    "v_mfma_f32_16x16x32_bf16 v[128:131], a[140:143], a[12:15], v[128:131]\n"
    "ds_write_b64 v187, v[208:209] offset:0\n"
    "v_accvgpr_read_b32 v201, a228\n"
    "v_mul_f32 v4, v4, v200\n"
    "v_mul_f32 v5, v5, v200\n"
    "v_mul_f32 v6, v6, v200\n"
    "v_mul_f32 v159, s54, v159\n"
    "v_mfma_f32_16x16x32_bf16 v[132:135], a[144:147], a[0:3], 0\n"
    "v_cmp_nle_f32 vcc, v201, v206\n"
    "v_rcp_f32 v201, v201\n"
    "v_mul_f32 v7, v7, v200\n"
    "v_cvt_pk_bf16_f32 v210, v4, v5\n"
    "v_cvt_pk_bf16_f32 v211, v6, v7\n"
    "v_mfma_f32_16x16x32_bf16 v[132:135], a[148:151], a[4:7], v[132:135]\n"
    "s_or_b64 s[60:61], s[60:61], vcc\n"
    "ds_write_b64 v188, v[210:211] offset:0\n"
    "v_mul_f32 v8, v8, v200\n"
    "v_mul_f32 v9, v9, v200\n"
    "v_mul_f32 v10, v10, v200\n"
    "v_mul_f32 v11, v11, v200\n"
    "v_cvt_pk_bf16_f32 v208, v8, v9\n"
    "v_cvt_pk_bf16_f32 v209, v10, v11\n"
    "v_mfma_f32_16x16x32_bf16 v[132:135], a[152:155], a[8:11], v[132:135]\n"
    "ds_read_b64_tr_b16 a[166:167], v178 offset:4352\n"
    "ds_write_b64 v189, v[208:209] offset:0\n"
    "v_mul_f32 v12, v12, v200\n"
    "v_mul_f32 v13, v13, v200\n"
    "v_mul_f32 v14, v14, v200\n"
    "v_exp_f32 v157, v157\n"
    "v_mfma_f32_16x16x32_bf16 v[132:135], a[156:159], a[12:15], v[132:135]\n"
    "v_mul_f32 v15, v15, v200\n"
    "v_cvt_pk_bf16_f32 v210, v12, v13\n"
    "v_cvt_pk_bf16_f32 v211, v14, v15\n"
    "v_cvt_pk_bf16_f32 v174, v156, v157\n"
    "v_mfma_f32_16x16x32_bf16 v[136:139], a[128:131], a[16:19], 0\n"
    "ds_write_b64 v190, v[210:211] offset:0\n"
    "v_mul_f32 v16, v16, v200\n"
    "v_mul_f32 v17, v17, v200\n"
    "v_mul_f32 v18, v18, v200\n"
    "v_mul_f32 v19, v19, v200\n"
    "v_cvt_pk_bf16_f32 v208, v16, v17\n"
    "v_cvt_pk_bf16_f32 v209, v18, v19\n"
    "v_mfma_f32_16x16x32_bf16 v[136:139], a[132:135], a[20:23], v[136:139]\n"
    "ds_write_b64 v191, v[208:209] offset:0\n"
    "v_mul_f32 v20, v20, v200\n"
    "v_mul_f32 v21, v21, v200\n"
    "v_mul_f32 v22, v22, v200\n"
    "v_mfma_f32_16x16x32_bf16 v[136:139], a[136:139], a[24:27], v[136:139]\n"
    "v_mul_f32 v23, v23, v200\n"
    "v_cvt_pk_bf16_f32 v210, v20, v21\n"
    "v_cvt_pk_bf16_f32 v211, v22, v23\n"
    "v_exp_f32 v158, v158\n"
    "v_exp_f32 v159, v159\n"
    "v_mfma_f32_16x16x32_bf16 v[136:139], a[140:143], a[28:31], v[136:139]\n"
    "ds_write_b64 v192, v[210:211] offset:0\n"
    "v_mul_f32 v24, v24, v200\n"
    "v_mul_f32 v25, v25, v200\n"
    "v_mul_f32 v26, v26, v200\n"
    "v_mul_f32 v27, v27, v200\n"
    "v_cvt_pk_bf16_f32 v208, v24, v25\n"
    "v_cvt_pk_bf16_f32 v209, v26, v27\n"
    "v_max3_f32 v204, v128, v129, v130\n"
    "v_max3_f32 v205, v131, v132, v133\n"
    "v_max3_f32 v204, v204, v134, v135\n"
    "v_cvt_pk_bf16_f32 v175, v158, v159\n"
    "v_mfma_f32_16x16x32_bf16 v[140:143], a[144:147], a[16:19], 0\n"
    "ds_read_b64_tr_b16 a[168:169], v178 offset:512\n"
    "ds_write_b64 v193, v[208:209] offset:0\n"
    "v_mul_f32 v28, v28, v200\n"
    "v_mul_f32 v29, v29, v200\n"
    "v_mul_f32 v30, v30, v200\n"
    "v_max_f32 v204, v204, v205\n"
    "v_mov_b32 v205, v204\n"
    "v_mfma_f32_16x16x32_bf16 v[140:143], a[148:151], a[20:23], v[140:143]\n"
    "v_mul_f32 v31, v31, v200\n"
    "v_cvt_pk_bf16_f32 v210, v28, v29\n"
    "v_cvt_pk_bf16_f32 v211, v30, v31\n"
    "v_permlane32_swap_b32 v204, v205\n"
    "v_max_f32 v204, v204, v205\n"
    "v_mfma_f32_16x16x32_bf16 v[140:143], a[152:155], a[24:27], v[140:143]\n"
    "ds_read_b64_tr_b16 a[170:171], v178 offset:4608\n"
    "ds_write_b64 v194, v[210:211] offset:0\n"
    "v_mov_b32 v205, v204\n"
    "v_mfma_f32_16x16x32_bf16 v[140:143], a[156:159], a[28:31], v[140:143]\n"
    "s_nop 0\n"
    "v_permlane16_swap_b32 v204, v205\n"
    "v_max_f32 v204, v204, v205\n"
    "v_mul_f32 v248, -1.0, v204\n"
    "v_mul_f32 v249, -1.0, v204\n"
    "v_mul_f32 v250, -1.0, v204\n"
    "v_mfma_f32_16x16x32_bf16 v[64:67], a[192:195], v[168:171], v[64:67]\n"
    "s_waitcnt lgkmcnt(0)\n"
    "ds_read_b64_tr_b16 a[172:173], v178 offset:768\n"
    "ds_read_b128 v[216:219], v185 offset:0\n"
    "ds_read_b128 v[220:223], v186 offset:1024\n"
    "v_mul_f32 v32, v32, v201\n"
    "v_mul_f32 v33, v33, v201\n"
    "v_mul_f32 v34, v34, v201\n"
    "v_mul_f32 v35, v35, v201\n"
    "v_cvt_pk_bf16_f32 v208, v32, v33\n"
    "v_cvt_pk_bf16_f32 v209, v34, v35\n"
    "v_mul_f32 v251, -1.0, v204\n"
    "v_sub_f32 v128, v128, v204\n"
    "v_sub_f32 v129, v129, v204\n"
    "v_mfma_f32_16x16x32_bf16 v[68:71], a[196:199], v[168:171], v[68:71]\n"
    "ds_read_b128 v[224:227], v185 offset:2048\n"
    "ds_read_b128 v[228:231], v186 offset:3072\n"
    "ds_write_b64 v187, v[208:209] offset:0\n"
    "v_mul_f32 v36, v36, v201\n"
    "v_mul_f32 v37, v37, v201\n"
    "v_mul_f32 v38, v38, v201\n"
    "v_sub_f32 v130, v130, v204\n"
    "v_sub_f32 v131, v131, v204\n"
    "v_sub_f32 v132, v132, v204\n"
    "v_mfma_f32_16x16x32_bf16 v[72:75], a[200:203], v[168:171], v[72:75]\n"
    "ds_read_b64_tr_b16 a[174:175], v178 offset:4864\n"
    "v_mul_f32 v39, v39, v201\n"
    "v_cvt_pk_bf16_f32 v210, v36, v37\n"
    "v_cvt_pk_bf16_f32 v211, v38, v39\n"
    "v_sub_f32 v133, v133, v204\n"
    "v_sub_f32 v134, v134, v204\n"
    "v_sub_f32 v135, v135, v204\n"
    "v_mfma_f32_16x16x32_bf16 v[76:79], a[204:207], v[168:171], v[76:79]\n"
    "s_waitcnt lgkmcnt(2)\n"
    "buffer_store_dwordx4 v[216:219], v184, s[48:51], 0 offen offset:0 sc1\n"
    "ds_write_b64 v188, v[210:211] offset:0\n"
    "v_mul_f32 v40, v40, v201\n"
    "v_mul_f32 v41, v41, v201\n"
    "v_mul_f32 v42, v42, v201\n"
    "v_mul_f32 v43, v43, v201\n"
    "v_cvt_pk_bf16_f32 v208, v40, v41\n"
    "v_cvt_pk_bf16_f32 v209, v42, v43\n"
    "v_mul_f32 v128, s54, v128\n"
    "v_mul_f32 v129, s54, v129\n"
    "v_mfma_f32_16x16x32_bf16 v[80:83], a[208:211], v[168:171], v[80:83]\n"
    "ds_read_b64_tr_b16 a[176:177], v178 offset:1024\n"
    "ds_write_b64 v189, v[208:209] offset:0\n"
    "v_mul_f32 v44, v44, v201\n"
    "v_mul_f32 v45, v45, v201\n"
    "v_mul_f32 v46, v46, v201\n"
    "v_exp_f32 v128, v128\n"
    "v_mul_f32 v130, s54, v130\n"
    "v_mfma_f32_16x16x32_bf16 v[84:87], a[212:215], v[168:171], v[84:87]\n"
    "buffer_store_dwordx4 v[220:223], v184, s[48:51], 0 offen offset:1024 sc1\n"
    "v_mul_f32 v47, v47, v201\n"
    "v_cvt_pk_bf16_f32 v210, v44, v45\n"
    "v_cvt_pk_bf16_f32 v211, v46, v47\n"
    "v_exp_f32 v129, v129\n"
    "v_mul_f32 v131, s54, v131\n"
    "v_mfma_f32_16x16x32_bf16 v[88:91], a[216:219], v[168:171], v[88:91]\n"
    "ds_read_b64_tr_b16 a[178:179], v178 offset:5120\n"
    "ds_write_b64 v190, v[210:211] offset:0\n"
    "v_mul_f32 v48, v48, v201\n"
    "v_mul_f32 v49, v49, v201\n"
    "v_mul_f32 v50, v50, v201\n"
    "v_mul_f32 v51, v51, v201\n"
    "v_cvt_pk_bf16_f32 v208, v48, v49\n"
    "v_cvt_pk_bf16_f32 v209, v50, v51\n"
    "v_exp_f32 v130, v130\n"
    "v_cvt_pk_bf16_f32 v160, v128, v129\n"
    "v_mfma_f32_16x16x32_bf16 v[92:95], a[220:223], v[168:171], v[92:95]\n"
    "buffer_store_dwordx4 v[224:227], v184, s[48:51], 0 offen offset:2048 sc1\n"
    "ds_write_b64 v191, v[208:209] offset:0\n"
    "v_mul_f32 v52, v52, v201\n"
    "v_mul_f32 v53, v53, v201\n"
    "v_mul_f32 v54, v54, v201\n"
    "v_exp_f32 v131, v131\n"
    "v_mul_f32 v132, s54, v132\n"
    "v_mfma_f32_16x16x32_bf16 a[232:235], a[240:243], v[168:171], a[232:235]\n"
    "ds_read_b64_tr_b16 a[180:181], v178 offset:1280\n"
    "v_mul_f32 v55, v55, v201\n"
    "v_cvt_pk_bf16_f32 v210, v52, v53\n"
    "v_cvt_pk_bf16_f32 v211, v54, v55\n"
    "v_exp_f32 v132, v132\n"
    "v_mul_f32 v133, s54, v133\n"
    "v_mfma_f32_16x16x32_bf16 v[96:99], a[192:195], v[172:175], v[96:99]\n"
    "buffer_store_dwordx4 v[228:231], v184, s[48:51], 0 offen offset:3072 sc1\n"
    "ds_write_b64 v192, v[210:211] offset:0\n"
    "v_mul_f32 v56, v56, v201\n"
    "v_mul_f32 v57, v57, v201\n"
    "v_mul_f32 v58, v58, v201\n"
    "v_mul_f32 v59, v59, v201\n"
    "v_cvt_pk_bf16_f32 v208, v56, v57\n"
    "v_cvt_pk_bf16_f32 v209, v58, v59\n"
    "v_exp_f32 v133, v133\n"
    "v_mfma_f32_16x16x32_bf16 v[100:103], a[196:199], v[172:175], v[100:103]\n"
    "ds_read_b64_tr_b16 a[182:183], v178 offset:5376\n"
    "ds_write_b64 v193, v[208:209] offset:0\n"
    "v_mul_f32 v60, v60, v201\n"
    "v_mul_f32 v61, v61, v201\n"
    "v_mul_f32 v62, v62, v201\n"
    "v_cvt_pk_bf16_f32 v161, v130, v131\n"
    "v_mul_f32 v134, s54, v134\n"
    "v_mul_f32 v135, s54, v135\n"
    "v_mfma_f32_16x16x32_bf16 v[104:107], a[200:203], v[172:175], v[104:107]\n"
    "v_accvgpr_read_b32 v202, a232\n"
    "v_mul_f32 v63, v63, v201\n"
    "v_cvt_pk_bf16_f32 v210, v60, v61\n"
    "v_cvt_pk_bf16_f32 v211, v62, v63\n"
    "v_exp_f32 v134, v134\n"
    "v_cvt_pk_bf16_f32 v162, v132, v133\n"
    "v_exp_f32 v135, v135\n"
    "v_mfma_f32_16x16x32_bf16 v[108:111], a[204:207], v[172:175], v[108:111]\n"
    "ds_write_b64 v194, v[210:211] offset:0\n"
    "v_cmp_nle_f32 vcc, v202, v206\n"
    "v_rcp_f32 v202, v202\n"
    "v_cvt_pk_bf16_f32 v163, v134, v135\n"
    "v_max3_f32 v204, v136, v137, v138\n"
    "v_max3_f32 v205, v139, v140, v141\n"
    "v_max3_f32 v204, v204, v142, v143\n"
    "v_mfma_f32_16x16x32_bf16 v[112:115], a[208:211], v[172:175], v[112:115]\n"
    "s_or_b64 s[60:61], s[60:61], vcc\n"
    "v_max_f32 v204, v204, v205\n"
    "v_mov_b32 v205, v204\n"
    "v_mfma_f32_16x16x32_bf16 v[116:119], a[212:215], v[172:175], v[116:119]\n"
    "s_waitcnt lgkmcnt(0)\n"
    "ds_read_b64_tr_b16 a[184:185], v178 offset:1536\n"
    "ds_read_b128 v[232:235], v185 offset:0\n"
    "ds_read_b128 v[236:239], v186 offset:1024\n"
    "v_mul_f32 v64, v64, v202\n"
    "v_mul_f32 v65, v65, v202\n"
    "v_mul_f32 v66, v66, v202\n"
    "v_mul_f32 v67, v67, v202\n"
    "v_cvt_pk_bf16_f32 v208, v64, v65\n"
    "v_cvt_pk_bf16_f32 v209, v66, v67\n"
    "v_permlane32_swap_b32 v204, v205\n"
    "v_max_f32 v204, v204, v205\n"
    "v_mov_b32 v205, v204\n"
    "v_mfma_f32_16x16x32_bf16 v[120:123], a[216:219], v[172:175], v[120:123]\n"
    "ds_read_b128 v[240:243], v185 offset:2048\n"
    "ds_read_b128 v[244:247], v186 offset:3072\n"
    "ds_write_b64 v187, v[208:209] offset:0\n"
    "v_mul_f32 v68, v68, v202\n"
    "v_mul_f32 v69, v69, v202\n"
    "v_mul_f32 v70, v70, v202\n"
    "v_permlane16_swap_b32 v204, v205\n"
    "v_max_f32 v204, v204, v205\n"
    "v_mul_f32 v252, -1.0, v204\n"
    "v_mfma_f32_16x16x32_bf16 v[124:127], a[220:223], v[172:175], v[124:127]\n"
    "v_mul_f32 v71, v71, v202\n"
    "v_cvt_pk_bf16_f32 v210, v68, v69\n"
    "v_cvt_pk_bf16_f32 v211, v70, v71\n"
    "v_mul_f32 v253, -1.0, v204\n"
    "v_mul_f32 v254, -1.0, v204\n"
    "v_mul_f32 v255, -1.0, v204\n"
    "v_mfma_f32_16x16x32_bf16 a[236:239], a[240:243], v[172:175], a[236:239]\n"
    "s_waitcnt lgkmcnt(1)\n"
    "buffer_store_dwordx4 v[232:235], v184, s[48:51], s66 offen offset:0 sc1\n"
    "ds_write_b64 v188, v[210:211] offset:0\n"
    "v_mul_f32 v72, v72, v202\n"
    "v_mul_f32 v73, v73, v202\n"
    "v_mul_f32 v74, v74, v202\n"
    "v_mul_f32 v75, v75, v202\n"
    "v_cvt_pk_bf16_f32 v208, v72, v73\n"
    "v_cvt_pk_bf16_f32 v209, v74, v75\n"
    "v_sub_f32 v136, v136, v204\n"
    "v_sub_f32 v137, v137, v204\n"
    "v_mfma_f32_16x16x32_bf16 v[144:147], a[128:131], a[32:35], 0\n"
    "ds_read_b64_tr_b16 a[186:187], v178 offset:5632\n"
    "ds_write_b64 v189, v[208:209] offset:0\n"
    "v_mul_f32 v76, v76, v202\n"
    "v_mul_f32 v77, v77, v202\n"
    "v_mul_f32 v78, v78, v202\n"
    "v_sub_f32 v138, v138, v204\n"
    "v_sub_f32 v139, v139, v204\n"
    "v_sub_f32 v140, v140, v204\n"
    "v_mfma_f32_16x16x32_bf16 v[144:147], a[132:135], a[36:39], v[144:147]\n"
    "buffer_store_dwordx4 v[236:239], v184, s[48:51], s66 offen offset:1024 sc1\n"
    "v_mul_f32 v79, v79, v202\n"
    "v_cvt_pk_bf16_f32 v210, v76, v77\n"
    "v_cvt_pk_bf16_f32 v211, v78, v79\n"
    "v_sub_f32 v141, v141, v204\n"
    "v_sub_f32 v142, v142, v204\n"
    "v_sub_f32 v143, v143, v204\n"
    "v_mfma_f32_16x16x32_bf16 v[144:147], a[136:139], a[40:43], v[144:147]\n"
    "ds_read_b64_tr_b16 a[188:189], v178 offset:1792\n"
    "ds_write_b64 v190, v[210:211] offset:0\n"
    "v_accvgpr_read_b32 v203, a236\n"
    "v_mul_f32 v80, v80, v202\n"
    "v_mul_f32 v81, v81, v202\n"
    "v_mul_f32 v82, v82, v202\n"
    "v_mul_f32 v83, v83, v202\n"
    "v_cvt_pk_bf16_f32 v208, v80, v81\n"
    "v_cvt_pk_bf16_f32 v209, v82, v83\n"
    "v_mul_f32 v136, s54, v136\n"
    "v_mul_f32 v137, s54, v137\n"
    "v_mfma_f32_16x16x32_bf16 v[144:147], a[140:143], a[44:47], v[144:147]\n"
    "buffer_store_dwordx4 v[240:243], v184, s[48:51], s66 offen offset:2048 sc1\n"
    "ds_write_b64 v191, v[208:209] offset:0\n"
    "v_cmp_nle_f32 vcc, v203, v206\n"
    "v_rcp_f32 v203, v203\n"
    "v_mul_f32 v84, v84, v202\n"
    "v_mul_f32 v85, v85, v202\n"
    "v_mul_f32 v86, v86, v202\n"
    "v_exp_f32 v136, v136\n"
    "v_mul_f32 v138, s54, v138\n"
    "v_mfma_f32_16x16x32_bf16 v[148:151], a[144:147], a[32:35], 0\n"
    "s_or_b64 s[60:61], s[60:61], vcc\n"
    "ds_read_b64_tr_b16 a[190:191], v178 offset:5888\n"
    "v_mul_f32 v87, v87, v202\n"
    "v_cvt_pk_bf16_f32 v210, v84, v85\n"
    "v_cvt_pk_bf16_f32 v211, v86, v87\n"
    "v_exp_f32 v137, v137\n"
    "v_mul_f32 v139, s54, v139\n"
    "v_mfma_f32_16x16x32_bf16 v[148:151], a[148:151], a[36:39], v[148:151]\n"
    "buffer_store_dwordx4 v[244:247], v184, s[48:51], s66 offen offset:3072 sc1\n"
    "ds_write_b64 v192, v[210:211] offset:0\n"
    "v_mul_f32 v88, v88, v202\n"
    "v_mul_f32 v89, v89, v202\n"
    "v_mul_f32 v90, v90, v202\n"
    "v_mul_f32 v91, v91, v202\n"
    "v_cvt_pk_bf16_f32 v208, v88, v89\n"
    "v_cvt_pk_bf16_f32 v209, v90, v91\n"
    "v_exp_f32 v138, v138\n"
    "v_mfma_f32_16x16x32_bf16 v[148:151], a[152:155], a[40:43], v[148:151]\n"
    "s_cmp_lg_u64 s[60:61], 0\n"
    "s_cselect_b32 s59, 1, 0\n"
    "ds_write_b64 v193, v[208:209] offset:0\n"
    "v_mul_f32 v92, v92, v202\n"
    "v_mul_f32 v93, v93, v202\n"
    "v_mul_f32 v94, v94, v202\n"
    "v_exp_f32 v139, v139\n"
    "v_cvt_pk_bf16_f32 v164, v136, v137\n"
    "v_mfma_f32_16x16x32_bf16 v[148:151], a[156:159], a[44:47], v[148:151]\n"
    "s_lshl_b32 s59, s59, s57\n"
    "s_or_b32 s62, s62, s59\n"
    "s_mov_b64 s[60:61], 0\n"
    "v_mul_f32 v95, v95, v202\n"
    "v_cvt_pk_bf16_f32 v210, v92, v93\n"
    "v_cvt_pk_bf16_f32 v211, v94, v95\n"
    "v_mul_f32 v140, s54, v140\n"
    "v_exp_f32 v140, v140\n"
    "v_mfma_f32_16x16x32_bf16 v[152:155], a[128:131], a[48:51], 0\n"
    "s_waitcnt lgkmcnt(0)\n"
    "ds_write_b64 v194, v[210:211] offset:0\n"
    "v_mul_f32 v141, s54, v141\n"
    "v_exp_f32 v141, v141\n"
    "v_mfma_f32_16x16x32_bf16 v[152:155], a[132:135], a[52:55], v[152:155]\n"
    "s_add_u32 s57, s57, 1\n"
    "v_cvt_pk_bf16_f32 v165, v138, v139\n"
    "v_mul_f32 v142, s54, v142\n"
    "v_exp_f32 v142, v142\n"
    "v_mfma_f32_16x16x32_bf16 v[152:155], a[136:139], a[56:59], v[152:155]\n"
    "s_waitcnt lgkmcnt(0)\n"
    "ds_read_b128 a[128:131], v176 offset:8192\n"
    "ds_read_b128 v[216:219], v185 offset:0\n"
    "ds_read_b128 v[220:223], v186 offset:1024\n"
    "v_mul_f32 v96, v96, v203\n"
    "v_mul_f32 v97, v97, v203\n"
    "v_mul_f32 v98, v98, v203\n"
    "v_mul_f32 v99, v99, v203\n"
    "v_cvt_pk_bf16_f32 v208, v96, v97\n"
    "v_cvt_pk_bf16_f32 v209, v98, v99\n"
    "v_mul_f32 v143, s54, v143\n"
    "v_exp_f32 v143, v143\n"
    "v_cvt_pk_bf16_f32 v166, v140, v141\n"
    "v_mfma_f32_16x16x32_bf16 v[152:155], a[140:143], a[60:63], v[152:155]\n"
    "ds_read_b128 v[224:227], v185 offset:2048\n"
    "ds_read_b128 v[228:231], v186 offset:3072\n"
    "ds_write_b64 v187, v[208:209] offset:0\n"
    "v_mul_f32 v100, v100, v203\n"
    "v_mul_f32 v101, v101, v203\n"
    "v_mul_f32 v102, v102, v203\n"
    "v_cvt_pk_bf16_f32 v167, v142, v143\n"
    "v_max3_f32 v204, v144, v145, v146\n"
    "v_max3_f32 v205, v147, v148, v149\n"
    "v_max3_f32 v204, v204, v150, v151\n"
    "v_mfma_f32_16x16x32_bf16 v[156:159], a[144:147], a[48:51], 0\n"
    "ds_read_b128 a[132:135], v176 offset:8704\n"
    "v_mul_f32 v103, v103, v203\n"
    "v_cvt_pk_bf16_f32 v210, v100, v101\n"
    "v_cvt_pk_bf16_f32 v211, v102, v103\n"
    "v_max_f32 v204, v204, v205\n"
    "v_mov_b32 v205, v204\n"
    "v_mfma_f32_16x16x32_bf16 v[156:159], a[148:151], a[52:55], v[156:159]\n"
    "s_waitcnt lgkmcnt(2)\n"
    "buffer_store_dwordx4 v[216:219], v184, s[48:51], s67 offen offset:0 sc1\n"
    "ds_write_b64 v188, v[210:211] offset:0\n"
    "v_mul_f32 v104, v104, v203\n"
    "v_mul_f32 v105, v105, v203\n"
    "v_mul_f32 v106, v106, v203\n"
    "v_mul_f32 v107, v107, v203\n"
    "v_cvt_pk_bf16_f32 v208, v104, v105\n"
    "v_cvt_pk_bf16_f32 v209, v106, v107\n"
    "v_permlane32_swap_b32 v204, v205\n"
    "v_max_f32 v204, v204, v205\n"
    "v_mov_b32 v205, v204\n"
    "v_mfma_f32_16x16x32_bf16 v[156:159], a[152:155], a[56:59], v[156:159]\n"
    "ds_read_b128 a[136:139], v176 offset:9216\n"
    "ds_write_b64 v189, v[208:209] offset:0\n"
    "v_mul_f32 v108, v108, v203\n"
    "v_mul_f32 v109, v109, v203\n"
    "v_mul_f32 v110, v110, v203\n"
    "v_permlane16_swap_b32 v204, v205\n"
    "v_max_f32 v204, v204, v205\n"
    "v_mul_f32 v196, -1.0, v204\n"
    "v_mfma_f32_16x16x32_bf16 v[156:159], a[156:159], a[60:63], v[156:159]\n"
    "buffer_store_dwordx4 v[220:223], v184, s[48:51], s67 offen offset:1024 sc1\n"
    "v_mul_f32 v111, v111, v203\n"
    "v_cvt_pk_bf16_f32 v210, v108, v109\n"
    "v_cvt_pk_bf16_f32 v211, v110, v111\n"
    "v_mul_f32 v197, -1.0, v204\n"
    "v_mul_f32 v198, -1.0, v204\n"
    "v_mfma_f32_16x16x32_bf16 v[0:3], a[160:163], v[160:163], 0\n"
    "ds_read_b128 a[140:143], v176 offset:9728\n"
    "ds_write_b64 v190, v[210:211] offset:0\n"
    "v_mul_f32 v112, v112, v203\n"
    "v_mul_f32 v113, v113, v203\n"
    "v_mul_f32 v114, v114, v203\n"
    "v_mul_f32 v115, v115, v203\n"
    "v_cvt_pk_bf16_f32 v208, v112, v113\n"
    "v_cvt_pk_bf16_f32 v209, v114, v115\n"
    "v_mul_f32 v199, -1.0, v204\n"
    "v_sub_f32 v144, v144, v204\n"
    "v_sub_f32 v145, v145, v204\n"
    "v_mfma_f32_16x16x32_bf16 v[4:7], a[164:167], v[160:163], 0\n"
    "buffer_store_dwordx4 v[224:227], v184, s[48:51], s67 offen offset:2048 sc1\n"
    "ds_write_b64 v191, v[208:209] offset:0\n"
    "v_mul_f32 v116, v116, v203\n"
    "v_mul_f32 v117, v117, v203\n"
    "v_mul_f32 v118, v118, v203\n"
    "v_sub_f32 v146, v146, v204\n"
    "v_sub_f32 v147, v147, v204\n"
    "v_sub_f32 v148, v148, v204\n"
    "v_mfma_f32_16x16x32_bf16 v[8:11], a[168:171], v[160:163], 0\n"
    "ds_read_b128 a[144:147], v176 offset:12288\n"
    "v_mul_f32 v119, v119, v203\n"
    "v_cvt_pk_bf16_f32 v210, v116, v117\n"
    "v_cvt_pk_bf16_f32 v211, v118, v119\n"
    "v_sub_f32 v149, v149, v204\n"
    "v_sub_f32 v150, v150, v204\n"
    "v_mfma_f32_16x16x32_bf16 v[12:15], a[172:175], v[160:163], 0\n"
    "buffer_store_dwordx4 v[228:231], v184, s[48:51], s67 offen offset:3072 sc1\n"
    "ds_write_b64 v192, v[210:211] offset:0\n"
    "v_mul_f32 v120, v120, v203\n"
    "v_mul_f32 v121, v121, v203\n"
    "v_mul_f32 v122, v122, v203\n"
    "v_mul_f32 v123, v123, v203\n"
    "v_cvt_pk_bf16_f32 v208, v120, v121\n"
    "v_cvt_pk_bf16_f32 v209, v122, v123\n"
    "v_sub_f32 v151, v151, v204\n"
    "v_mul_f32 v144, s54, v144\n"
    "v_mul_f32 v145, s54, v145\n"
    "v_mfma_f32_16x16x32_bf16 v[16:19], a[176:179], v[160:163], 0\n"
    "ds_read_b128 a[148:151], v176 offset:12800\n"
    "ds_write_b64 v193, v[208:209] offset:0\n"
    "v_mul_f32 v124, v124, v203\n"
    "v_mul_f32 v125, v125, v203\n"
    "v_mul_f32 v126, v126, v203\n"
    "v_exp_f32 v144, v144\n"
    "v_mfma_f32_16x16x32_bf16 v[20:23], a[180:183], v[160:163], 0\n"
    "v_mul_f32 v127, v127, v203\n"
    "v_cvt_pk_bf16_f32 v210, v124, v125\n"
    "v_cvt_pk_bf16_f32 v211, v126, v127\n"
    "v_exp_f32 v145, v145\n"
    "v_mul_f32 v146, s54, v146\n"
    "v_mfma_f32_16x16x32_bf16 v[24:27], a[184:187], v[160:163], 0\n"
    "ds_read_b128 a[152:155], v176 offset:13312\n"
    "ds_write_b64 v194, v[210:211] offset:0\n"
    "v_exp_f32 v146, v146\n"
    "v_mul_f32 v147, s54, v147\n"
    "v_mfma_f32_16x16x32_bf16 v[28:31], a[188:191], v[160:163], 0\n"
    "v_exp_f32 v147, v147\n"
    "v_cvt_pk_bf16_f32 v168, v144, v145\n"
    "v_mul_f32 v148, s54, v148\n"
    "v_mfma_f32_16x16x32_bf16 a[224:227], a[240:243], v[160:163], 0\n"
    "s_waitcnt lgkmcnt(0)\n"
    "ds_read_b128 a[156:159], v176 offset:13824\n"
    "ds_read_b128 v[232:235], v185 offset:0\n"
    "ds_read_b128 v[236:239], v186 offset:1024\n"
    "v_exp_f32 v148, v148\n"
    "v_mul_f32 v149, s54, v149\n"
    "v_mfma_f32_16x16x32_bf16 v[32:35], a[160:163], v[164:167], 0\n"
    "ds_read_b128 v[240:243], v185 offset:2048\n"
    "ds_read_b128 v[244:247], v186 offset:3072\n"
    "v_exp_f32 v149, v149\n"
    "v_cvt_pk_bf16_f32 v169, v146, v147\n"
    "v_mfma_f32_16x16x32_bf16 v[36:39], a[164:167], v[164:167], 0\n"
    "v_mul_f32 v150, s54, v150\n"
    "v_exp_f32 v150, v150\n"
    "v_mul_f32 v151, s54, v151\n"
    "v_mfma_f32_16x16x32_bf16 v[40:43], a[168:171], v[164:167], 0\n"
    "s_waitcnt lgkmcnt(0)\n"
    "buffer_store_dwordx4 v[232:235], v184, s[48:51], s68 offen offset:0 sc1\n"
    "v_exp_f32 v151, v151\n"
    "v_cvt_pk_bf16_f32 v170, v148, v149\n"
    "v_mfma_f32_16x16x32_bf16 v[44:47], a[172:175], v[164:167], 0\n"
    "v_cvt_pk_bf16_f32 v171, v150, v151\n"
    "v_max3_f32 v204, v152, v153, v154\n"
    "v_max3_f32 v205, v155, v156, v157\n"
    "v_mfma_f32_16x16x32_bf16 v[48:51], a[176:179], v[164:167], 0\n"
    "buffer_store_dwordx4 v[236:239], v184, s[48:51], s68 offen offset:1024 sc1\n"
    "v_max3_f32 v204, v204, v158, v159\n"
    "v_max_f32 v204, v204, v205\n"
    "v_mov_b32 v205, v204\n"
    "v_mfma_f32_16x16x32_bf16 v[52:55], a[180:183], v[164:167], 0\n"
    "ds_read_b64_tr_b16 a[192:193], v178 offset:8192\n"
    "v_permlane32_swap_b32 v204, v205\n"
    "v_max_f32 v204, v204, v205\n"
    "v_mfma_f32_16x16x32_bf16 v[56:59], a[184:187], v[164:167], 0\n"
    "buffer_store_dwordx4 v[240:243], v184, s[48:51], s68 offen offset:2048 sc1\n"
    "v_mov_b32 v205, v204\n"
    "v_mfma_f32_16x16x32_bf16 v[60:63], a[188:191], v[164:167], 0\n"
    "ds_read_b64_tr_b16 a[194:195], v178 offset:12288\n"
    "v_permlane16_swap_b32 v204, v205\n"
    "v_max_f32 v204, v204, v205\n"
    "v_mfma_f32_16x16x32_bf16 a[228:231], a[240:243], v[164:167], 0\n"
    "buffer_store_dwordx4 v[244:247], v184, s[48:51], s68 offen offset:3072 sc1\n"
    "v_mul_f32 v212, -1.0, v204\n"
    "v_mul_f32 v213, -1.0, v204\n"
    "s_cmp_lt_u32 s57, s79\n"
    "s_cbranch_scc1 Lmha_LOOP\n"
    "Lmha_LOOPB:\n"
    "v_mfma_f32_16x16x32_bf16 v[128:131], a[128:131], a[0:3], v[248:251]\n"
    "v_mul_f32 v214, -1.0, v204\n"
    "v_mul_f32 v215, -1.0, v204\n"
    "v_sub_f32 v152, v152, v204\n"
    "v_sub_f32 v153, v153, v204\n"
    "v_mfma_f32_16x16x32_bf16 v[128:131], a[132:135], a[4:7], v[128:131]\n"
    "v_sub_f32 v154, v154, v204\n"
    "v_sub_f32 v155, v155, v204\n"
    "v_sub_f32 v156, v156, v204\n"
    "v_mfma_f32_16x16x32_bf16 v[128:131], a[136:139], a[8:11], v[128:131]\n"
    "ds_read_b64_tr_b16 a[196:197], v178 offset:8448\n"
    "v_sub_f32 v157, v157, v204\n"
    "v_sub_f32 v158, v158, v204\n"
    "v_sub_f32 v159, v159, v204\n"
    "v_mfma_f32_16x16x32_bf16 v[128:131], a[140:143], a[12:15], v[128:131]\n"
    "v_mul_f32 v152, s54, v152\n"
    "v_exp_f32 v152, v152\n"
    "v_mfma_f32_16x16x32_bf16 v[132:135], a[144:147], a[0:3], v[248:251]\n"
    "v_mul_f32 v153, s54, v153\n"
    "v_exp_f32 v153, v153\n"
    "v_mul_f32 v154, s54, v154\n"
    "v_mfma_f32_16x16x32_bf16 v[132:135], a[148:151], a[4:7], v[132:135]\n"
    "v_exp_f32 v154, v154\n"
    "v_mul_f32 v155, s54, v155\n"
    "v_mfma_f32_16x16x32_bf16 v[132:135], a[152:155], a[8:11], v[132:135]\n"
    "ds_read_b64_tr_b16 a[198:199], v178 offset:12544\n"
    "v_exp_f32 v155, v155\n"
    "v_mfma_f32_16x16x32_bf16 v[132:135], a[156:159], a[12:15], v[132:135]\n"
    "v_cvt_pk_bf16_f32 v172, v152, v153\n"
    "v_mul_f32 v156, s54, v156\n"
    "v_exp_f32 v156, v156\n"
    "v_mfma_f32_16x16x32_bf16 v[136:139], a[128:131], a[16:19], v[252:255]\n"
    "v_mul_f32 v157, s54, v157\n"
    "v_exp_f32 v157, v157\n"
    "v_mfma_f32_16x16x32_bf16 v[136:139], a[132:135], a[20:23], v[136:139]\n"
    "v_cvt_pk_bf16_f32 v173, v154, v155\n"
    "v_mul_f32 v158, s54, v158\n"
    "v_exp_f32 v158, v158\n"
    "v_mfma_f32_16x16x32_bf16 v[136:139], a[136:139], a[24:27], v[136:139]\n"
    "v_mul_f32 v159, s54, v159\n"
    "v_exp_f32 v159, v159\n"
    "v_cvt_pk_bf16_f32 v174, v156, v157\n"
    "v_mfma_f32_16x16x32_bf16 v[136:139], a[140:143], a[28:31], v[136:139]\n"
    "v_mul_f32 v128, s54, v128\n"
    "v_mul_f32 v129, s54, v129\n"
    "v_cvt_pk_bf16_f32 v175, v158, v159\n"
    "v_mfma_f32_16x16x32_bf16 v[140:143], a[144:147], a[16:19], v[252:255]\n"
    "ds_read_b64_tr_b16 a[200:201], v178 offset:8704\n"
    "v_mul_f32 v130, s54, v130\n"
    "v_mfma_f32_16x16x32_bf16 v[140:143], a[148:151], a[20:23], v[140:143]\n"
    "v_exp_f32 v128, v128\n"
    "v_mfma_f32_16x16x32_bf16 v[140:143], a[152:155], a[24:27], v[140:143]\n"
    "ds_read_b64_tr_b16 a[202:203], v178 offset:12800\n"
    "v_mul_f32 v131, s54, v131\n"
    "v_mfma_f32_16x16x32_bf16 v[140:143], a[156:159], a[28:31], v[140:143]\n"
    "v_exp_f32 v129, v129\n"
    "v_mul_f32 v132, s54, v132\n"
    "v_mfma_f32_16x16x32_bf16 v[64:67], a[160:163], v[168:171], 0\n"
    "ds_read_b64_tr_b16 a[204:205], v178 offset:8960\n"
    "v_cvt_pk_bf16_f32 v160, v128, v129\n"
    "v_mfma_f32_16x16x32_bf16 v[68:71], a[164:167], v[168:171], 0\n"
    "v_exp_f32 v130, v130\n"
    "v_mfma_f32_16x16x32_bf16 v[72:75], a[168:171], v[168:171], 0\n"
    "ds_read_b64_tr_b16 a[206:207], v178 offset:13056\n"
    "v_mul_f32 v133, s54, v133\n"
    "v_mfma_f32_16x16x32_bf16 v[76:79], a[172:175], v[168:171], 0\n"
    "v_exp_f32 v131, v131\n"
    "v_mfma_f32_16x16x32_bf16 v[80:83], a[176:179], v[168:171], 0\n"
    "ds_read_b64_tr_b16 a[208:209], v178 offset:9216\n"
    "v_cvt_pk_bf16_f32 v161, v130, v131\n"
    "v_mfma_f32_16x16x32_bf16 v[84:87], a[180:183], v[168:171], 0\n"
    "v_exp_f32 v132, v132\n"
    "v_mfma_f32_16x16x32_bf16 v[88:91], a[184:187], v[168:171], 0\n"
    "ds_read_b64_tr_b16 a[210:211], v178 offset:13312\n"
    "v_mul_f32 v134, s54, v134\n"
    "v_mfma_f32_16x16x32_bf16 v[92:95], a[188:191], v[168:171], 0\n"
    "v_exp_f32 v133, v133\n"
    "v_mul_f32 v135, s54, v135\n"
    "v_mfma_f32_16x16x32_bf16 a[232:235], a[240:243], v[168:171], 0\n"
    "ds_read_b64_tr_b16 a[212:213], v178 offset:9472\n"
    "v_cvt_pk_bf16_f32 v162, v132, v133\n"
    "v_mfma_f32_16x16x32_bf16 v[96:99], a[160:163], v[172:175], 0\n"
    "v_exp_f32 v134, v134\n"
    "v_mfma_f32_16x16x32_bf16 v[100:103], a[164:167], v[172:175], 0\n"
    "ds_read_b64_tr_b16 a[214:215], v178 offset:13568\n"
    "v_mfma_f32_16x16x32_bf16 v[104:107], a[168:171], v[172:175], 0\n"
    "v_exp_f32 v135, v135\n"
    "v_mfma_f32_16x16x32_bf16 v[108:111], a[172:175], v[172:175], 0\n"
    "v_cvt_pk_bf16_f32 v163, v134, v135\n"
    "v_mul_f32 v136, s54, v136\n"
    "v_mfma_f32_16x16x32_bf16 v[112:115], a[176:179], v[172:175], 0\n"
    "v_exp_f32 v136, v136\n"
    "v_mfma_f32_16x16x32_bf16 v[116:119], a[180:183], v[172:175], 0\n"
    "ds_read_b64_tr_b16 a[216:217], v178 offset:9728\n"
    "v_mul_f32 v137, s54, v137\n"
    "v_mfma_f32_16x16x32_bf16 v[120:123], a[184:187], v[172:175], 0\n"
    "v_exp_f32 v137, v137\n"
    "v_mfma_f32_16x16x32_bf16 v[124:127], a[188:191], v[172:175], 0\n"
    "v_mul_f32 v138, s54, v138\n"
    "v_mul_f32 v139, s54, v139\n"
    "v_mfma_f32_16x16x32_bf16 a[236:239], a[240:243], v[172:175], 0\n"
    "v_exp_f32 v138, v138\n"
    "v_mfma_f32_16x16x32_bf16 v[144:147], a[128:131], a[32:35], v[196:199]\n"
    "ds_read_b64_tr_b16 a[218:219], v178 offset:13824\n"
    "v_cvt_pk_bf16_f32 v164, v136, v137\n"
    "v_mfma_f32_16x16x32_bf16 v[144:147], a[132:135], a[36:39], v[144:147]\n"
    "v_exp_f32 v139, v139\n"
    "v_mfma_f32_16x16x32_bf16 v[144:147], a[136:139], a[40:43], v[144:147]\n"
    "ds_read_b64_tr_b16 a[220:221], v178 offset:9984\n"
    "v_mul_f32 v140, s54, v140\n"
    "v_mfma_f32_16x16x32_bf16 v[144:147], a[140:143], a[44:47], v[144:147]\n"
    "v_exp_f32 v140, v140\n"
    "v_mfma_f32_16x16x32_bf16 v[148:151], a[144:147], a[32:35], v[196:199]\n"
    "ds_read_b64_tr_b16 a[222:223], v178 offset:14080\n"
    "v_mul_f32 v141, s54, v141\n"
    "v_mfma_f32_16x16x32_bf16 v[148:151], a[148:151], a[36:39], v[148:151]\n"
    "v_exp_f32 v141, v141\n"
    "v_mfma_f32_16x16x32_bf16 v[148:151], a[152:155], a[40:43], v[148:151]\n"
    "v_cvt_pk_bf16_f32 v165, v138, v139\n"
    "v_mul_f32 v142, s54, v142\n"
    "v_mfma_f32_16x16x32_bf16 v[148:151], a[156:159], a[44:47], v[148:151]\n"
    "v_exp_f32 v142, v142\n"
    "v_mfma_f32_16x16x32_bf16 v[152:155], a[128:131], a[48:51], v[212:215]\n"
    "s_waitcnt vmcnt(24) lgkmcnt(0)\n"
    "s_barrier\n"
    "v_mul_f32 v143, s54, v143\n"
    "v_cvt_pk_bf16_f32 v166, v140, v141\n"
    "v_mfma_f32_16x16x32_bf16 v[152:155], a[132:135], a[52:55], v[152:155]\n"
    "v_exp_f32 v143, v143\n"
    "v_mfma_f32_16x16x32_bf16 v[152:155], a[136:139], a[56:59], v[152:155]\n"
    "ds_read_b128 a[128:131], v176 offset:32768\n"
    "v_cvt_pk_bf16_f32 v167, v142, v143\n"
    "v_mfma_f32_16x16x32_bf16 v[152:155], a[140:143], a[60:63], v[152:155]\n"
    "v_mul_f32 v144, s54, v144\n"
    "v_mul_f32 v145, s54, v145\n"
    "v_mfma_f32_16x16x32_bf16 v[156:159], a[144:147], a[48:51], v[212:215]\n"
    "ds_read_b128 a[132:135], v176 offset:33280\n"
    "v_mul_f32 v146, s54, v146\n"
    "v_mfma_f32_16x16x32_bf16 v[156:159], a[148:151], a[52:55], v[156:159]\n"
    "v_exp_f32 v144, v144\n"
    "v_mfma_f32_16x16x32_bf16 v[156:159], a[152:155], a[56:59], v[156:159]\n"
    "ds_read_b128 a[136:139], v176 offset:33792\n"
    "v_mul_f32 v147, s54, v147\n"
    "v_mfma_f32_16x16x32_bf16 v[156:159], a[156:159], a[60:63], v[156:159]\n"
    "v_exp_f32 v145, v145\n"
    "v_mul_f32 v148, s54, v148\n"
    "v_mfma_f32_16x16x32_bf16 v[0:3], a[192:195], v[160:163], v[0:3]\n"
    "ds_read_b128 a[140:143], v176 offset:34304\n"
    "v_cvt_pk_bf16_f32 v168, v144, v145\n"
    "v_mfma_f32_16x16x32_bf16 v[4:7], a[196:199], v[160:163], v[4:7]\n"
    "v_exp_f32 v146, v146\n"
    "v_mfma_f32_16x16x32_bf16 v[8:11], a[200:203], v[160:163], v[8:11]\n"
    "ds_read_b128 a[144:147], v176 offset:36864\n"
    "v_mul_f32 v149, s54, v149\n"
    "v_mfma_f32_16x16x32_bf16 v[12:15], a[204:207], v[160:163], v[12:15]\n"
    "v_exp_f32 v147, v147\n"
    "v_mfma_f32_16x16x32_bf16 v[16:19], a[208:211], v[160:163], v[16:19]\n"
    "s_add_u32 m0, s52, 0x18000\n"
    "ds_read_b128 a[148:151], v176 offset:37376\n"
    "v_cvt_pk_bf16_f32 v169, v146, v147\n"
    "v_mfma_f32_16x16x32_bf16 v[20:23], a[212:215], v[160:163], v[20:23]\n"
    "buffer_load_dwordx4 v181, s[36:39], s72 offen offset:0 nt lds\n"
    "v_mul_f32 v150, s54, v150\n"
    "v_mfma_f32_16x16x32_bf16 v[24:27], a[216:219], v[160:163], v[24:27]\n"
    "ds_read_b128 a[152:155], v176 offset:37888\n"
    "v_mul_f32 v151, s54, v151\n"
    "v_mfma_f32_16x16x32_bf16 v[28:31], a[220:223], v[160:163], v[28:31]\n"
    "v_exp_f32 v148, v148\n"
    "v_mfma_f32_16x16x32_bf16 a[224:227], a[240:243], v[160:163], a[224:227]\n"
    "ds_read_b128 a[156:159], v176 offset:38400\n"
    "v_mfma_f32_16x16x32_bf16 v[32:35], a[192:195], v[164:167], v[32:35]\n"
    "v_exp_f32 v149, v149\n"
    "v_mfma_f32_16x16x32_bf16 v[36:39], a[196:199], v[164:167], v[36:39]\n"
    "v_exp_f32 v150, v150\n"
    "v_mfma_f32_16x16x32_bf16 v[40:43], a[200:203], v[164:167], v[40:43]\n"
    "v_exp_f32 v151, v151\n"
    "v_cvt_pk_bf16_f32 v170, v148, v149\n"
    "v_mfma_f32_16x16x32_bf16 v[44:47], a[204:207], v[164:167], v[44:47]\n"
    "v_cvt_pk_bf16_f32 v171, v150, v151\n"
    "v_mul_f32 v152, s54, v152\n"
    "v_mfma_f32_16x16x32_bf16 v[48:51], a[208:211], v[164:167], v[48:51]\n"
    "v_exp_f32 v152, v152\n"
    "v_mfma_f32_16x16x32_bf16 v[52:55], a[212:215], v[164:167], v[52:55]\n"
    "ds_read_b64_tr_b16 a[160:161], v178 offset:32768\n"
    "v_mul_f32 v153, s54, v153\n"
    "v_mfma_f32_16x16x32_bf16 v[56:59], a[216:219], v[164:167], v[56:59]\n"
    "buffer_load_dwordx4 v182, s[36:39], s72 offen offset:2048 nt lds\n"
    "v_mul_f32 v154, s54, v154\n"
    "v_mfma_f32_16x16x32_bf16 v[60:63], a[220:223], v[164:167], v[60:63]\n"
    "s_waitcnt lgkmcnt(1)\n"
    "ds_read_b64_tr_b16 a[162:163], v178 offset:36864\n"
    "v_mul_f32 v155, s54, v155\n"
    "v_mfma_f32_16x16x32_bf16 a[228:231], a[240:243], v[164:167], a[228:231]\n"
    "v_exp_f32 v153, v153\n"
    "v_mfma_f32_16x16x32_bf16 v[128:131], a[128:131], a[0:3], v[248:251]\n"
    "v_exp_f32 v154, v154\n"
    "v_mfma_f32_16x16x32_bf16 v[128:131], a[132:135], a[4:7], v[128:131]\n"
    "v_exp_f32 v155, v155\n"
    "v_mfma_f32_16x16x32_bf16 v[128:131], a[136:139], a[8:11], v[128:131]\n"
    "ds_read_b64_tr_b16 a[164:165], v178 offset:33024\n"
    "v_cvt_pk_bf16_f32 v172, v152, v153\n"
    "v_mul_f32 v156, s54, v156\n"
    "v_mfma_f32_16x16x32_bf16 v[128:131], a[140:143], a[12:15], v[128:131]\n"
    "v_exp_f32 v156, v156\n"
    "v_mfma_f32_16x16x32_bf16 v[132:135], a[144:147], a[0:3], v[248:251]\n"
    "v_mul_f32 v157, s54, v157\n"
    "v_cvt_pk_bf16_f32 v173, v154, v155\n"
    "v_mfma_f32_16x16x32_bf16 v[132:135], a[148:151], a[4:7], v[132:135]\n"
    "v_exp_f32 v157, v157\n"
    "v_mfma_f32_16x16x32_bf16 v[132:135], a[152:155], a[8:11], v[132:135]\n"
    "ds_read_b64_tr_b16 a[166:167], v178 offset:37120\n"
    "v_mul_f32 v158, s54, v158\n"
    "v_mfma_f32_16x16x32_bf16 v[132:135], a[156:159], a[12:15], v[132:135]\n"
    "v_exp_f32 v158, v158\n"
    "v_mfma_f32_16x16x32_bf16 v[136:139], a[128:131], a[16:19], v[252:255]\n"
    "s_add_u32 m0, s52, 0x18380\n"
    "v_mul_f32 v159, s54, v159\n"
    "v_cvt_pk_bf16_f32 v174, v156, v157\n"
    "v_mfma_f32_16x16x32_bf16 v[136:139], a[132:135], a[20:23], v[136:139]\n"
    "buffer_load_dwordx4 v181, s[36:39], s72 offen offset:128 nt lds\n"
    "v_mfma_f32_16x16x32_bf16 v[136:139], a[136:139], a[24:27], v[136:139]\n"
    "v_exp_f32 v159, v159\n"
    "v_mfma_f32_16x16x32_bf16 v[136:139], a[140:143], a[28:31], v[136:139]\n"
    "v_cvt_pk_bf16_f32 v175, v158, v159\n"
    "v_mul_f32 v128, s54, v128\n"
    "v_mfma_f32_16x16x32_bf16 v[140:143], a[144:147], a[16:19], v[252:255]\n"
    "ds_read_b64_tr_b16 a[168:169], v178 offset:33280\n"
    "v_mul_f32 v129, s54, v129\n"
    "v_mfma_f32_16x16x32_bf16 v[140:143], a[148:151], a[20:23], v[140:143]\n"
    "v_exp_f32 v128, v128\n"
    "v_mul_f32 v130, s54, v130\n"
    "v_mfma_f32_16x16x32_bf16 v[140:143], a[152:155], a[24:27], v[140:143]\n"
    "ds_read_b64_tr_b16 a[170:171], v178 offset:37376\n"
    "v_mul_f32 v131, s54, v131\n"
    "v_mfma_f32_16x16x32_bf16 v[140:143], a[156:159], a[28:31], v[140:143]\n"
    "v_exp_f32 v129, v129\n"
    "v_mfma_f32_16x16x32_bf16 v[64:67], a[192:195], v[168:171], v[64:67]\n"
    "ds_read_b64_tr_b16 a[172:173], v178 offset:33536\n"
    "v_cvt_pk_bf16_f32 v160, v128, v129\n"
    "v_mfma_f32_16x16x32_bf16 v[68:71], a[196:199], v[168:171], v[68:71]\n"
    "v_exp_f32 v130, v130\n"
    "v_mul_f32 v132, s54, v132\n"
    "v_mfma_f32_16x16x32_bf16 v[72:75], a[200:203], v[168:171], v[72:75]\n"
    "ds_read_b64_tr_b16 a[174:175], v178 offset:37632\n"
    "v_mul_f32 v133, s54, v133\n"
    "v_mfma_f32_16x16x32_bf16 v[76:79], a[204:207], v[168:171], v[76:79]\n"
    "buffer_load_dwordx4 v182, s[36:39], s72 offen offset:2176 nt lds\n"
    "v_mul_f32 v134, s54, v134\n"
    "v_mfma_f32_16x16x32_bf16 v[80:83], a[208:211], v[168:171], v[80:83]\n"
    "ds_read_b64_tr_b16 a[176:177], v178 offset:33792\n"
    "v_mul_f32 v135, s54, v135\n"
    "v_mfma_f32_16x16x32_bf16 v[84:87], a[212:215], v[168:171], v[84:87]\n"
    "v_exp_f32 v131, v131\n"
    "v_mfma_f32_16x16x32_bf16 v[88:91], a[216:219], v[168:171], v[88:91]\n"
    "ds_read_b64_tr_b16 a[178:179], v178 offset:37888\n"
    "v_cvt_pk_bf16_f32 v161, v130, v131\n"
    "v_mfma_f32_16x16x32_bf16 v[92:95], a[220:223], v[168:171], v[92:95]\n"
    "v_exp_f32 v132, v132\n"
    "v_mfma_f32_16x16x32_bf16 a[232:235], a[240:243], v[168:171], a[232:235]\n"
    "ds_read_b64_tr_b16 a[180:181], v178 offset:34048\n"
    "v_mfma_f32_16x16x32_bf16 v[96:99], a[192:195], v[172:175], v[96:99]\n"
    "v_exp_f32 v133, v133\n"
    "v_mfma_f32_16x16x32_bf16 v[100:103], a[196:199], v[172:175], v[100:103]\n"
    "ds_read_b64_tr_b16 a[182:183], v178 offset:38144\n"
    "v_cvt_pk_bf16_f32 v162, v132, v133\n"
    "v_mfma_f32_16x16x32_bf16 v[104:107], a[200:203], v[172:175], v[104:107]\n"
    "v_exp_f32 v134, v134\n"
    "v_exp_f32 v135, v135\n"
    "v_mfma_f32_16x16x32_bf16 v[108:111], a[204:207], v[172:175], v[108:111]\n"
    "v_cvt_pk_bf16_f32 v163, v134, v135\n"
    "v_mul_f32 v136, s54, v136\n"
    "v_mfma_f32_16x16x32_bf16 v[112:115], a[208:211], v[172:175], v[112:115]\n"
    "v_exp_f32 v136, v136\n"
    "v_mfma_f32_16x16x32_bf16 v[116:119], a[212:215], v[172:175], v[116:119]\n"
    "s_add_u32 m0, s52, 0x1c000\n"
    "ds_read_b64_tr_b16 a[184:185], v178 offset:34304\n"
    "v_mul_f32 v137, s54, v137\n"
    "v_mfma_f32_16x16x32_bf16 v[120:123], a[216:219], v[172:175], v[120:123]\n"
    "buffer_load_dwordx4 v183, s[40:43], s72 offen offset:0 nt lds\n"
    "v_mul_f32 v138, s54, v138\n"
    "v_mfma_f32_16x16x32_bf16 v[124:127], a[220:223], v[172:175], v[124:127]\n"
    "v_exp_f32 v137, v137\n"
    "v_mul_f32 v139, s54, v139\n"
    "v_mfma_f32_16x16x32_bf16 a[236:239], a[240:243], v[172:175], a[236:239]\n"
    "v_exp_f32 v138, v138\n"
    "v_mfma_f32_16x16x32_bf16 v[144:147], a[128:131], a[32:35], v[196:199]\n"
    "ds_read_b64_tr_b16 a[186:187], v178 offset:38400\n"
    "v_cvt_pk_bf16_f32 v164, v136, v137\n"
    "v_mfma_f32_16x16x32_bf16 v[144:147], a[132:135], a[36:39], v[144:147]\n"
    "v_exp_f32 v139, v139\n"
    "v_mfma_f32_16x16x32_bf16 v[144:147], a[136:139], a[40:43], v[144:147]\n"
    "ds_read_b64_tr_b16 a[188:189], v178 offset:34560\n"
    "v_mul_f32 v140, s54, v140\n"
    "v_mfma_f32_16x16x32_bf16 v[144:147], a[140:143], a[44:47], v[144:147]\n"
    "v_exp_f32 v140, v140\n"
    "v_mfma_f32_16x16x32_bf16 v[148:151], a[144:147], a[32:35], v[196:199]\n"
    "ds_read_b64_tr_b16 a[190:191], v178 offset:38656\n"
    "v_mul_f32 v141, s54, v141\n"
    "v_mfma_f32_16x16x32_bf16 v[148:151], a[148:151], a[36:39], v[148:151]\n"
    "v_exp_f32 v141, v141\n"
    "v_mfma_f32_16x16x32_bf16 v[148:151], a[152:155], a[40:43], v[148:151]\n"
    "v_cvt_pk_bf16_f32 v165, v138, v139\n"
    "v_mul_f32 v142, s54, v142\n"
    "v_mul_f32 v143, s54, v143\n"
    "v_mfma_f32_16x16x32_bf16 v[148:151], a[156:159], a[44:47], v[148:151]\n"
    "buffer_load_dwordx4 v183, s[40:43], s72 offen offset:2048 nt lds\n"
    "v_cvt_pk_bf16_f32 v166, v140, v141\n"
    "v_mfma_f32_16x16x32_bf16 v[152:155], a[128:131], a[48:51], v[212:215]\n"
    "s_waitcnt lgkmcnt(0)\n"
    "v_exp_f32 v142, v142\n"
    "v_mfma_f32_16x16x32_bf16 v[152:155], a[132:135], a[52:55], v[152:155]\n"
    "v_exp_f32 v143, v143\n"
    "v_mfma_f32_16x16x32_bf16 v[152:155], a[136:139], a[56:59], v[152:155]\n"
    "ds_read_b128 a[128:131], v176 offset:40960\n"
    "v_cvt_pk_bf16_f32 v167, v142, v143\n"
    "v_mfma_f32_16x16x32_bf16 v[152:155], a[140:143], a[60:63], v[152:155]\n"
    "v_mul_f32 v144, s54, v144\n"
    "v_mul_f32 v145, s54, v145\n"
    "v_mfma_f32_16x16x32_bf16 v[156:159], a[144:147], a[48:51], v[212:215]\n"
    "ds_read_b128 a[132:135], v176 offset:41472\n"
    "v_mul_f32 v146, s54, v146\n"
    "v_mfma_f32_16x16x32_bf16 v[156:159], a[148:151], a[52:55], v[156:159]\n"
    "v_exp_f32 v144, v144\n"
    "v_mfma_f32_16x16x32_bf16 v[156:159], a[152:155], a[56:59], v[156:159]\n"
    "ds_read_b128 a[136:139], v176 offset:41984\n"
    "v_mul_f32 v147, s54, v147\n"
    "v_mfma_f32_16x16x32_bf16 v[156:159], a[156:159], a[60:63], v[156:159]\n"
    "v_exp_f32 v145, v145\n"
    "v_mul_f32 v148, s54, v148\n"
    "v_mfma_f32_16x16x32_bf16 v[0:3], a[160:163], v[160:163], v[0:3]\n"
    "ds_read_b128 a[140:143], v176 offset:42496\n"
    "v_cvt_pk_bf16_f32 v168, v144, v145\n"
    "v_mfma_f32_16x16x32_bf16 v[4:7], a[164:167], v[160:163], v[4:7]\n"
    "v_exp_f32 v146, v146\n"
    "v_mfma_f32_16x16x32_bf16 v[8:11], a[168:171], v[160:163], v[8:11]\n"
    "s_add_u32 m0, s52, 0x1c380\n"
    "ds_read_b128 a[144:147], v176 offset:45056\n"
    "v_mul_f32 v149, s54, v149\n"
    "v_mfma_f32_16x16x32_bf16 v[12:15], a[172:175], v[160:163], v[12:15]\n"
    "buffer_load_dwordx4 v183, s[40:43], s72 offen offset:128 nt lds\n"
    "v_mul_f32 v150, s54, v150\n"
    "v_mfma_f32_16x16x32_bf16 v[16:19], a[176:179], v[160:163], v[16:19]\n"
    "ds_read_b128 a[148:151], v176 offset:45568\n"
    "v_mul_f32 v151, s54, v151\n"
    "v_mfma_f32_16x16x32_bf16 v[20:23], a[180:183], v[160:163], v[20:23]\n"
    "v_exp_f32 v147, v147\n"
    "v_mfma_f32_16x16x32_bf16 v[24:27], a[184:187], v[160:163], v[24:27]\n"
    "ds_read_b128 a[152:155], v176 offset:46080\n"
    "v_cvt_pk_bf16_f32 v169, v146, v147\n"
    "v_mfma_f32_16x16x32_bf16 v[28:31], a[188:191], v[160:163], v[28:31]\n"
    "v_exp_f32 v148, v148\n"
    "v_mfma_f32_16x16x32_bf16 a[224:227], a[240:243], v[160:163], a[224:227]\n"
    "ds_read_b128 a[156:159], v176 offset:46592\n"
    "v_mfma_f32_16x16x32_bf16 v[32:35], a[160:163], v[164:167], v[32:35]\n"
    "v_exp_f32 v149, v149\n"
    "v_mfma_f32_16x16x32_bf16 v[36:39], a[164:167], v[164:167], v[36:39]\n"
    "v_exp_f32 v150, v150\n"
    "v_mfma_f32_16x16x32_bf16 v[40:43], a[168:171], v[164:167], v[40:43]\n"
    "v_exp_f32 v151, v151\n"
    "v_cvt_pk_bf16_f32 v170, v148, v149\n"
    "v_mfma_f32_16x16x32_bf16 v[44:47], a[172:175], v[164:167], v[44:47]\n"
    "v_cvt_pk_bf16_f32 v171, v150, v151\n"
    "v_mul_f32 v152, s54, v152\n"
    "v_mfma_f32_16x16x32_bf16 v[48:51], a[176:179], v[164:167], v[48:51]\n"
    "buffer_load_dwordx4 v183, s[40:43], s72 offen offset:2176 nt lds\n"
    "v_mul_f32 v153, s54, v153\n"
    "v_mfma_f32_16x16x32_bf16 v[52:55], a[180:183], v[164:167], v[52:55]\n"
    "ds_read_b64_tr_b16 a[192:193], v178 offset:40960\n"
    "v_exp_f32 v152, v152\n"
    "v_mfma_f32_16x16x32_bf16 v[56:59], a[184:187], v[164:167], v[56:59]\n"
    "v_exp_f32 v153, v153\n"
    "v_mfma_f32_16x16x32_bf16 v[60:63], a[188:191], v[164:167], v[60:63]\n"
    "s_waitcnt lgkmcnt(1)\n"
    "ds_read_b64_tr_b16 a[194:195], v178 offset:45056\n"
    "v_mul_f32 v154, s54, v154\n"
    "v_mfma_f32_16x16x32_bf16 a[228:231], a[240:243], v[164:167], a[228:231]\n"
    "v_exp_f32 v154, v154\n"
    "v_mfma_f32_16x16x32_bf16 v[128:131], a[128:131], a[0:3], v[248:251]\n"
    "v_mul_f32 v155, s54, v155\n"
    "v_exp_f32 v155, v155\n"
    "v_mfma_f32_16x16x32_bf16 v[128:131], a[132:135], a[4:7], v[128:131]\n"
    "v_cvt_pk_bf16_f32 v172, v152, v153\n"
    "v_mul_f32 v156, s54, v156\n"
    "v_mfma_f32_16x16x32_bf16 v[128:131], a[136:139], a[8:11], v[128:131]\n"
    "ds_read_b64_tr_b16 a[196:197], v178 offset:41216\n"
    "v_mul_f32 v157, s54, v157\n"
    "v_mfma_f32_16x16x32_bf16 v[128:131], a[140:143], a[12:15], v[128:131]\n"
    "s_add_u32 s44, s44, 0x1000000\n"
    "s_addc_u32 s45, s45, 0\n"
    "s_cmp_lt_u32 s57, 3\n"
    "s_cselect_b32 s46, 0x10000, 0\n"
    "v_mfma_f32_16x16x32_bf16 v[132:135], a[144:147], a[0:3], v[248:251]\n"
    "v_exp_f32 v156, v156\n"
    "v_cvt_pk_bf16_f32 v173, v154, v155\n"
    "v_mfma_f32_16x16x32_bf16 v[132:135], a[148:151], a[4:7], v[132:135]\n"
    "v_exp_f32 v157, v157\n"
    "v_mfma_f32_16x16x32_bf16 v[132:135], a[152:155], a[8:11], v[132:135]\n"
    "s_add_u32 s58, s53, 0x0\n"
    "s_add_u32 m0, s52, 0x20000\n"
    "ds_read_b64_tr_b16 a[198:199], v178 offset:45312\n"
    "v_mfma_f32_16x16x32_bf16 v[132:135], a[156:159], a[12:15], v[132:135]\n"
    "buffer_load_dwordx4 v181, s[44:47], s58 offen offset:0 nt lds\n"
    "v_mul_f32 v158, s54, v158\n"
    "v_mfma_f32_16x16x32_bf16 v[136:139], a[128:131], a[16:19], v[252:255]\n"
    "v_exp_f32 v158, v158\n"
    "v_mul_f32 v159, s54, v159\n"
    "v_mfma_f32_16x16x32_bf16 v[136:139], a[132:135], a[20:23], v[136:139]\n"
    "v_exp_f32 v159, v159\n"
    "v_mfma_f32_16x16x32_bf16 v[136:139], a[136:139], a[24:27], v[136:139]\n"
    "v_cvt_pk_bf16_f32 v174, v156, v157\n"
    "v_cvt_pk_bf16_f32 v175, v158, v159\n"
    "v_mfma_f32_16x16x32_bf16 v[136:139], a[140:143], a[28:31], v[136:139]\n"
    "v_mul_f32 v128, s54, v128\n"
    "v_mul_f32 v129, s54, v129\n"
    "v_mfma_f32_16x16x32_bf16 v[140:143], a[144:147], a[16:19], v[252:255]\n"
    "ds_read_b64_tr_b16 a[200:201], v178 offset:41472\n"
    "v_mul_f32 v130, s54, v130\n"
    "v_mfma_f32_16x16x32_bf16 v[140:143], a[148:151], a[20:23], v[140:143]\n"
    "v_exp_f32 v128, v128\n"
    "v_mul_f32 v131, s54, v131\n"
    "v_mfma_f32_16x16x32_bf16 v[140:143], a[152:155], a[24:27], v[140:143]\n"
    "ds_read_b64_tr_b16 a[202:203], v178 offset:45568\n"
    "v_mul_f32 v132, s54, v132\n"
    "v_mfma_f32_16x16x32_bf16 v[140:143], a[156:159], a[28:31], v[140:143]\n"
    "v_exp_f32 v129, v129\n"
    "v_mfma_f32_16x16x32_bf16 v[64:67], a[160:163], v[168:171], v[64:67]\n"
    "s_add_u32 m0, s52, 0x1fc00\n"
    "ds_read_b64_tr_b16 a[204:205], v178 offset:41728\n"
    "v_cvt_pk_bf16_f32 v160, v128, v129\n"
    "v_mfma_f32_16x16x32_bf16 v[68:71], a[164:167], v[168:171], v[68:71]\n"
    "buffer_load_dwordx4 v182, s[44:47], s58 offen offset:2048 nt lds\n"
    "v_mul_f32 v133, s54, v133\n"
    "v_mfma_f32_16x16x32_bf16 v[72:75], a[168:171], v[168:171], v[72:75]\n"
    "ds_read_b64_tr_b16 a[206:207], v178 offset:45824\n"
    "v_mul_f32 v134, s54, v134\n"
    "v_mfma_f32_16x16x32_bf16 v[76:79], a[172:175], v[168:171], v[76:79]\n"
    "v_exp_f32 v130, v130\n"
    "v_mul_f32 v135, s54, v135\n"
    "v_mfma_f32_16x16x32_bf16 v[80:83], a[176:179], v[168:171], v[80:83]\n"
    "ds_read_b64_tr_b16 a[208:209], v178 offset:41984\n"
    "v_mfma_f32_16x16x32_bf16 v[84:87], a[180:183], v[168:171], v[84:87]\n"
    "v_exp_f32 v131, v131\n"
    "v_mfma_f32_16x16x32_bf16 v[88:91], a[184:187], v[168:171], v[88:91]\n"
    "ds_read_b64_tr_b16 a[210:211], v178 offset:46080\n"
    "v_cvt_pk_bf16_f32 v161, v130, v131\n"
    "v_mfma_f32_16x16x32_bf16 v[92:95], a[188:191], v[168:171], v[92:95]\n"
    "v_exp_f32 v132, v132\n"
    "v_mfma_f32_16x16x32_bf16 a[232:235], a[240:243], v[168:171], a[232:235]\n"
    "ds_read_b64_tr_b16 a[212:213], v178 offset:42240\n"
    "v_mfma_f32_16x16x32_bf16 v[96:99], a[160:163], v[172:175], v[96:99]\n"
    "v_exp_f32 v133, v133\n"
    "v_mfma_f32_16x16x32_bf16 v[100:103], a[164:167], v[172:175], v[100:103]\n"
    "ds_read_b64_tr_b16 a[214:215], v178 offset:46336\n"
    "v_cvt_pk_bf16_f32 v162, v132, v133\n"
    "v_mfma_f32_16x16x32_bf16 v[104:107], a[168:171], v[172:175], v[104:107]\n"
    "v_exp_f32 v134, v134\n"
    "v_exp_f32 v135, v135\n"
    "v_mfma_f32_16x16x32_bf16 v[108:111], a[172:175], v[172:175], v[108:111]\n"
    "s_add_u32 s58, s53, 0x1000\n"
    "s_add_u32 m0, s52, 0x20800\n"
    "v_cvt_pk_bf16_f32 v163, v134, v135\n"
    "v_mfma_f32_16x16x32_bf16 v[112:115], a[176:179], v[172:175], v[112:115]\n"
    "buffer_load_dwordx4 v181, s[44:47], s58 offen offset:0 nt lds\n"
    "v_mul_f32 v136, s54, v136\n"
    "v_mfma_f32_16x16x32_bf16 v[116:119], a[180:183], v[172:175], v[116:119]\n"
    "ds_read_b64_tr_b16 a[216:217], v178 offset:42496\n"
    "v_exp_f32 v136, v136\n"
    "v_mfma_f32_16x16x32_bf16 v[120:123], a[184:187], v[172:175], v[120:123]\n"
    "v_mul_f32 v137, s54, v137\n"
    "v_mul_f32 v138, s54, v138\n"
    "v_mfma_f32_16x16x32_bf16 v[124:127], a[188:191], v[172:175], v[124:127]\n"
    "v_exp_f32 v137, v137\n"
    "v_mfma_f32_16x16x32_bf16 a[236:239], a[240:243], v[172:175], a[236:239]\n"
    "v_exp_f32 v138, v138\n"
    "v_mfma_f32_16x16x32_bf16 v[144:147], a[128:131], a[32:35], v[196:199]\n"
    "ds_read_b64_tr_b16 a[218:219], v178 offset:46592\n"
    "v_mul_f32 v139, s54, v139\n"
    "v_mfma_f32_16x16x32_bf16 v[144:147], a[132:135], a[36:39], v[144:147]\n"
    "v_exp_f32 v139, v139\n"
    "v_mfma_f32_16x16x32_bf16 v[144:147], a[136:139], a[40:43], v[144:147]\n"
    "ds_read_b64_tr_b16 a[220:221], v178 offset:42752\n"
    "v_cvt_pk_bf16_f32 v164, v136, v137\n"
    "v_mul_f32 v140, s54, v140\n"
    "v_mfma_f32_16x16x32_bf16 v[144:147], a[140:143], a[44:47], v[144:147]\n"
    "v_exp_f32 v140, v140\n"
    "v_mfma_f32_16x16x32_bf16 v[148:151], a[144:147], a[32:35], v[196:199]\n"
    "s_add_u32 m0, s52, 0x20400\n"
    "ds_read_b64_tr_b16 a[222:223], v178 offset:46848\n"
    "v_mul_f32 v141, s54, v141\n"
    "v_mfma_f32_16x16x32_bf16 v[148:151], a[148:151], a[36:39], v[148:151]\n"
    "buffer_load_dwordx4 v182, s[44:47], s58 offen offset:2048 nt lds\n"
    "v_cvt_pk_bf16_f32 v165, v138, v139\n"
    "v_mfma_f32_16x16x32_bf16 v[148:151], a[152:155], a[40:43], v[148:151]\n"
    "v_exp_f32 v141, v141\n"
    "v_mfma_f32_16x16x32_bf16 v[148:151], a[156:159], a[44:47], v[148:151]\n"
    "v_mul_f32 v142, s54, v142\n"
    "v_mul_f32 v143, s54, v143\n"
    "v_mfma_f32_16x16x32_bf16 v[152:155], a[128:131], a[48:51], v[212:215]\n"
    "s_waitcnt vmcnt(28) lgkmcnt(0)\n"
    "s_barrier\n"
    "v_exp_f32 v142, v142\n"
    "v_cvt_pk_bf16_f32 v166, v140, v141\n"
    "v_mfma_f32_16x16x32_bf16 v[152:155], a[132:135], a[52:55], v[152:155]\n"
    "v_exp_f32 v143, v143\n"
    "v_mfma_f32_16x16x32_bf16 v[152:155], a[136:139], a[56:59], v[152:155]\n"
    "s_add_u32 m0, s52, 0x0\n"
    "ds_read_b128 a[128:131], v177 offset:0\n"
    "v_cvt_pk_bf16_f32 v167, v142, v143\n"
    "v_mfma_f32_16x16x32_bf16 v[152:155], a[140:143], a[60:63], v[152:155]\n"
    "buffer_load_dwordx4 v181, s[36:39], s73 offen offset:0 nt lds\n"
    "v_mul_f32 v144, s54, v144\n"
    "v_mfma_f32_16x16x32_bf16 v[156:159], a[144:147], a[48:51], v[212:215]\n"
    "ds_read_b128 a[132:135], v177 offset:512\n"
    "v_mul_f32 v145, s54, v145\n"
    "v_mfma_f32_16x16x32_bf16 v[156:159], a[148:151], a[52:55], v[156:159]\n"
    "v_exp_f32 v144, v144\n"
    "v_mul_f32 v146, s54, v146\n"
    "v_mfma_f32_16x16x32_bf16 v[156:159], a[152:155], a[56:59], v[156:159]\n"
    "ds_read_b128 a[136:139], v177 offset:1024\n"
    "v_mul_f32 v147, s54, v147\n"
    "v_mfma_f32_16x16x32_bf16 v[156:159], a[156:159], a[60:63], v[156:159]\n"
    "v_exp_f32 v145, v145\n"
    "v_mfma_f32_16x16x32_bf16 v[0:3], a[192:195], v[160:163], v[0:3]\n"
    "ds_read_b128 a[140:143], v177 offset:1536\n"
    "v_cvt_pk_bf16_f32 v168, v144, v145\n"
    "v_mfma_f32_16x16x32_bf16 v[4:7], a[196:199], v[160:163], v[4:7]\n"
    "v_exp_f32 v146, v146\n"
    "v_mfma_f32_16x16x32_bf16 v[8:11], a[200:203], v[160:163], v[8:11]\n"
    "ds_read_b128 a[144:147], v177 offset:4096\n"
    "v_mul_f32 v148, s54, v148\n"
    "v_mfma_f32_16x16x32_bf16 v[12:15], a[204:207], v[160:163], v[12:15]\n"
    "v_exp_f32 v147, v147\n"
    "v_mul_f32 v149, s54, v149\n"
    "v_mfma_f32_16x16x32_bf16 v[16:19], a[208:211], v[160:163], v[16:19]\n"
    "ds_read_b128 a[148:151], v177 offset:4608\n"
    "v_cvt_pk_bf16_f32 v169, v146, v147\n"
    "v_mfma_f32_16x16x32_bf16 v[20:23], a[212:215], v[160:163], v[20:23]\n"
    "v_exp_f32 v148, v148\n"
    "v_mfma_f32_16x16x32_bf16 v[24:27], a[216:219], v[160:163], v[24:27]\n"
    "ds_read_b128 a[152:155], v177 offset:5120\n"
    "v_mul_f32 v150, s54, v150\n"
    "v_mfma_f32_16x16x32_bf16 v[28:31], a[220:223], v[160:163], v[28:31]\n"
    "buffer_load_dwordx4 v182, s[36:39], s73 offen offset:2048 nt lds\n"
    "v_mul_f32 v151, s54, v151\n"
    "v_mfma_f32_16x16x32_bf16 a[224:227], a[240:243], v[160:163], a[224:227]\n"
    "ds_read_b128 a[156:159], v177 offset:5632\n"
    "v_mfma_f32_16x16x32_bf16 v[32:35], a[192:195], v[164:167], v[32:35]\n"
    "v_exp_f32 v149, v149\n"
    "v_mfma_f32_16x16x32_bf16 v[36:39], a[196:199], v[164:167], v[36:39]\n"
    "v_exp_f32 v150, v150\n"
    "v_mfma_f32_16x16x32_bf16 v[40:43], a[200:203], v[164:167], v[40:43]\n"
    "v_exp_f32 v151, v151\n"
    "v_cvt_pk_bf16_f32 v170, v148, v149\n"
    "v_mfma_f32_16x16x32_bf16 v[44:47], a[204:207], v[164:167], v[44:47]\n"
    "v_cvt_pk_bf16_f32 v171, v150, v151\n"
    "v_mul_f32 v152, s54, v152\n"
    "v_mfma_f32_16x16x32_bf16 v[48:51], a[208:211], v[164:167], v[48:51]\n"
    "v_exp_f32 v152, v152\n"
    "v_mfma_f32_16x16x32_bf16 v[52:55], a[212:215], v[164:167], v[52:55]\n"
    "ds_read_b64_tr_b16 a[160:161], v179 offset:0\n"
    "v_mul_f32 v153, s54, v153\n"
    "v_mfma_f32_16x16x32_bf16 v[56:59], a[216:219], v[164:167], v[56:59]\n"
    "v_exp_f32 v153, v153\n"
    "v_mfma_f32_16x16x32_bf16 v[60:63], a[220:223], v[164:167], v[60:63]\n"
    "s_waitcnt lgkmcnt(1)\n"
    "s_add_u32 m0, s52, 0x380\n"
    "ds_read_b64_tr_b16 a[162:163], v179 offset:4096\n"
    "v_mfma_f32_16x16x32_bf16 a[228:231], a[240:243], v[164:167], a[228:231]\n"
    "buffer_load_dwordx4 v181, s[36:39], s73 offen offset:128 nt lds\n"
    "v_mul_f32 v154, s54, v154\n"
    "v_mfma_f32_16x16x32_bf16 v[128:131], a[128:131], a[0:3], v[248:251]\n"
    "v_exp_f32 v154, v154\n"
    "v_mfma_f32_16x16x32_bf16 v[128:131], a[132:135], a[4:7], v[128:131]\n"
    "v_mul_f32 v155, s54, v155\n"
    "v_cvt_pk_bf16_f32 v172, v152, v153\n"
    "v_mfma_f32_16x16x32_bf16 v[128:131], a[136:139], a[8:11], v[128:131]\n"
    "ds_read_b64_tr_b16 a[164:165], v179 offset:256\n"
    "v_exp_f32 v155, v155\n"
    "v_mfma_f32_16x16x32_bf16 v[128:131], a[140:143], a[12:15], v[128:131]\n"
    "v_mul_f32 v156, s54, v156\n"
    "v_mul_f32 v157, s54, v157\n"
    "v_mfma_f32_16x16x32_bf16 v[132:135], a[144:147], a[0:3], v[248:251]\n"
    "v_exp_f32 v156, v156\n"
    "v_mfma_f32_16x16x32_bf16 v[132:135], a[148:151], a[4:7], v[132:135]\n"
    "v_exp_f32 v157, v157\n"
    "v_mfma_f32_16x16x32_bf16 v[132:135], a[152:155], a[8:11], v[132:135]\n"
    "ds_read_b64_tr_b16 a[166:167], v179 offset:4352\n"
    "v_cvt_pk_bf16_f32 v173, v154, v155\n"
    "v_mfma_f32_16x16x32_bf16 v[132:135], a[156:159], a[12:15], v[132:135]\n"
    "v_mul_f32 v158, s54, v158\n"
    "v_mul_f32 v159, s54, v159\n"
    "v_mfma_f32_16x16x32_bf16 v[136:139], a[128:131], a[16:19], v[252:255]\n"
    "v_exp_f32 v158, v158\n"
    "v_mfma_f32_16x16x32_bf16 v[136:139], a[132:135], a[20:23], v[136:139]\n"
    "v_exp_f32 v159, v159\n"
    "v_mfma_f32_16x16x32_bf16 v[136:139], a[136:139], a[24:27], v[136:139]\n"
    "v_cvt_pk_bf16_f32 v174, v156, v157\n"
    "v_cvt_pk_bf16_f32 v175, v158, v159\n"
    "v_mfma_f32_16x16x32_bf16 v[136:139], a[140:143], a[28:31], v[136:139]\n"
    "buffer_load_dwordx4 v182, s[36:39], s73 offen offset:2176 nt lds\n"
    "v_mul_f32 v128, s54, v128\n"
    "v_mfma_f32_16x16x32_bf16 v[140:143], a[144:147], a[16:19], v[252:255]\n"
    "ds_read_b64_tr_b16 a[168:169], v179 offset:512\n"
    "v_mul_f32 v129, s54, v129\n"
    "v_mfma_f32_16x16x32_bf16 v[140:143], a[148:151], a[20:23], v[140:143]\n"
    "v_exp_f32 v128, v128\n"
    "v_mul_f32 v130, s54, v130\n"
    "v_mfma_f32_16x16x32_bf16 v[140:143], a[152:155], a[24:27], v[140:143]\n"
    "ds_read_b64_tr_b16 a[170:171], v179 offset:4608\n"
    "v_mul_f32 v131, s54, v131\n"
    "v_mfma_f32_16x16x32_bf16 v[140:143], a[156:159], a[28:31], v[140:143]\n"
    "v_exp_f32 v129, v129\n"
    "v_mfma_f32_16x16x32_bf16 v[64:67], a[192:195], v[168:171], v[64:67]\n"
    "ds_read_b64_tr_b16 a[172:173], v179 offset:768\n"
    "v_cvt_pk_bf16_f32 v160, v128, v129\n"
    "v_mfma_f32_16x16x32_bf16 v[68:71], a[196:199], v[168:171], v[68:71]\n"
    "v_exp_f32 v130, v130\n"
    "v_mul_f32 v132, s54, v132\n"
    "v_mfma_f32_16x16x32_bf16 v[72:75], a[200:203], v[168:171], v[72:75]\n"
    "ds_read_b64_tr_b16 a[174:175], v179 offset:4864\n"
    "v_mul_f32 v133, s54, v133\n"
    "v_mfma_f32_16x16x32_bf16 v[76:79], a[204:207], v[168:171], v[76:79]\n"
    "v_exp_f32 v131, v131\n"
    "v_mfma_f32_16x16x32_bf16 v[80:83], a[208:211], v[168:171], v[80:83]\n"
    "s_add_u32 m0, s52, 0x4000\n"
    "ds_read_b64_tr_b16 a[176:177], v179 offset:1024\n"
    "v_cvt_pk_bf16_f32 v161, v130, v131\n"
    "v_mfma_f32_16x16x32_bf16 v[84:87], a[212:215], v[168:171], v[84:87]\n"
    "buffer_load_dwordx4 v183, s[40:43], s73 offen offset:0 nt lds\n"
    "v_exp_f32 v132, v132\n"
    "v_mfma_f32_16x16x32_bf16 v[88:91], a[216:219], v[168:171], v[88:91]\n"
    "ds_read_b64_tr_b16 a[178:179], v179 offset:5120\n"
    "v_mul_f32 v134, s54, v134\n"
    "v_mfma_f32_16x16x32_bf16 v[92:95], a[220:223], v[168:171], v[92:95]\n"
    "v_exp_f32 v133, v133\n"
    "v_mfma_f32_16x16x32_bf16 a[232:235], a[240:243], v[168:171], a[232:235]\n"
    "ds_read_b64_tr_b16 a[180:181], v179 offset:1280\n"
    "v_mul_f32 v135, s54, v135\n"
    "v_mfma_f32_16x16x32_bf16 v[96:99], a[192:195], v[172:175], v[96:99]\n"
    "v_exp_f32 v134, v134\n"
    "v_cvt_pk_bf16_f32 v162, v132, v133\n"
    "v_mfma_f32_16x16x32_bf16 v[100:103], a[196:199], v[172:175], v[100:103]\n"
    "ds_read_b64_tr_b16 a[182:183], v179 offset:5376\n"
    "v_mfma_f32_16x16x32_bf16 v[104:107], a[200:203], v[172:175], v[104:107]\n"
    "v_exp_f32 v135, v135\n"
    "v_mfma_f32_16x16x32_bf16 v[108:111], a[204:207], v[172:175], v[108:111]\n"
    "v_cvt_pk_bf16_f32 v163, v134, v135\n"
    "v_mul_f32 v136, s54, v136\n"
    "v_mfma_f32_16x16x32_bf16 v[112:115], a[208:211], v[172:175], v[112:115]\n"
    "v_exp_f32 v136, v136\n"
    "v_mfma_f32_16x16x32_bf16 v[116:119], a[212:215], v[172:175], v[116:119]\n"
    "ds_read_b64_tr_b16 a[184:185], v179 offset:1536\n"
    "v_mul_f32 v137, s54, v137\n"
    "v_mul_f32 v138, s54, v138\n"
    "v_mfma_f32_16x16x32_bf16 v[120:123], a[216:219], v[172:175], v[120:123]\n"
    "v_exp_f32 v137, v137\n"
    "v_mfma_f32_16x16x32_bf16 v[124:127], a[220:223], v[172:175], v[124:127]\n"
    "v_exp_f32 v138, v138\n"
    "v_mfma_f32_16x16x32_bf16 a[236:239], a[240:243], v[172:175], a[236:239]\n"
    "buffer_load_dwordx4 v183, s[40:43], s73 offen offset:2048 nt lds\n"
    "v_mul_f32 v139, s54, v139\n"
    "v_mfma_f32_16x16x32_bf16 v[144:147], a[128:131], a[32:35], v[196:199]\n"
    "ds_read_b64_tr_b16 a[186:187], v179 offset:5632\n"
    "v_cvt_pk_bf16_f32 v164, v136, v137\n"
    "v_mfma_f32_16x16x32_bf16 v[144:147], a[132:135], a[36:39], v[144:147]\n"
    "v_exp_f32 v139, v139\n"
    "v_mfma_f32_16x16x32_bf16 v[144:147], a[136:139], a[40:43], v[144:147]\n"
    "ds_read_b64_tr_b16 a[188:189], v179 offset:1792\n"
    "v_mul_f32 v140, s54, v140\n"
    "v_mul_f32 v141, s54, v141\n"
    "v_mfma_f32_16x16x32_bf16 v[144:147], a[140:143], a[44:47], v[144:147]\n"
    "v_exp_f32 v140, v140\n"
    "v_mfma_f32_16x16x32_bf16 v[148:151], a[144:147], a[32:35], v[196:199]\n"
    "ds_read_b64_tr_b16 a[190:191], v179 offset:5888\n"
    "v_cvt_pk_bf16_f32 v165, v138, v139\n"
    "v_mfma_f32_16x16x32_bf16 v[148:151], a[148:151], a[36:39], v[148:151]\n"
    "v_exp_f32 v141, v141\n"
    "v_mfma_f32_16x16x32_bf16 v[148:151], a[152:155], a[40:43], v[148:151]\n"
    "v_mul_f32 v142, s54, v142\n"
    "v_mul_f32 v143, s54, v143\n"
    "v_mfma_f32_16x16x32_bf16 v[148:151], a[156:159], a[44:47], v[148:151]\n"
    "v_exp_f32 v142, v142\n"
    "v_mfma_f32_16x16x32_bf16 v[152:155], a[128:131], a[48:51], v[212:215]\n"
    "s_waitcnt lgkmcnt(0)\n"
    "s_add_u32 m0, s52, 0x4380\n"
    "v_exp_f32 v143, v143\n"
    "v_mfma_f32_16x16x32_bf16 v[152:155], a[132:135], a[52:55], v[152:155]\n"
    "buffer_load_dwordx4 v183, s[40:43], s73 offen offset:128 nt lds\n"
    "v_cvt_pk_bf16_f32 v166, v140, v141\n"
    "v_mfma_f32_16x16x32_bf16 v[152:155], a[136:139], a[56:59], v[152:155]\n"
    "ds_read_b128 a[128:131], v177 offset:8192\n"
    "v_cvt_pk_bf16_f32 v167, v142, v143\n"
    "v_mfma_f32_16x16x32_bf16 v[152:155], a[140:143], a[60:63], v[152:155]\n"
    "v_mul_f32 v144, s54, v144\n"
    "v_mul_f32 v145, s54, v145\n"
    "v_mfma_f32_16x16x32_bf16 v[156:159], a[144:147], a[48:51], v[212:215]\n"
    "ds_read_b128 a[132:135], v177 offset:8704\n"
    "v_mul_f32 v146, s54, v146\n"
    "v_mfma_f32_16x16x32_bf16 v[156:159], a[148:151], a[52:55], v[156:159]\n"
    "v_exp_f32 v144, v144\n"
    "v_mfma_f32_16x16x32_bf16 v[156:159], a[152:155], a[56:59], v[156:159]\n"
    "ds_read_b128 a[136:139], v177 offset:9216\n"
    "v_mul_f32 v147, s54, v147\n"
    "v_mfma_f32_16x16x32_bf16 v[156:159], a[156:159], a[60:63], v[156:159]\n"
    "v_exp_f32 v145, v145\n"
    "v_mul_f32 v148, s54, v148\n"
    "v_mfma_f32_16x16x32_bf16 v[0:3], a[160:163], v[160:163], v[0:3]\n"
    "ds_read_b128 a[140:143], v177 offset:9728\n"
    "v_cvt_pk_bf16_f32 v168, v144, v145\n"
    "v_mfma_f32_16x16x32_bf16 v[4:7], a[164:167], v[160:163], v[4:7]\n"
    "v_exp_f32 v146, v146\n"
    "v_mfma_f32_16x16x32_bf16 v[8:11], a[168:171], v[160:163], v[8:11]\n"
    "ds_read_b128 a[144:147], v177 offset:12288\n"
    "v_mul_f32 v149, s54, v149\n"
    "v_mfma_f32_16x16x32_bf16 v[12:15], a[172:175], v[160:163], v[12:15]\n"
    "v_exp_f32 v147, v147\n"
    "v_mfma_f32_16x16x32_bf16 v[16:19], a[176:179], v[160:163], v[16:19]\n"
    "ds_read_b128 a[148:151], v177 offset:12800\n"
    "v_cvt_pk_bf16_f32 v169, v146, v147\n"
    "v_mfma_f32_16x16x32_bf16 v[20:23], a[180:183], v[160:163], v[20:23]\n"
    "buffer_load_dwordx4 v183, s[40:43], s73 offen offset:2176 nt lds\n"
    "v_mul_f32 v150, s54, v150\n"
    "v_mfma_f32_16x16x32_bf16 v[24:27], a[184:187], v[160:163], v[24:27]\n"
    "ds_read_b128 a[152:155], v177 offset:13312\n"
    "v_mul_f32 v151, s54, v151\n"
    "v_mfma_f32_16x16x32_bf16 v[28:31], a[188:191], v[160:163], v[28:31]\n"
    "v_exp_f32 v148, v148\n"
    "v_mfma_f32_16x16x32_bf16 a[224:227], a[240:243], v[160:163], a[224:227]\n"
    "ds_read_b128 a[156:159], v177 offset:13824\n"
    "v_mfma_f32_16x16x32_bf16 v[32:35], a[160:163], v[164:167], v[32:35]\n"
    "v_exp_f32 v149, v149\n"
    "v_mfma_f32_16x16x32_bf16 v[36:39], a[164:167], v[164:167], v[36:39]\n"
    "v_exp_f32 v150, v150\n"
    "v_mfma_f32_16x16x32_bf16 v[40:43], a[168:171], v[164:167], v[40:43]\n"
    "v_exp_f32 v151, v151\n"
    "v_cvt_pk_bf16_f32 v170, v148, v149\n"
    "v_mfma_f32_16x16x32_bf16 v[44:47], a[172:175], v[164:167], v[44:47]\n"
    "v_cvt_pk_bf16_f32 v171, v150, v151\n"
    "v_mul_f32 v152, s54, v152\n"
    "v_mfma_f32_16x16x32_bf16 v[48:51], a[176:179], v[164:167], v[48:51]\n"
    "v_exp_f32 v152, v152\n"
    "v_mfma_f32_16x16x32_bf16 v[52:55], a[180:183], v[164:167], v[52:55]\n"
    "s_add_u32 s58, s53, 0x80\n"
    "s_add_u32 m0, s52, 0x24000\n"
    "ds_read_b64_tr_b16 a[192:193], v179 offset:8192\n"
    "v_mfma_f32_16x16x32_bf16 v[56:59], a[184:187], v[164:167], v[56:59]\n"
    "buffer_load_dwordx4 v181, s[44:47], s58 offen offset:0 nt lds\n"
    "v_mul_f32 v153, s54, v153\n"
    "v_mfma_f32_16x16x32_bf16 v[60:63], a[188:191], v[164:167], v[60:63]\n"
    "s_waitcnt lgkmcnt(1)\n"
    "ds_read_b64_tr_b16 a[194:195], v179 offset:12288\n"
    "v_exp_f32 v153, v153\n"
    "v_mfma_f32_16x16x32_bf16 a[228:231], a[240:243], v[164:167], a[228:231]\n"
    "v_mul_f32 v154, s54, v154\n"
    "v_mul_f32 v155, s54, v155\n"
    "v_mfma_f32_16x16x32_bf16 v[128:131], a[128:131], a[0:3], v[248:251]\n"
    "v_exp_f32 v154, v154\n"
    "v_mfma_f32_16x16x32_bf16 v[128:131], a[132:135], a[4:7], v[128:131]\n"
    "v_exp_f32 v155, v155\n"
    "v_mfma_f32_16x16x32_bf16 v[128:131], a[136:139], a[8:11], v[128:131]\n"
    "ds_read_b64_tr_b16 a[196:197], v179 offset:8448\n"
    "v_cvt_pk_bf16_f32 v172, v152, v153\n"
    "v_mfma_f32_16x16x32_bf16 v[128:131], a[140:143], a[12:15], v[128:131]\n"
    "v_mul_f32 v156, s54, v156\n"
    "v_mul_f32 v157, s54, v157\n"
    "v_mfma_f32_16x16x32_bf16 v[132:135], a[144:147], a[0:3], v[248:251]\n"
    "v_exp_f32 v156, v156\n"
    "v_mfma_f32_16x16x32_bf16 v[132:135], a[148:151], a[4:7], v[132:135]\n"
    "v_exp_f32 v157, v157\n"
    "v_mfma_f32_16x16x32_bf16 v[132:135], a[152:155], a[8:11], v[132:135]\n"
    "ds_read_b64_tr_b16 a[198:199], v179 offset:12544\n"
    "v_cvt_pk_bf16_f32 v173, v154, v155\n"
    "v_mul_f32 v158, s54, v158\n"
    "v_mfma_f32_16x16x32_bf16 v[132:135], a[156:159], a[12:15], v[132:135]\n"
    "v_exp_f32 v158, v158\n"
    "v_mfma_f32_16x16x32_bf16 v[136:139], a[128:131], a[16:19], v[252:255]\n"
    "s_add_u32 m0, s52, 0x23c00\n"
    "v_mul_f32 v159, s54, v159\n"
    "v_cvt_pk_bf16_f32 v174, v156, v157\n"
    "v_mfma_f32_16x16x32_bf16 v[136:139], a[132:135], a[20:23], v[136:139]\n"
    "buffer_load_dwordx4 v182, s[44:47], s58 offen offset:2048 nt lds\n"
    "v_mfma_f32_16x16x32_bf16 v[136:139], a[136:139], a[24:27], v[136:139]\n"
    "v_exp_f32 v159, v159\n"
    "v_mfma_f32_16x16x32_bf16 v[136:139], a[140:143], a[28:31], v[136:139]\n"
    "v_cvt_pk_bf16_f32 v175, v158, v159\n"
    "v_mul_f32 v128, s54, v128\n"
    "v_mfma_f32_16x16x32_bf16 v[140:143], a[144:147], a[16:19], v[252:255]\n"
    "ds_read_b64_tr_b16 a[200:201], v179 offset:8704\n"
    "v_exp_f32 v128, v128\n"
    "v_mfma_f32_16x16x32_bf16 v[140:143], a[148:151], a[20:23], v[140:143]\n"
    "v_mul_f32 v129, s54, v129\n"
    "v_mul_f32 v130, s54, v130\n"
    "v_mfma_f32_16x16x32_bf16 v[140:143], a[152:155], a[24:27], v[140:143]\n"
    "ds_read_b64_tr_b16 a[202:203], v179 offset:12800\n"
    "v_mul_f32 v131, s54, v131\n"
    "v_mfma_f32_16x16x32_bf16 v[140:143], a[156:159], a[28:31], v[140:143]\n"
    "v_exp_f32 v129, v129\n"
    "v_mfma_f32_16x16x32_bf16 v[64:67], a[160:163], v[168:171], v[64:67]\n"
    "ds_read_b64_tr_b16 a[204:205], v179 offset:8960\n"
    "v_exp_f32 v130, v130\n"
    "v_mfma_f32_16x16x32_bf16 v[68:71], a[164:167], v[168:171], v[68:71]\n"
    "v_exp_f32 v131, v131\n"
    "v_mfma_f32_16x16x32_bf16 v[72:75], a[168:171], v[168:171], v[72:75]\n"
    "s_add_u32 s58, s53, 0x1080\n"
    "s_add_u32 m0, s52, 0x24800\n"
    "ds_read_b64_tr_b16 a[206:207], v179 offset:13056\n"
    "v_mfma_f32_16x16x32_bf16 v[76:79], a[172:175], v[168:171], v[76:79]\n"
    "buffer_load_dwordx4 v181, s[44:47], s58 offen offset:0 nt lds\n"
    "v_cvt_pk_bf16_f32 v160, v128, v129\n"
    "v_mul_f32 v132, s54, v132\n"
    "v_mfma_f32_16x16x32_bf16 v[80:83], a[176:179], v[168:171], v[80:83]\n"
    "ds_read_b64_tr_b16 a[208:209], v179 offset:9216\n"
    "v_mul_f32 v133, s54, v133\n"
    "v_mfma_f32_16x16x32_bf16 v[84:87], a[180:183], v[168:171], v[84:87]\n"
    "v_exp_f32 v132, v132\n"
    "v_mfma_f32_16x16x32_bf16 v[88:91], a[184:187], v[168:171], v[88:91]\n"
    "ds_read_b64_tr_b16 a[210:211], v179 offset:13312\n"
    "v_exp_f32 v133, v133\n"
    "v_mfma_f32_16x16x32_bf16 v[92:95], a[188:191], v[168:171], v[92:95]\n"
    "v_cvt_pk_bf16_f32 v161, v130, v131\n"
    "v_mul_f32 v134, s54, v134\n"
    "v_mfma_f32_16x16x32_bf16 a[232:235], a[240:243], v[168:171], a[232:235]\n"
    "ds_read_b64_tr_b16 a[212:213], v179 offset:9472\n"
    "v_mul_f32 v135, s54, v135\n"
    "v_mfma_f32_16x16x32_bf16 v[96:99], a[160:163], v[172:175], v[96:99]\n"
    "v_exp_f32 v134, v134\n"
    "v_mfma_f32_16x16x32_bf16 v[100:103], a[164:167], v[172:175], v[100:103]\n"
    "ds_read_b64_tr_b16 a[214:215], v179 offset:13568\n"
    "v_exp_f32 v135, v135\n"
    "v_mfma_f32_16x16x32_bf16 v[104:107], a[168:171], v[172:175], v[104:107]\n"
    "v_cvt_pk_bf16_f32 v162, v132, v133\n"
    "v_cvt_pk_bf16_f32 v163, v134, v135\n"
    "v_mfma_f32_16x16x32_bf16 v[108:111], a[172:175], v[172:175], v[108:111]\n"
    "v_mul_f32 v136, s54, v136\n"
    "v_mul_f32 v137, s54, v137\n"
    "v_mfma_f32_16x16x32_bf16 v[112:115], a[176:179], v[172:175], v[112:115]\n"
    "v_exp_f32 v136, v136\n"
    "v_mfma_f32_16x16x32_bf16 v[116:119], a[180:183], v[172:175], v[116:119]\n"
    "s_add_u32 m0, s52, 0x24400\n"
    "ds_read_b64_tr_b16 a[216:217], v179 offset:9728\n"
    "v_mul_f32 v138, s54, v138\n"
    "v_mfma_f32_16x16x32_bf16 v[120:123], a[184:187], v[172:175], v[120:123]\n"
    "buffer_load_dwordx4 v182, s[44:47], s58 offen offset:2048 nt lds\n"
    "v_mul_f32 v139, s54, v139\n"
    "v_mfma_f32_16x16x32_bf16 v[124:127], a[188:191], v[172:175], v[124:127]\n"
    "v_exp_f32 v137, v137\n"
    "v_mfma_f32_16x16x32_bf16 a[236:239], a[240:243], v[172:175], a[236:239]\n"
    "v_exp_f32 v138, v138\n"
    "v_mfma_f32_16x16x32_bf16 v[144:147], a[128:131], a[32:35], v[196:199]\n"
    "ds_read_b64_tr_b16 a[218:219], v179 offset:13824\n"
    "v_cvt_pk_bf16_f32 v164, v136, v137\n"
    "v_mfma_f32_16x16x32_bf16 v[144:147], a[132:135], a[36:39], v[144:147]\n"
    "v_exp_f32 v139, v139\n"
    "v_mfma_f32_16x16x32_bf16 v[144:147], a[136:139], a[40:43], v[144:147]\n"
    "ds_read_b64_tr_b16 a[220:221], v179 offset:9984\n"
    "v_mul_f32 v140, s54, v140\n"
    "v_mfma_f32_16x16x32_bf16 v[144:147], a[140:143], a[44:47], v[144:147]\n"
    "v_exp_f32 v140, v140\n"
    "v_mfma_f32_16x16x32_bf16 v[148:151], a[144:147], a[32:35], v[196:199]\n"
    "ds_read_b64_tr_b16 a[222:223], v179 offset:14080\n"
    "v_mul_f32 v141, s54, v141\n"
    "v_mfma_f32_16x16x32_bf16 v[148:151], a[148:151], a[36:39], v[148:151]\n"
    "v_exp_f32 v141, v141\n"
    "v_mfma_f32_16x16x32_bf16 v[148:151], a[152:155], a[40:43], v[148:151]\n"
    "v_cvt_pk_bf16_f32 v165, v138, v139\n"
    "v_mul_f32 v142, s54, v142\n"
    "v_mfma_f32_16x16x32_bf16 v[148:151], a[156:159], a[44:47], v[148:151]\n"
    "v_exp_f32 v142, v142\n"
    "v_mfma_f32_16x16x32_bf16 v[152:155], a[128:131], a[48:51], v[212:215]\n"
    "s_waitcnt vmcnt(12) lgkmcnt(0)\n"
    "s_barrier\n"
    "v_mul_f32 v143, s54, v143\n"
    "v_cvt_pk_bf16_f32 v166, v140, v141\n"
    "v_mfma_f32_16x16x32_bf16 v[152:155], a[132:135], a[52:55], v[152:155]\n"
    "v_exp_f32 v143, v143\n"
    "v_mfma_f32_16x16x32_bf16 v[152:155], a[136:139], a[56:59], v[152:155]\n"
    "s_add_u32 m0, s52, 0x8000\n"
    "ds_read_b128 a[128:131], v177 offset:32768\n"
    "v_cvt_pk_bf16_f32 v167, v142, v143\n"
    "v_mfma_f32_16x16x32_bf16 v[152:155], a[140:143], a[60:63], v[152:155]\n"
    "buffer_load_dwordx4 v181, s[36:39], s74 offen offset:0 nt lds\n"
    "v_mul_f32 v144, s54, v144\n"
    "v_mfma_f32_16x16x32_bf16 v[156:159], a[144:147], a[48:51], v[212:215]\n"
    "ds_read_b128 a[132:135], v177 offset:33280\n"
    "v_mul_f32 v145, s54, v145\n"
    "v_mfma_f32_16x16x32_bf16 v[156:159], a[148:151], a[52:55], v[156:159]\n"
    "v_exp_f32 v144, v144\n"
    "v_mul_f32 v146, s54, v146\n"
    "v_mfma_f32_16x16x32_bf16 v[156:159], a[152:155], a[56:59], v[156:159]\n"
    "ds_read_b128 a[136:139], v177 offset:33792\n"
    "v_mul_f32 v147, s54, v147\n"
    "v_mfma_f32_16x16x32_bf16 v[156:159], a[156:159], a[60:63], v[156:159]\n"
    "v_exp_f32 v145, v145\n"
    "v_mfma_f32_16x16x32_bf16 v[0:3], a[192:195], v[160:163], v[0:3]\n"
    "ds_read_b128 a[140:143], v177 offset:34304\n"
    "v_cvt_pk_bf16_f32 v168, v144, v145\n"
    "v_mfma_f32_16x16x32_bf16 v[4:7], a[196:199], v[160:163], v[4:7]\n"
    "v_exp_f32 v146, v146\n"
    "v_mfma_f32_16x16x32_bf16 v[8:11], a[200:203], v[160:163], v[8:11]\n"
    "ds_read_b128 a[144:147], v177 offset:36864\n"
    "v_mul_f32 v148, s54, v148\n"
    "v_mfma_f32_16x16x32_bf16 v[12:15], a[204:207], v[160:163], v[12:15]\n"
    "v_exp_f32 v147, v147\n"
    "v_mul_f32 v149, s54, v149\n"
    "v_mfma_f32_16x16x32_bf16 v[16:19], a[208:211], v[160:163], v[16:19]\n"
    "ds_read_b128 a[148:151], v177 offset:37376\n"
    "v_cvt_pk_bf16_f32 v169, v146, v147\n"
    "v_mfma_f32_16x16x32_bf16 v[20:23], a[212:215], v[160:163], v[20:23]\n"
    "v_exp_f32 v148, v148\n"
    "v_mfma_f32_16x16x32_bf16 v[24:27], a[216:219], v[160:163], v[24:27]\n"
    "ds_read_b128 a[152:155], v177 offset:37888\n"
    "v_mul_f32 v150, s54, v150\n"
    "v_mfma_f32_16x16x32_bf16 v[28:31], a[220:223], v[160:163], v[28:31]\n"
    "buffer_load_dwordx4 v182, s[36:39], s74 offen offset:2048 nt lds\n"
    "v_mul_f32 v151, s54, v151\n"
    "v_mfma_f32_16x16x32_bf16 a[224:227], a[240:243], v[160:163], a[224:227]\n"
    "ds_read_b128 a[156:159], v177 offset:38400\n"
    "v_mfma_f32_16x16x32_bf16 v[32:35], a[192:195], v[164:167], v[32:35]\n"
    "v_exp_f32 v149, v149\n"
    "v_mfma_f32_16x16x32_bf16 v[36:39], a[196:199], v[164:167], v[36:39]\n"
    "v_exp_f32 v150, v150\n"
    "v_mfma_f32_16x16x32_bf16 v[40:43], a[200:203], v[164:167], v[40:43]\n"
    "v_exp_f32 v151, v151\n"
    "v_cvt_pk_bf16_f32 v170, v148, v149\n"
    "v_mfma_f32_16x16x32_bf16 v[44:47], a[204:207], v[164:167], v[44:47]\n"
    "v_cvt_pk_bf16_f32 v171, v150, v151\n"
    "v_mul_f32 v152, s54, v152\n"
    "v_mfma_f32_16x16x32_bf16 v[48:51], a[208:211], v[164:167], v[48:51]\n"
    "v_exp_f32 v152, v152\n"
    "v_mfma_f32_16x16x32_bf16 v[52:55], a[212:215], v[164:167], v[52:55]\n"
    "ds_read_b64_tr_b16 a[160:161], v179 offset:32768\n"
    "v_mul_f32 v153, s54, v153\n"
    "v_mfma_f32_16x16x32_bf16 v[56:59], a[216:219], v[164:167], v[56:59]\n"
    "v_exp_f32 v153, v153\n"
    "v_mfma_f32_16x16x32_bf16 v[60:63], a[220:223], v[164:167], v[60:63]\n"
    "s_waitcnt lgkmcnt(1)\n"
    "s_add_u32 m0, s52, 0x8380\n"
    "ds_read_b64_tr_b16 a[162:163], v179 offset:36864\n"
    "v_mfma_f32_16x16x32_bf16 a[228:231], a[240:243], v[164:167], a[228:231]\n"
    "buffer_load_dwordx4 v181, s[36:39], s74 offen offset:128 nt lds\n"
    "v_mul_f32 v154, s54, v154\n"
    "v_mfma_f32_16x16x32_bf16 v[128:131], a[128:131], a[0:3], v[248:251]\n"
    "v_exp_f32 v154, v154\n"
    "v_mfma_f32_16x16x32_bf16 v[128:131], a[132:135], a[4:7], v[128:131]\n"
    "v_mul_f32 v155, s54, v155\n"
    "v_cvt_pk_bf16_f32 v172, v152, v153\n"
    "v_mfma_f32_16x16x32_bf16 v[128:131], a[136:139], a[8:11], v[128:131]\n"
    "ds_read_b64_tr_b16 a[164:165], v179 offset:33024\n"
    "v_exp_f32 v155, v155\n"
    "v_mfma_f32_16x16x32_bf16 v[128:131], a[140:143], a[12:15], v[128:131]\n"
    "v_mul_f32 v156, s54, v156\n"
    "v_mul_f32 v157, s54, v157\n"
    "v_mfma_f32_16x16x32_bf16 v[132:135], a[144:147], a[0:3], v[248:251]\n"
    "v_exp_f32 v156, v156\n"
    "v_mfma_f32_16x16x32_bf16 v[132:135], a[148:151], a[4:7], v[132:135]\n"
    "v_exp_f32 v157, v157\n"
    "v_mfma_f32_16x16x32_bf16 v[132:135], a[152:155], a[8:11], v[132:135]\n"
    "ds_read_b64_tr_b16 a[166:167], v179 offset:37120\n"
    "v_cvt_pk_bf16_f32 v173, v154, v155\n"
    "v_mfma_f32_16x16x32_bf16 v[132:135], a[156:159], a[12:15], v[132:135]\n"
    "v_mul_f32 v158, s54, v158\n"
    "v_mul_f32 v159, s54, v159\n"
    "v_mfma_f32_16x16x32_bf16 v[136:139], a[128:131], a[16:19], v[252:255]\n"
    "v_exp_f32 v158, v158\n"
    "v_mfma_f32_16x16x32_bf16 v[136:139], a[132:135], a[20:23], v[136:139]\n"
    "v_exp_f32 v159, v159\n"
    "v_mfma_f32_16x16x32_bf16 v[136:139], a[136:139], a[24:27], v[136:139]\n"
    "v_cvt_pk_bf16_f32 v174, v156, v157\n"
    "v_cvt_pk_bf16_f32 v175, v158, v159\n"
    "v_mfma_f32_16x16x32_bf16 v[136:139], a[140:143], a[28:31], v[136:139]\n"
    "buffer_load_dwordx4 v182, s[36:39], s74 offen offset:2176 nt lds\n"
    "v_mul_f32 v128, s54, v128\n"
    "v_mfma_f32_16x16x32_bf16 v[140:143], a[144:147], a[16:19], v[252:255]\n"
    "ds_read_b64_tr_b16 a[168:169], v179 offset:33280\n"
    "v_mul_f32 v129, s54, v129\n"
    "v_mfma_f32_16x16x32_bf16 v[140:143], a[148:151], a[20:23], v[140:143]\n"
    "v_exp_f32 v128, v128\n"
    "v_mul_f32 v130, s54, v130\n"
    "v_mfma_f32_16x16x32_bf16 v[140:143], a[152:155], a[24:27], v[140:143]\n"
    "ds_read_b64_tr_b16 a[170:171], v179 offset:37376\n"
    "v_mul_f32 v131, s54, v131\n"
    "v_mfma_f32_16x16x32_bf16 v[140:143], a[156:159], a[28:31], v[140:143]\n"
    "v_exp_f32 v129, v129\n"
    "v_mfma_f32_16x16x32_bf16 v[64:67], a[192:195], v[168:171], v[64:67]\n"
    "ds_read_b64_tr_b16 a[172:173], v179 offset:33536\n"
    "v_cvt_pk_bf16_f32 v160, v128, v129\n"
    "v_mfma_f32_16x16x32_bf16 v[68:71], a[196:199], v[168:171], v[68:71]\n"
    "v_exp_f32 v130, v130\n"
    "v_mul_f32 v132, s54, v132\n"
    "v_mfma_f32_16x16x32_bf16 v[72:75], a[200:203], v[168:171], v[72:75]\n"
    "ds_read_b64_tr_b16 a[174:175], v179 offset:37632\n"
    "v_mul_f32 v133, s54, v133\n"
    "v_mfma_f32_16x16x32_bf16 v[76:79], a[204:207], v[168:171], v[76:79]\n"
    "v_exp_f32 v131, v131\n"
    "v_mfma_f32_16x16x32_bf16 v[80:83], a[208:211], v[168:171], v[80:83]\n"
    "s_add_u32 m0, s52, 0xc000\n"
    "ds_read_b64_tr_b16 a[176:177], v179 offset:33792\n"
    "v_cvt_pk_bf16_f32 v161, v130, v131\n"
    "v_mfma_f32_16x16x32_bf16 v[84:87], a[212:215], v[168:171], v[84:87]\n"
    "buffer_load_dwordx4 v183, s[40:43], s74 offen offset:0 nt lds\n"
    "v_exp_f32 v132, v132\n"
    "v_mfma_f32_16x16x32_bf16 v[88:91], a[216:219], v[168:171], v[88:91]\n"
    "ds_read_b64_tr_b16 a[178:179], v179 offset:37888\n"
    "v_mul_f32 v134, s54, v134\n"
    "v_mfma_f32_16x16x32_bf16 v[92:95], a[220:223], v[168:171], v[92:95]\n"
    "v_exp_f32 v133, v133\n"
    "v_mfma_f32_16x16x32_bf16 a[232:235], a[240:243], v[168:171], a[232:235]\n"
    "ds_read_b64_tr_b16 a[180:181], v179 offset:34048\n"
    "v_mul_f32 v135, s54, v135\n"
    "v_mfma_f32_16x16x32_bf16 v[96:99], a[192:195], v[172:175], v[96:99]\n"
    "v_exp_f32 v134, v134\n"
    "v_cvt_pk_bf16_f32 v162, v132, v133\n"
    "v_mfma_f32_16x16x32_bf16 v[100:103], a[196:199], v[172:175], v[100:103]\n"
    "ds_read_b64_tr_b16 a[182:183], v179 offset:38144\n"
    "v_mfma_f32_16x16x32_bf16 v[104:107], a[200:203], v[172:175], v[104:107]\n"
    "v_exp_f32 v135, v135\n"
    "v_mfma_f32_16x16x32_bf16 v[108:111], a[204:207], v[172:175], v[108:111]\n"
    "ds_read_b128 a[64:67], v180 offset:0\n"
    "v_cvt_pk_bf16_f32 v163, v134, v135\n"
    "v_mfma_f32_16x16x32_bf16 v[112:115], a[208:211], v[172:175], v[112:115]\n"
    "v_mul_f32 v136, s54, v136\n"
    "v_exp_f32 v136, v136\n"
    "v_mfma_f32_16x16x32_bf16 v[116:119], a[212:215], v[172:175], v[116:119]\n"
    "ds_read_b64_tr_b16 a[184:185], v179 offset:34304\n"
    "v_mul_f32 v137, s54, v137\n"
    "v_mfma_f32_16x16x32_bf16 v[120:123], a[216:219], v[172:175], v[120:123]\n"
    "v_exp_f32 v137, v137\n"
    "v_mul_f32 v138, s54, v138\n"
    "v_mfma_f32_16x16x32_bf16 v[124:127], a[220:223], v[172:175], v[124:127]\n"
    "ds_read_b128 a[68:71], v180 offset:512\n"
    "v_mul_f32 v139, s54, v139\n"
    "v_mfma_f32_16x16x32_bf16 a[236:239], a[240:243], v[172:175], a[236:239]\n"
    "buffer_load_dwordx4 v183, s[40:43], s74 offen offset:2048 nt lds\n"
    "v_cvt_pk_bf16_f32 v164, v136, v137\n"
    "v_mfma_f32_16x16x32_bf16 v[144:147], a[128:131], a[32:35], v[196:199]\n"
    "ds_read_b64_tr_b16 a[186:187], v179 offset:38400\n"
    "v_exp_f32 v138, v138\n"
    "v_mfma_f32_16x16x32_bf16 v[144:147], a[132:135], a[36:39], v[144:147]\n"
    "v_exp_f32 v139, v139\n"
    "v_mfma_f32_16x16x32_bf16 v[144:147], a[136:139], a[40:43], v[144:147]\n"
    "ds_read_b64_tr_b16 a[188:189], v179 offset:34560\n"
    "v_mul_f32 v140, s54, v140\n"
    "v_mul_f32 v141, s54, v141\n"
    "v_mfma_f32_16x16x32_bf16 v[144:147], a[140:143], a[44:47], v[144:147]\n"
    "v_exp_f32 v140, v140\n"
    "v_mfma_f32_16x16x32_bf16 v[148:151], a[144:147], a[32:35], v[196:199]\n"
    "ds_read_b64_tr_b16 a[190:191], v179 offset:38656\n"
    "v_exp_f32 v141, v141\n"
    "v_mfma_f32_16x16x32_bf16 v[148:151], a[148:151], a[36:39], v[148:151]\n"
    "v_cvt_pk_bf16_f32 v165, v138, v139\n"
    "v_mul_f32 v142, s54, v142\n"
    "v_mfma_f32_16x16x32_bf16 v[148:151], a[152:155], a[40:43], v[148:151]\n"
    "ds_read_b128 a[80:83], v180 offset:2048\n"
    "v_mul_f32 v143, s54, v143\n"
    "v_mfma_f32_16x16x32_bf16 v[148:151], a[156:159], a[44:47], v[148:151]\n"
    "v_exp_f32 v142, v142\n"
    "v_cvt_pk_bf16_f32 v166, v140, v141\n"
    "v_mfma_f32_16x16x32_bf16 v[152:155], a[128:131], a[48:51], v[212:215]\n"
    "s_waitcnt lgkmcnt(0)\n"
    "s_add_u32 m0, s52, 0xc380\n"
    "v_mfma_f32_16x16x32_bf16 v[152:155], a[132:135], a[52:55], v[152:155]\n"
    "buffer_load_dwordx4 v183, s[40:43], s74 offen offset:128 nt lds\n"
    "ds_read_b128 a[84:87], v180 offset:2560\n"
    "v_mfma_f32_16x16x32_bf16 v[152:155], a[136:139], a[56:59], v[152:155]\n"
    "ds_read_b128 a[128:131], v177 offset:40960\n"
    "v_exp_f32 v143, v143\n"
    "v_mfma_f32_16x16x32_bf16 v[152:155], a[140:143], a[60:63], v[152:155]\n"
    "v_cvt_pk_bf16_f32 v167, v142, v143\n"
    "v_mul_f32 v144, s54, v144\n"
    "v_mfma_f32_16x16x32_bf16 v[156:159], a[144:147], a[48:51], v[212:215]\n"
    "ds_read_b128 a[132:135], v177 offset:41472\n"
    "v_mul_f32 v145, s54, v145\n"
    "v_mfma_f32_16x16x32_bf16 v[156:159], a[148:151], a[52:55], v[156:159]\n"
    "v_exp_f32 v144, v144\n"
    "v_mul_f32 v146, s54, v146\n"
    "v_mfma_f32_16x16x32_bf16 v[156:159], a[152:155], a[56:59], v[156:159]\n"
    "ds_read_b128 a[136:139], v177 offset:41984\n"
    "v_mul_f32 v147, s54, v147\n"
    "v_mfma_f32_16x16x32_bf16 v[156:159], a[156:159], a[60:63], v[156:159]\n"
    "v_exp_f32 v145, v145\n"
    "v_mfma_f32_16x16x32_bf16 v[0:3], a[160:163], v[160:163], v[0:3]\n"
    "ds_read_b128 a[140:143], v177 offset:42496\n"
    "v_cvt_pk_bf16_f32 v168, v144, v145\n"
    "v_mfma_f32_16x16x32_bf16 v[4:7], a[164:167], v[160:163], v[4:7]\n"
    "v_exp_f32 v146, v146\n"
    "v_mfma_f32_16x16x32_bf16 v[8:11], a[168:171], v[160:163], v[8:11]\n"
    "ds_read_b128 a[144:147], v177 offset:45056\n"
    "v_mul_f32 v148, s54, v148\n"
    "v_mfma_f32_16x16x32_bf16 v[12:15], a[172:175], v[160:163], v[12:15]\n"
    "v_exp_f32 v147, v147\n"
    "v_mul_f32 v149, s54, v149\n"
    "v_mfma_f32_16x16x32_bf16 v[16:19], a[176:179], v[160:163], v[16:19]\n"
    "ds_read_b128 a[148:151], v177 offset:45568\n"
    "v_cvt_pk_bf16_f32 v169, v146, v147\n"
    "v_mfma_f32_16x16x32_bf16 v[20:23], a[180:183], v[160:163], v[20:23]\n"
    "buffer_load_dwordx4 v183, s[40:43], s74 offen offset:2176 nt lds\n"
    "v_mul_f32 v150, s54, v150\n"
    "v_mfma_f32_16x16x32_bf16 v[24:27], a[184:187], v[160:163], v[24:27]\n"
    "ds_read_b128 a[152:155], v177 offset:46080\n"
    "v_mul_f32 v151, s54, v151\n"
    "v_mfma_f32_16x16x32_bf16 v[28:31], a[188:191], v[160:163], v[28:31]\n"
    "v_exp_f32 v148, v148\n"
    "v_mfma_f32_16x16x32_bf16 a[224:227], a[240:243], v[160:163], a[224:227]\n"
    "ds_read_b128 a[156:159], v177 offset:46592\n"
    "v_mfma_f32_16x16x32_bf16 v[32:35], a[160:163], v[164:167], v[32:35]\n"
    "v_exp_f32 v149, v149\n"
    "v_mfma_f32_16x16x32_bf16 v[36:39], a[164:167], v[164:167], v[36:39]\n"
    "v_exp_f32 v150, v150\n"
    "v_mfma_f32_16x16x32_bf16 v[40:43], a[168:171], v[164:167], v[40:43]\n"
    "v_exp_f32 v151, v151\n"
    "v_cvt_pk_bf16_f32 v170, v148, v149\n"
    "v_mfma_f32_16x16x32_bf16 v[44:47], a[172:175], v[164:167], v[44:47]\n"
    "v_cvt_pk_bf16_f32 v171, v150, v151\n"
    "v_mul_f32 v152, s54, v152\n"
    "v_mfma_f32_16x16x32_bf16 v[48:51], a[176:179], v[164:167], v[48:51]\n"
    "v_exp_f32 v152, v152\n"
    "v_mfma_f32_16x16x32_bf16 v[52:55], a[180:183], v[164:167], v[52:55]\n"
    "s_add_u32 m0, s52, 0x10000\n"
    "ds_read_b64_tr_b16 a[192:193], v179 offset:40960\n"
    "v_mul_f32 v153, s54, v153\n"
    "v_mfma_f32_16x16x32_bf16 v[56:59], a[184:187], v[164:167], v[56:59]\n"
    "buffer_load_dwordx4 v181, s[36:39], s75 offen offset:0 nt lds\n"
    "v_mul_f32 v154, s54, v154\n"
    "v_mfma_f32_16x16x32_bf16 v[60:63], a[188:191], v[164:167], v[60:63]\n"
    "s_waitcnt lgkmcnt(1)\n"
    "ds_read_b64_tr_b16 a[194:195], v179 offset:45056\n"
    "v_mul_f32 v155, s54, v155\n"
    "v_mfma_f32_16x16x32_bf16 a[228:231], a[240:243], v[164:167], a[228:231]\n"
    "v_exp_f32 v153, v153\n"
    "v_mfma_f32_16x16x32_bf16 v[128:131], a[128:131], a[0:3], v[248:251]\n"
    "v_exp_f32 v154, v154\n"
    "v_mfma_f32_16x16x32_bf16 v[128:131], a[132:135], a[4:7], v[128:131]\n"
    "v_exp_f32 v155, v155\n"
    "v_mfma_f32_16x16x32_bf16 v[128:131], a[136:139], a[8:11], v[128:131]\n"
    "ds_read_b64_tr_b16 a[196:197], v179 offset:41216\n"
    "v_cvt_pk_bf16_f32 v172, v152, v153\n"
    "v_mul_f32 v156, s54, v156\n"
    "v_mfma_f32_16x16x32_bf16 v[128:131], a[140:143], a[12:15], v[128:131]\n"
    "v_exp_f32 v156, v156\n"
    "v_mfma_f32_16x16x32_bf16 v[132:135], a[144:147], a[0:3], v[248:251]\n"
    "v_mul_f32 v157, s54, v157\n"
    "v_cvt_pk_bf16_f32 v173, v154, v155\n"
    "v_mfma_f32_16x16x32_bf16 v[132:135], a[148:151], a[4:7], v[132:135]\n"
    "v_exp_f32 v157, v157\n"
    "v_mfma_f32_16x16x32_bf16 v[132:135], a[152:155], a[8:11], v[132:135]\n"
    "ds_read_b64_tr_b16 a[198:199], v179 offset:45312\n"
    "v_mul_f32 v158, s54, v158\n"
    "v_mfma_f32_16x16x32_bf16 v[132:135], a[156:159], a[12:15], v[132:135]\n"
    "v_exp_f32 v158, v158\n"
    "v_mfma_f32_16x16x32_bf16 v[136:139], a[128:131], a[16:19], v[252:255]\n"
    "v_mul_f32 v159, s54, v159\n"
    "v_cvt_pk_bf16_f32 v174, v156, v157\n"
    "v_mfma_f32_16x16x32_bf16 v[136:139], a[132:135], a[20:23], v[136:139]\n"
    "buffer_load_dwordx4 v182, s[36:39], s75 offen offset:2048 nt lds\n"
    "v_mfma_f32_16x16x32_bf16 v[136:139], a[136:139], a[24:27], v[136:139]\n"
    "v_exp_f32 v159, v159\n"
    "v_mfma_f32_16x16x32_bf16 v[136:139], a[140:143], a[28:31], v[136:139]\n"
    "v_cvt_pk_bf16_f32 v175, v158, v159\n"
    "v_mul_f32 v128, s54, v128\n"
    "v_mfma_f32_16x16x32_bf16 v[140:143], a[144:147], a[16:19], v[252:255]\n"
    "ds_read_b64_tr_b16 a[200:201], v179 offset:41472\n"
    "v_mul_f32 v129, s54, v129\n"
    "v_mfma_f32_16x16x32_bf16 v[140:143], a[148:151], a[20:23], v[140:143]\n"
    "v_exp_f32 v128, v128\n"
    "v_mul_f32 v130, s54, v130\n"
    "v_mfma_f32_16x16x32_bf16 v[140:143], a[152:155], a[24:27], v[140:143]\n"
    "ds_read_b64_tr_b16 a[202:203], v179 offset:45568\n"
    "v_mul_f32 v131, s54, v131\n"
    "v_mfma_f32_16x16x32_bf16 v[140:143], a[156:159], a[28:31], v[140:143]\n"
    "v_exp_f32 v129, v129\n"
    "v_mfma_f32_16x16x32_bf16 v[64:67], a[160:163], v[168:171], v[64:67]\n"
    "ds_read_b64_tr_b16 a[204:205], v179 offset:41728\n"
    "v_cvt_pk_bf16_f32 v160, v128, v129\n"
    "v_mfma_f32_16x16x32_bf16 v[68:71], a[164:167], v[168:171], v[68:71]\n"
    "v_exp_f32 v130, v130\n"
    "v_mul_f32 v132, s54, v132\n"
    "v_mfma_f32_16x16x32_bf16 v[72:75], a[168:171], v[168:171], v[72:75]\n"
    "s_add_u32 m0, s52, 0x10380\n"
    "ds_read_b64_tr_b16 a[206:207], v179 offset:45824\n"
    "v_mul_f32 v133, s54, v133\n"
    "v_mfma_f32_16x16x32_bf16 v[76:79], a[172:175], v[168:171], v[76:79]\n"
    "buffer_load_dwordx4 v181, s[36:39], s75 offen offset:128 nt lds\n"
    "v_mul_f32 v134, s54, v134\n"
    "v_mfma_f32_16x16x32_bf16 v[80:83], a[176:179], v[168:171], v[80:83]\n"
    "ds_read_b64_tr_b16 a[208:209], v179 offset:41984\n"
    "v_mul_f32 v135, s54, v135\n"
    "v_mfma_f32_16x16x32_bf16 v[84:87], a[180:183], v[168:171], v[84:87]\n"
    "v_exp_f32 v131, v131\n"
    "v_mfma_f32_16x16x32_bf16 v[88:91], a[184:187], v[168:171], v[88:91]\n"
    "ds_read_b64_tr_b16 a[210:211], v179 offset:46080\n"
    "v_cvt_pk_bf16_f32 v161, v130, v131\n"
    "v_mfma_f32_16x16x32_bf16 v[92:95], a[188:191], v[168:171], v[92:95]\n"
    "v_exp_f32 v132, v132\n"
    "v_mfma_f32_16x16x32_bf16 a[232:235], a[240:243], v[168:171], a[232:235]\n"
    "ds_read_b64_tr_b16 a[212:213], v179 offset:42240\n"
    "v_mfma_f32_16x16x32_bf16 v[96:99], a[160:163], v[172:175], v[96:99]\n"
    "v_exp_f32 v133, v133\n"
    "v_mfma_f32_16x16x32_bf16 v[100:103], a[164:167], v[172:175], v[100:103]\n"
    "ds_read_b64_tr_b16 a[214:215], v179 offset:46336\n"
    "v_cvt_pk_bf16_f32 v162, v132, v133\n"
    "v_mfma_f32_16x16x32_bf16 v[104:107], a[168:171], v[172:175], v[104:107]\n"
    "v_exp_f32 v134, v134\n"
    "v_exp_f32 v135, v135\n"
    "v_mfma_f32_16x16x32_bf16 v[108:111], a[172:175], v[172:175], v[108:111]\n"
    "v_cvt_pk_bf16_f32 v163, v134, v135\n"
    "v_mul_f32 v136, s54, v136\n"
    "v_mfma_f32_16x16x32_bf16 v[112:115], a[176:179], v[172:175], v[112:115]\n"
    "v_exp_f32 v136, v136\n"
    "v_mfma_f32_16x16x32_bf16 v[116:119], a[180:183], v[172:175], v[116:119]\n"
    "ds_read_b64_tr_b16 a[216:217], v179 offset:42496\n"
    "v_mul_f32 v137, s54, v137\n"
    "v_mfma_f32_16x16x32_bf16 v[120:123], a[184:187], v[172:175], v[120:123]\n"
    "buffer_load_dwordx4 v182, s[36:39], s75 offen offset:2176 nt lds\n"
    "v_mul_f32 v138, s54, v138\n"
    "v_mfma_f32_16x16x32_bf16 v[124:127], a[188:191], v[172:175], v[124:127]\n"
    "v_exp_f32 v137, v137\n"
    "v_mfma_f32_16x16x32_bf16 a[236:239], a[240:243], v[172:175], a[236:239]\n"
    "v_exp_f32 v138, v138\n"
    "v_mfma_f32_16x16x32_bf16 v[144:147], a[128:131], a[32:35], v[196:199]\n"
    "ds_read_b64_tr_b16 a[218:219], v179 offset:46592\n"
    "v_mul_f32 v139, s54, v139\n"
    "v_mfma_f32_16x16x32_bf16 v[144:147], a[132:135], a[36:39], v[144:147]\n"
    "v_exp_f32 v139, v139\n"
    "v_mfma_f32_16x16x32_bf16 v[144:147], a[136:139], a[40:43], v[144:147]\n"
    "ds_read_b64_tr_b16 a[220:221], v179 offset:42752\n"
    "v_cvt_pk_bf16_f32 v164, v136, v137\n"
    "v_mul_f32 v140, s54, v140\n"
    "v_mfma_f32_16x16x32_bf16 v[144:147], a[140:143], a[44:47], v[144:147]\n"
    "v_exp_f32 v140, v140\n"
    "v_mfma_f32_16x16x32_bf16 v[148:151], a[144:147], a[32:35], v[196:199]\n"
    "ds_read_b64_tr_b16 a[222:223], v179 offset:46848\n"
    "v_mul_f32 v141, s54, v141\n"
    "v_mfma_f32_16x16x32_bf16 v[148:151], a[148:151], a[36:39], v[148:151]\n"
    "v_exp_f32 v141, v141\n"
    "v_mfma_f32_16x16x32_bf16 v[148:151], a[152:155], a[40:43], v[148:151]\n"
    "v_cvt_pk_bf16_f32 v165, v138, v139\n"
    "v_mul_f32 v142, s54, v142\n"
    "v_mfma_f32_16x16x32_bf16 v[148:151], a[156:159], a[44:47], v[148:151]\n"
    "v_exp_f32 v142, v142\n"
    "v_mfma_f32_16x16x32_bf16 v[152:155], a[128:131], a[48:51], v[212:215]\n"
    "s_waitcnt vmcnt(12) lgkmcnt(0)\n"
    "s_barrier\n"
    "v_mul_f32 v143, s54, v143\n"
    "v_cvt_pk_bf16_f32 v166, v140, v141\n"
    "v_mfma_f32_16x16x32_bf16 v[152:155], a[132:135], a[52:55], v[152:155]\n"
    "v_exp_f32 v143, v143\n"
    "v_mfma_f32_16x16x32_bf16 v[152:155], a[136:139], a[56:59], v[152:155]\n"
    "s_add_u32 m0, s52, 0x14000\n"
    "ds_read_b128 a[128:131], v176 offset:0\n"
    "v_cvt_pk_bf16_f32 v167, v142, v143\n"
    "v_mfma_f32_16x16x32_bf16 v[152:155], a[140:143], a[60:63], v[152:155]\n"
    "buffer_load_dwordx4 v183, s[40:43], s75 offen offset:0 nt lds\n"
    "v_mul_f32 v144, s54, v144\n"
    "v_mfma_f32_16x16x32_bf16 v[156:159], a[144:147], a[48:51], v[212:215]\n"
    "ds_read_b128 a[132:135], v176 offset:512\n"
    "v_mul_f32 v145, s54, v145\n"
    "v_mfma_f32_16x16x32_bf16 v[156:159], a[148:151], a[52:55], v[156:159]\n"
    "v_exp_f32 v144, v144\n"
    "v_mul_f32 v146, s54, v146\n"
    "v_mfma_f32_16x16x32_bf16 v[156:159], a[152:155], a[56:59], v[156:159]\n"
    "ds_read_b128 a[136:139], v176 offset:1024\n"
    "v_mul_f32 v147, s54, v147\n"
    "v_mfma_f32_16x16x32_bf16 v[156:159], a[156:159], a[60:63], v[156:159]\n"
    "v_exp_f32 v145, v145\n"
    "v_mfma_f32_16x16x32_bf16 v[0:3], a[192:195], v[160:163], v[0:3]\n"
    "ds_read_b128 a[140:143], v176 offset:1536\n"
    "v_cvt_pk_bf16_f32 v168, v144, v145\n"
    "v_mfma_f32_16x16x32_bf16 v[4:7], a[196:199], v[160:163], v[4:7]\n"
    "v_exp_f32 v146, v146\n"
    "v_mfma_f32_16x16x32_bf16 v[8:11], a[200:203], v[160:163], v[8:11]\n"
    "ds_read_b128 a[144:147], v176 offset:4096\n"
    "v_mul_f32 v148, s54, v148\n"
    "v_mfma_f32_16x16x32_bf16 v[12:15], a[204:207], v[160:163], v[12:15]\n"
    "v_exp_f32 v147, v147\n"
    "v_mul_f32 v149, s54, v149\n"
    "v_mfma_f32_16x16x32_bf16 v[16:19], a[208:211], v[160:163], v[16:19]\n"
    "ds_read_b128 a[148:151], v176 offset:4608\n"
    "v_cvt_pk_bf16_f32 v169, v146, v147\n"
    "v_mfma_f32_16x16x32_bf16 v[20:23], a[212:215], v[160:163], v[20:23]\n"
    "v_exp_f32 v148, v148\n"
    "v_mfma_f32_16x16x32_bf16 v[24:27], a[216:219], v[160:163], v[24:27]\n"
    "ds_read_b128 a[152:155], v176 offset:5120\n"
    "v_mul_f32 v150, s54, v150\n"
    "v_mfma_f32_16x16x32_bf16 v[28:31], a[220:223], v[160:163], v[28:31]\n"
    "buffer_load_dwordx4 v183, s[40:43], s75 offen offset:2048 nt lds\n"
    "v_mul_f32 v151, s54, v151\n"
    "v_mfma_f32_16x16x32_bf16 a[224:227], a[240:243], v[160:163], a[224:227]\n"
    "ds_read_b128 a[156:159], v176 offset:5632\n"
    "v_mfma_f32_16x16x32_bf16 v[32:35], a[192:195], v[164:167], v[32:35]\n"
    "v_exp_f32 v149, v149\n"
    "v_mfma_f32_16x16x32_bf16 v[36:39], a[196:199], v[164:167], v[36:39]\n"
    "v_exp_f32 v150, v150\n"
    "v_mfma_f32_16x16x32_bf16 v[40:43], a[200:203], v[164:167], v[40:43]\n"
    "v_exp_f32 v151, v151\n"
    "v_cvt_pk_bf16_f32 v170, v148, v149\n"
    "v_mfma_f32_16x16x32_bf16 v[44:47], a[204:207], v[164:167], v[44:47]\n"
    "v_cvt_pk_bf16_f32 v171, v150, v151\n"
    "v_mul_f32 v152, s54, v152\n"
    "v_mfma_f32_16x16x32_bf16 v[48:51], a[208:211], v[164:167], v[48:51]\n"
    "v_exp_f32 v152, v152\n"
    "v_mfma_f32_16x16x32_bf16 v[52:55], a[212:215], v[164:167], v[52:55]\n"
    "ds_read_b64_tr_b16 a[160:161], v178 offset:0\n"
    "v_mul_f32 v153, s54, v153\n"
    "v_mfma_f32_16x16x32_bf16 v[56:59], a[216:219], v[164:167], v[56:59]\n"
    "v_exp_f32 v153, v153\n"
    "v_mfma_f32_16x16x32_bf16 v[60:63], a[220:223], v[164:167], v[60:63]\n"
    "s_waitcnt lgkmcnt(1)\n"
    "s_add_u32 m0, s52, 0x14380\n"
    "ds_read_b64_tr_b16 a[162:163], v178 offset:4096\n"
    "v_mfma_f32_16x16x32_bf16 a[228:231], a[240:243], v[164:167], a[228:231]\n"
    "buffer_load_dwordx4 v183, s[40:43], s75 offen offset:128 nt lds\n"
    "v_mul_f32 v154, s54, v154\n"
    "v_mfma_f32_16x16x32_bf16 v[128:131], a[128:131], a[0:3], v[248:251]\n"
    "v_exp_f32 v154, v154\n"
    "v_mfma_f32_16x16x32_bf16 v[128:131], a[132:135], a[4:7], v[128:131]\n"
    "v_mul_f32 v155, s54, v155\n"
    "v_cvt_pk_bf16_f32 v172, v152, v153\n"
    "v_mfma_f32_16x16x32_bf16 v[128:131], a[136:139], a[8:11], v[128:131]\n"
    "ds_read_b64_tr_b16 a[164:165], v178 offset:256\n"
    "v_exp_f32 v155, v155\n"
    "v_mfma_f32_16x16x32_bf16 v[128:131], a[140:143], a[12:15], v[128:131]\n"
    "v_mul_f32 v156, s54, v156\n"
    "v_mul_f32 v157, s54, v157\n"
    "v_mfma_f32_16x16x32_bf16 v[132:135], a[144:147], a[0:3], v[248:251]\n"
    "v_exp_f32 v156, v156\n"
    "v_mfma_f32_16x16x32_bf16 v[132:135], a[148:151], a[4:7], v[132:135]\n"
    "v_exp_f32 v157, v157\n"
    "v_mfma_f32_16x16x32_bf16 v[132:135], a[152:155], a[8:11], v[132:135]\n"
    "ds_read_b64_tr_b16 a[166:167], v178 offset:4352\n"
    "v_cvt_pk_bf16_f32 v173, v154, v155\n"
    "v_mfma_f32_16x16x32_bf16 v[132:135], a[156:159], a[12:15], v[132:135]\n"
    "v_mul_f32 v158, s54, v158\n"
    "v_mul_f32 v159, s54, v159\n"
    "v_mfma_f32_16x16x32_bf16 v[136:139], a[128:131], a[16:19], v[252:255]\n"
    "v_exp_f32 v158, v158\n"
    "v_mfma_f32_16x16x32_bf16 v[136:139], a[132:135], a[20:23], v[136:139]\n"
    "v_exp_f32 v159, v159\n"
    "v_mfma_f32_16x16x32_bf16 v[136:139], a[136:139], a[24:27], v[136:139]\n"
    "v_cvt_pk_bf16_f32 v174, v156, v157\n"
    "v_cvt_pk_bf16_f32 v175, v158, v159\n"
    "v_mfma_f32_16x16x32_bf16 v[136:139], a[140:143], a[28:31], v[136:139]\n"
    "buffer_load_dwordx4 v183, s[40:43], s75 offen offset:2176 nt lds\n"
    "v_mul_f32 v128, s54, v128\n"
    "v_mfma_f32_16x16x32_bf16 v[140:143], a[144:147], a[16:19], v[252:255]\n"
    "ds_read_b64_tr_b16 a[168:169], v178 offset:512\n"
    "v_exp_f32 v128, v128\n"
    "v_mfma_f32_16x16x32_bf16 v[140:143], a[148:151], a[20:23], v[140:143]\n"
    "v_mul_f32 v129, s54, v129\n"
    "v_mul_f32 v130, s54, v130\n"
    "v_mfma_f32_16x16x32_bf16 v[140:143], a[152:155], a[24:27], v[140:143]\n"
    "ds_read_b64_tr_b16 a[170:171], v178 offset:4608\n"
    "v_mul_f32 v131, s54, v131\n"
    "v_mfma_f32_16x16x32_bf16 v[140:143], a[156:159], a[28:31], v[140:143]\n"
    "v_exp_f32 v129, v129\n"
    "v_mfma_f32_16x16x32_bf16 v[64:67], a[192:195], v[168:171], v[64:67]\n"
    "ds_read_b64_tr_b16 a[172:173], v178 offset:768\n"
    "v_exp_f32 v130, v130\n"
    "v_mfma_f32_16x16x32_bf16 v[68:71], a[196:199], v[168:171], v[68:71]\n"
    "v_exp_f32 v131, v131\n"
    "v_mfma_f32_16x16x32_bf16 v[72:75], a[200:203], v[168:171], v[72:75]\n"
    "ds_read_b64_tr_b16 a[174:175], v178 offset:4864\n"
    "v_cvt_pk_bf16_f32 v160, v128, v129\n"
    "v_mfma_f32_16x16x32_bf16 v[76:79], a[204:207], v[168:171], v[76:79]\n"
    "v_mul_f32 v132, s54, v132\n"
    "v_exp_f32 v132, v132\n"
    "v_mfma_f32_16x16x32_bf16 v[80:83], a[208:211], v[168:171], v[80:83]\n"
    "s_add_u32 s58, s53, 0x2000\n"
    "s_add_u32 m0, s52, 0x20000\n"
    "ds_read_b64_tr_b16 a[176:177], v178 offset:1024\n"
    "v_mfma_f32_16x16x32_bf16 v[84:87], a[212:215], v[168:171], v[84:87]\n"
    "buffer_load_dwordx4 v181, s[44:47], s58 offen offset:0 nt lds\n"
    "v_mul_f32 v133, s54, v133\n"
    "v_mfma_f32_16x16x32_bf16 v[88:91], a[216:219], v[168:171], v[88:91]\n"
    "ds_read_b64_tr_b16 a[178:179], v178 offset:5120\n"
    "v_exp_f32 v133, v133\n"
    "v_mfma_f32_16x16x32_bf16 v[92:95], a[220:223], v[168:171], v[92:95]\n"
    "v_cvt_pk_bf16_f32 v161, v130, v131\n"
    "v_mul_f32 v134, s54, v134\n"
    "v_mfma_f32_16x16x32_bf16 a[232:235], a[240:243], v[168:171], a[232:235]\n"
    "ds_read_b64_tr_b16 a[180:181], v178 offset:1280\n"
    "v_mul_f32 v135, s54, v135\n"
    "v_mfma_f32_16x16x32_bf16 v[96:99], a[192:195], v[172:175], v[96:99]\n"
    "v_exp_f32 v134, v134\n"
    "v_mfma_f32_16x16x32_bf16 v[100:103], a[196:199], v[172:175], v[100:103]\n"
    "ds_read_b64_tr_b16 a[182:183], v178 offset:5376\n"
    "v_exp_f32 v135, v135\n"
    "v_mfma_f32_16x16x32_bf16 v[104:107], a[200:203], v[172:175], v[104:107]\n"
    "v_cvt_pk_bf16_f32 v162, v132, v133\n"
    "v_cvt_pk_bf16_f32 v163, v134, v135\n"
    "v_mfma_f32_16x16x32_bf16 v[108:111], a[204:207], v[172:175], v[108:111]\n"
    "ds_read_b128 a[72:75], v180 offset:16384\n"
    "v_mul_f32 v136, s54, v136\n"
    "v_mfma_f32_16x16x32_bf16 v[112:115], a[208:211], v[172:175], v[112:115]\n"
    "v_exp_f32 v136, v136\n"
    "v_mul_f32 v137, s54, v137\n"
    "v_mfma_f32_16x16x32_bf16 v[116:119], a[212:215], v[172:175], v[116:119]\n"
    "ds_read_b64_tr_b16 a[184:185], v178 offset:1536\n"
    "v_mul_f32 v138, s54, v138\n"
    "v_mfma_f32_16x16x32_bf16 v[120:123], a[216:219], v[172:175], v[120:123]\n"
    "v_exp_f32 v137, v137\n"
    "v_mul_f32 v139, s54, v139\n"
    "v_mfma_f32_16x16x32_bf16 v[124:127], a[220:223], v[172:175], v[124:127]\n"
    "s_add_u32 m0, s52, 0x1fc00\n"
    "ds_read_b128 a[76:79], v180 offset:16896\n"
    "v_cvt_pk_bf16_f32 v164, v136, v137\n"
    "v_mfma_f32_16x16x32_bf16 a[236:239], a[240:243], v[172:175], a[236:239]\n"
    "buffer_load_dwordx4 v182, s[44:47], s58 offen offset:2048 nt lds\n"
    "v_mul_f32 v140, s54, v140\n"
    "v_mfma_f32_16x16x32_bf16 v[144:147], a[128:131], a[32:35], v[196:199]\n"
    "ds_read_b64_tr_b16 a[186:187], v178 offset:5632\n"
    "v_exp_f32 v138, v138\n"
    "v_mfma_f32_16x16x32_bf16 v[144:147], a[132:135], a[36:39], v[144:147]\n"
    "v_exp_f32 v139, v139\n"
    "v_mfma_f32_16x16x32_bf16 v[144:147], a[136:139], a[40:43], v[144:147]\n"
    "ds_read_b64_tr_b16 a[188:189], v178 offset:1792\n"
    "v_exp_f32 v140, v140\n"
    "v_mfma_f32_16x16x32_bf16 v[144:147], a[140:143], a[44:47], v[144:147]\n"
    "v_mul_f32 v141, s54, v141\n"
    "v_cvt_pk_bf16_f32 v165, v138, v139\n"
    "v_mfma_f32_16x16x32_bf16 v[148:151], a[144:147], a[32:35], v[196:199]\n"
    "ds_read_b64_tr_b16 a[190:191], v178 offset:5888\n"
    "v_exp_f32 v141, v141\n"
    "v_mfma_f32_16x16x32_bf16 v[148:151], a[148:151], a[36:39], v[148:151]\n"
    "v_mul_f32 v142, s54, v142\n"
    "v_mul_f32 v143, s54, v143\n"
    "v_mfma_f32_16x16x32_bf16 v[148:151], a[152:155], a[40:43], v[148:151]\n"
    "ds_read_b128 a[88:91], v180 offset:18432\n"
    "v_cvt_pk_bf16_f32 v166, v140, v141\n"
    "v_mfma_f32_16x16x32_bf16 v[148:151], a[156:159], a[44:47], v[148:151]\n"
    "v_exp_f32 v142, v142\n"
    "v_mfma_f32_16x16x32_bf16 v[152:155], a[128:131], a[48:51], v[212:215]\n"
    "s_waitcnt lgkmcnt(0)\n"
    "s_add_u32 s58, s53, 0x3000\n"
    "s_add_u32 m0, s52, 0x20800\n"
    "v_mfma_f32_16x16x32_bf16 v[152:155], a[132:135], a[52:55], v[152:155]\n"
    "buffer_load_dwordx4 v181, s[44:47], s58 offen offset:0 nt lds\n"
    "ds_read_b128 a[92:95], v180 offset:18944\n"
    "v_mfma_f32_16x16x32_bf16 v[152:155], a[136:139], a[56:59], v[152:155]\n"
    "ds_read_b128 a[128:131], v176 offset:8192\n"
    "v_exp_f32 v143, v143\n"
    "v_mfma_f32_16x16x32_bf16 v[152:155], a[140:143], a[60:63], v[152:155]\n"
    "v_cvt_pk_bf16_f32 v167, v142, v143\n"
    "v_mul_f32 v144, s54, v144\n"
    "v_mfma_f32_16x16x32_bf16 v[156:159], a[144:147], a[48:51], v[212:215]\n"
    "ds_read_b128 a[132:135], v176 offset:8704\n"
    "v_mul_f32 v145, s54, v145\n"
    "v_mfma_f32_16x16x32_bf16 v[156:159], a[148:151], a[52:55], v[156:159]\n"
    "v_exp_f32 v144, v144\n"
    "v_mul_f32 v146, s54, v146\n"
    "v_mfma_f32_16x16x32_bf16 v[156:159], a[152:155], a[56:59], v[156:159]\n"
    "ds_read_b128 a[136:139], v176 offset:9216\n"
    "v_mul_f32 v147, s54, v147\n"
    "v_mfma_f32_16x16x32_bf16 v[156:159], a[156:159], a[60:63], v[156:159]\n"
    "v_exp_f32 v145, v145\n"
    "v_mfma_f32_16x16x32_bf16 v[0:3], a[160:163], v[160:163], v[0:3]\n"
    "ds_read_b128 a[140:143], v176 offset:9728\n"
    "v_cvt_pk_bf16_f32 v168, v144, v145\n"
    "v_mfma_f32_16x16x32_bf16 v[4:7], a[164:167], v[160:163], v[4:7]\n"
    "v_exp_f32 v146, v146\n"
    "v_mfma_f32_16x16x32_bf16 v[8:11], a[168:171], v[160:163], v[8:11]\n"
    "ds_read_b128 a[144:147], v176 offset:12288\n"
    "v_mul_f32 v148, s54, v148\n"
    "v_mfma_f32_16x16x32_bf16 v[12:15], a[172:175], v[160:163], v[12:15]\n"
    "v_exp_f32 v147, v147\n"
    "v_mul_f32 v149, s54, v149\n"
    "v_mfma_f32_16x16x32_bf16 v[16:19], a[176:179], v[160:163], v[16:19]\n"
    "s_add_u32 m0, s52, 0x20400\n"
    "ds_read_b128 a[148:151], v176 offset:12800\n"
    "v_cvt_pk_bf16_f32 v169, v146, v147\n"
    "v_mfma_f32_16x16x32_bf16 v[20:23], a[180:183], v[160:163], v[20:23]\n"
    "buffer_load_dwordx4 v182, s[44:47], s58 offen offset:2048 nt lds\n"
    "v_mul_f32 v150, s54, v150\n"
    "v_mfma_f32_16x16x32_bf16 v[24:27], a[184:187], v[160:163], v[24:27]\n"
    "ds_read_b128 a[152:155], v176 offset:13312\n"
    "v_mul_f32 v151, s54, v151\n"
    "v_mfma_f32_16x16x32_bf16 v[28:31], a[188:191], v[160:163], v[28:31]\n"
    "v_exp_f32 v148, v148\n"
    "v_mfma_f32_16x16x32_bf16 a[224:227], a[240:243], v[160:163], a[224:227]\n"
    "ds_read_b128 a[156:159], v176 offset:13824\n"
    "v_mfma_f32_16x16x32_bf16 v[32:35], a[160:163], v[164:167], v[32:35]\n"
    "v_exp_f32 v149, v149\n"
    "v_mfma_f32_16x16x32_bf16 v[36:39], a[164:167], v[164:167], v[36:39]\n"
    "v_exp_f32 v150, v150\n"
    "v_mfma_f32_16x16x32_bf16 v[40:43], a[168:171], v[164:167], v[40:43]\n"
    "v_exp_f32 v151, v151\n"
    "v_cvt_pk_bf16_f32 v170, v148, v149\n"
    "v_mfma_f32_16x16x32_bf16 v[44:47], a[172:175], v[164:167], v[44:47]\n"
    "v_cvt_pk_bf16_f32 v171, v150, v151\n"
    "v_mul_f32 v152, s54, v152\n"
    "v_mfma_f32_16x16x32_bf16 v[48:51], a[176:179], v[164:167], v[48:51]\n"
    "v_exp_f32 v152, v152\n"
    "v_mfma_f32_16x16x32_bf16 v[52:55], a[180:183], v[164:167], v[52:55]\n"
    "s_add_u32 m0, s52, 0x18000\n"
    "ds_read_b64_tr_b16 a[192:193], v178 offset:8192\n"
    "v_mul_f32 v153, s54, v153\n"
    "v_mfma_f32_16x16x32_bf16 v[56:59], a[184:187], v[164:167], v[56:59]\n"
    "buffer_load_dwordx4 v181, s[36:39], s76 offen offset:0 nt lds\n"
    "v_mul_f32 v154, s54, v154\n"
    "v_mfma_f32_16x16x32_bf16 v[60:63], a[188:191], v[164:167], v[60:63]\n"
    "s_waitcnt lgkmcnt(1)\n"
    "ds_read_b64_tr_b16 a[194:195], v178 offset:12288\n"
    "v_mul_f32 v155, s54, v155\n"
    "v_mfma_f32_16x16x32_bf16 a[228:231], a[240:243], v[164:167], a[228:231]\n"
    "v_exp_f32 v153, v153\n"
    "v_mfma_f32_16x16x32_bf16 v[128:131], a[128:131], a[0:3], v[248:251]\n"
    "v_exp_f32 v154, v154\n"
    "v_mfma_f32_16x16x32_bf16 v[128:131], a[132:135], a[4:7], v[128:131]\n"
    "v_exp_f32 v155, v155\n"
    "v_mfma_f32_16x16x32_bf16 v[128:131], a[136:139], a[8:11], v[128:131]\n"
    "ds_read_b64_tr_b16 a[196:197], v178 offset:8448\n"
    "v_cvt_pk_bf16_f32 v172, v152, v153\n"
    "v_mul_f32 v156, s54, v156\n"
    "v_mfma_f32_16x16x32_bf16 v[128:131], a[140:143], a[12:15], v[128:131]\n"
    "v_exp_f32 v156, v156\n"
    "v_mfma_f32_16x16x32_bf16 v[132:135], a[144:147], a[0:3], v[248:251]\n"
    "v_mul_f32 v157, s54, v157\n"
    "v_cvt_pk_bf16_f32 v173, v154, v155\n"
    "v_mfma_f32_16x16x32_bf16 v[132:135], a[148:151], a[4:7], v[132:135]\n"
    "v_exp_f32 v157, v157\n"
    "v_mfma_f32_16x16x32_bf16 v[132:135], a[152:155], a[8:11], v[132:135]\n"
    "ds_read_b64_tr_b16 a[198:199], v178 offset:12544\n"
    "v_mul_f32 v158, s54, v158\n"
    "v_mfma_f32_16x16x32_bf16 v[132:135], a[156:159], a[12:15], v[132:135]\n"
    "v_exp_f32 v158, v158\n"
    "v_mfma_f32_16x16x32_bf16 v[136:139], a[128:131], a[16:19], v[252:255]\n"
    "v_mul_f32 v159, s54, v159\n"
    "v_cvt_pk_bf16_f32 v174, v156, v157\n"
    "v_mfma_f32_16x16x32_bf16 v[136:139], a[132:135], a[20:23], v[136:139]\n"
    "buffer_load_dwordx4 v182, s[36:39], s76 offen offset:2048 nt lds\n"
    "v_mfma_f32_16x16x32_bf16 v[136:139], a[136:139], a[24:27], v[136:139]\n"
    "v_exp_f32 v159, v159\n"
    "v_mfma_f32_16x16x32_bf16 v[136:139], a[140:143], a[28:31], v[136:139]\n"
    "v_cvt_pk_bf16_f32 v175, v158, v159\n"
    "v_mul_f32 v128, s54, v128\n"
    "v_mfma_f32_16x16x32_bf16 v[140:143], a[144:147], a[16:19], v[252:255]\n"
    "ds_read_b64_tr_b16 a[200:201], v178 offset:8704\n"
    "v_mul_f32 v129, s54, v129\n"
    "v_mfma_f32_16x16x32_bf16 v[140:143], a[148:151], a[20:23], v[140:143]\n"
    "v_exp_f32 v128, v128\n"
    "v_mul_f32 v130, s54, v130\n"
    "v_mfma_f32_16x16x32_bf16 v[140:143], a[152:155], a[24:27], v[140:143]\n"
    "ds_read_b64_tr_b16 a[202:203], v178 offset:12800\n"
    "v_mul_f32 v131, s54, v131\n"
    "v_mfma_f32_16x16x32_bf16 v[140:143], a[156:159], a[28:31], v[140:143]\n"
    "v_exp_f32 v129, v129\n"
    "v_mfma_f32_16x16x32_bf16 v[64:67], a[160:163], v[168:171], v[64:67]\n"
    "ds_read_b64_tr_b16 a[204:205], v178 offset:8960\n"
    "v_cvt_pk_bf16_f32 v160, v128, v129\n"
    "v_mfma_f32_16x16x32_bf16 v[68:71], a[164:167], v[168:171], v[68:71]\n"
    "v_exp_f32 v130, v130\n"
    "v_mul_f32 v132, s54, v132\n"
    "v_mfma_f32_16x16x32_bf16 v[72:75], a[168:171], v[168:171], v[72:75]\n"
    "s_add_u32 m0, s52, 0x18380\n"
    "ds_read_b64_tr_b16 a[206:207], v178 offset:13056\n"
    "v_mul_f32 v133, s54, v133\n"
    "v_mfma_f32_16x16x32_bf16 v[76:79], a[172:175], v[168:171], v[76:79]\n"
    "buffer_load_dwordx4 v181, s[36:39], s76 offen offset:128 nt lds\n"
    "v_mul_f32 v134, s54, v134\n"
    "v_mfma_f32_16x16x32_bf16 v[80:83], a[176:179], v[168:171], v[80:83]\n"
    "ds_read_b64_tr_b16 a[208:209], v178 offset:9216\n"
    "v_mul_f32 v135, s54, v135\n"
    "v_mfma_f32_16x16x32_bf16 v[84:87], a[180:183], v[168:171], v[84:87]\n"
    "v_exp_f32 v131, v131\n"
    "v_mfma_f32_16x16x32_bf16 v[88:91], a[184:187], v[168:171], v[88:91]\n"
    "ds_read_b64_tr_b16 a[210:211], v178 offset:13312\n"
    "v_cvt_pk_bf16_f32 v161, v130, v131\n"
    "v_mfma_f32_16x16x32_bf16 v[92:95], a[188:191], v[168:171], v[92:95]\n"
    "v_exp_f32 v132, v132\n"
    "v_mfma_f32_16x16x32_bf16 a[232:235], a[240:243], v[168:171], a[232:235]\n"
    "ds_read_b64_tr_b16 a[212:213], v178 offset:9472\n"
    "v_mfma_f32_16x16x32_bf16 v[96:99], a[160:163], v[172:175], v[96:99]\n"
    "v_exp_f32 v133, v133\n"
    "v_mfma_f32_16x16x32_bf16 v[100:103], a[164:167], v[172:175], v[100:103]\n"
    "ds_read_b64_tr_b16 a[214:215], v178 offset:13568\n"
    "v_cvt_pk_bf16_f32 v162, v132, v133\n"
    "v_mfma_f32_16x16x32_bf16 v[104:107], a[168:171], v[172:175], v[104:107]\n"
    "v_exp_f32 v134, v134\n"
    "v_exp_f32 v135, v135\n"
    "v_mfma_f32_16x16x32_bf16 v[108:111], a[172:175], v[172:175], v[108:111]\n"
    "v_cvt_pk_bf16_f32 v163, v134, v135\n"
    "v_mul_f32 v136, s54, v136\n"
    "v_mfma_f32_16x16x32_bf16 v[112:115], a[176:179], v[172:175], v[112:115]\n"
    "v_exp_f32 v136, v136\n"
    "v_mfma_f32_16x16x32_bf16 v[116:119], a[180:183], v[172:175], v[116:119]\n"
    "ds_read_b64_tr_b16 a[216:217], v178 offset:9728\n"
    "v_mul_f32 v137, s54, v137\n"
    "v_mfma_f32_16x16x32_bf16 v[120:123], a[184:187], v[172:175], v[120:123]\n"
    "buffer_load_dwordx4 v182, s[36:39], s76 offen offset:2176 nt lds\n"
    "v_mul_f32 v138, s54, v138\n"
    "v_mfma_f32_16x16x32_bf16 v[124:127], a[188:191], v[172:175], v[124:127]\n"
    "v_exp_f32 v137, v137\n"
    "v_mfma_f32_16x16x32_bf16 a[236:239], a[240:243], v[172:175], a[236:239]\n"
    "v_exp_f32 v138, v138\n"
    "v_mfma_f32_16x16x32_bf16 v[144:147], a[128:131], a[32:35], v[196:199]\n"
    "ds_read_b64_tr_b16 a[218:219], v178 offset:13824\n"
    "v_mul_f32 v139, s54, v139\n"
    "v_mfma_f32_16x16x32_bf16 v[144:147], a[132:135], a[36:39], v[144:147]\n"
    "v_exp_f32 v139, v139\n"
    "v_mfma_f32_16x16x32_bf16 v[144:147], a[136:139], a[40:43], v[144:147]\n"
    "ds_read_b64_tr_b16 a[220:221], v178 offset:9984\n"
    "v_cvt_pk_bf16_f32 v164, v136, v137\n"
    "v_mul_f32 v140, s54, v140\n"
    "v_mfma_f32_16x16x32_bf16 v[144:147], a[140:143], a[44:47], v[144:147]\n"
    "v_exp_f32 v140, v140\n"
    "v_mfma_f32_16x16x32_bf16 v[148:151], a[144:147], a[32:35], v[196:199]\n"
    "ds_read_b64_tr_b16 a[222:223], v178 offset:14080\n"
    "v_mul_f32 v141, s54, v141\n"
    "v_mfma_f32_16x16x32_bf16 v[148:151], a[148:151], a[36:39], v[148:151]\n"
    "v_exp_f32 v141, v141\n"
    "v_mfma_f32_16x16x32_bf16 v[148:151], a[152:155], a[40:43], v[148:151]\n"
    "v_cvt_pk_bf16_f32 v165, v138, v139\n"
    "v_mul_f32 v142, s54, v142\n"
    "v_mfma_f32_16x16x32_bf16 v[148:151], a[156:159], a[44:47], v[148:151]\n"
    "v_exp_f32 v142, v142\n"
    "v_mfma_f32_16x16x32_bf16 v[152:155], a[128:131], a[48:51], v[212:215]\n"
    "s_waitcnt vmcnt(16) lgkmcnt(0)\n"
    "s_barrier\n"
    "v_mul_f32 v143, s54, v143\n"
    "v_cvt_pk_bf16_f32 v166, v140, v141\n"
    "v_mfma_f32_16x16x32_bf16 v[152:155], a[132:135], a[52:55], v[152:155]\n"
    "v_exp_f32 v143, v143\n"
    "v_mfma_f32_16x16x32_bf16 v[152:155], a[136:139], a[56:59], v[152:155]\n"
    "s_add_u32 m0, s52, 0x1c000\n"
    "ds_read_b128 a[128:131], v176 offset:32768\n"
    "v_cvt_pk_bf16_f32 v167, v142, v143\n"
    "v_mfma_f32_16x16x32_bf16 v[152:155], a[140:143], a[60:63], v[152:155]\n"
    "buffer_load_dwordx4 v183, s[40:43], s76 offen offset:0 nt lds\n"
    "v_mul_f32 v144, s54, v144\n"
    "v_mfma_f32_16x16x32_bf16 v[156:159], a[144:147], a[48:51], v[212:215]\n"
    "ds_read_b128 a[132:135], v176 offset:33280\n"
    "v_mul_f32 v145, s54, v145\n"
    "v_mfma_f32_16x16x32_bf16 v[156:159], a[148:151], a[52:55], v[156:159]\n"
    "v_exp_f32 v144, v144\n"
    "v_mul_f32 v146, s54, v146\n"
    "v_mfma_f32_16x16x32_bf16 v[156:159], a[152:155], a[56:59], v[156:159]\n"
    "ds_read_b128 a[136:139], v176 offset:33792\n"
    "v_mul_f32 v147, s54, v147\n"
    "v_mfma_f32_16x16x32_bf16 v[156:159], a[156:159], a[60:63], v[156:159]\n"
    "v_exp_f32 v145, v145\n"
    "v_mfma_f32_16x16x32_bf16 v[0:3], a[192:195], v[160:163], v[0:3]\n"
    "ds_read_b128 a[140:143], v176 offset:34304\n"
    "v_cvt_pk_bf16_f32 v168, v144, v145\n"
    "v_mfma_f32_16x16x32_bf16 v[4:7], a[196:199], v[160:163], v[4:7]\n"
    "v_exp_f32 v146, v146\n"
    "v_mfma_f32_16x16x32_bf16 v[8:11], a[200:203], v[160:163], v[8:11]\n"
    "ds_read_b128 a[144:147], v176 offset:36864\n"
    "v_mul_f32 v148, s54, v148\n"
    "v_mfma_f32_16x16x32_bf16 v[12:15], a[204:207], v[160:163], v[12:15]\n"
    "v_exp_f32 v147, v147\n"
    "v_mul_f32 v149, s54, v149\n"
    "v_mfma_f32_16x16x32_bf16 v[16:19], a[208:211], v[160:163], v[16:19]\n"
    "ds_read_b128 a[148:151], v176 offset:37376\n"
    "v_cvt_pk_bf16_f32 v169, v146, v147\n"
    "v_mfma_f32_16x16x32_bf16 v[20:23], a[212:215], v[160:163], v[20:23]\n"
    "v_exp_f32 v148, v148\n"
    "v_mfma_f32_16x16x32_bf16 v[24:27], a[216:219], v[160:163], v[24:27]\n"
    "ds_read_b128 a[152:155], v176 offset:37888\n"
    "v_mul_f32 v150, s54, v150\n"
    "v_mfma_f32_16x16x32_bf16 v[28:31], a[220:223], v[160:163], v[28:31]\n"
    "buffer_load_dwordx4 v183, s[40:43], s76 offen offset:2048 nt lds\n"
    "v_mul_f32 v151, s54, v151\n"
    "v_mfma_f32_16x16x32_bf16 a[224:227], a[240:243], v[160:163], a[224:227]\n"
    "ds_read_b128 a[156:159], v176 offset:38400\n"
    "v_mfma_f32_16x16x32_bf16 v[32:35], a[192:195], v[164:167], v[32:35]\n"
    "v_exp_f32 v149, v149\n"
    "v_mfma_f32_16x16x32_bf16 v[36:39], a[196:199], v[164:167], v[36:39]\n"
    "v_exp_f32 v150, v150\n"
    "v_mfma_f32_16x16x32_bf16 v[40:43], a[200:203], v[164:167], v[40:43]\n"
    "v_exp_f32 v151, v151\n"
    "v_cvt_pk_bf16_f32 v170, v148, v149\n"
    "v_mfma_f32_16x16x32_bf16 v[44:47], a[204:207], v[164:167], v[44:47]\n"
    "v_cvt_pk_bf16_f32 v171, v150, v151\n"
    "v_mul_f32 v152, s54, v152\n"
    "v_mfma_f32_16x16x32_bf16 v[48:51], a[208:211], v[164:167], v[48:51]\n"
    "v_exp_f32 v152, v152\n"
    "v_mfma_f32_16x16x32_bf16 v[52:55], a[212:215], v[164:167], v[52:55]\n"
    "ds_read_b64_tr_b16 a[160:161], v178 offset:32768\n"
    "v_mul_f32 v153, s54, v153\n"
    "v_mfma_f32_16x16x32_bf16 v[56:59], a[216:219], v[164:167], v[56:59]\n"
    "v_exp_f32 v153, v153\n"
    "v_mfma_f32_16x16x32_bf16 v[60:63], a[220:223], v[164:167], v[60:63]\n"
    "s_waitcnt lgkmcnt(1)\n"
    "s_add_u32 m0, s52, 0x1c380\n"
    "ds_read_b64_tr_b16 a[162:163], v178 offset:36864\n"
    "v_mfma_f32_16x16x32_bf16 a[228:231], a[240:243], v[164:167], a[228:231]\n"
    "buffer_load_dwordx4 v183, s[40:43], s76 offen offset:128 nt lds\n"
    "v_mul_f32 v154, s54, v154\n"
    "v_mfma_f32_16x16x32_bf16 v[128:131], a[128:131], a[0:3], v[248:251]\n"
    "v_exp_f32 v154, v154\n"
    "v_mfma_f32_16x16x32_bf16 v[128:131], a[132:135], a[4:7], v[128:131]\n"
    "v_mul_f32 v155, s54, v155\n"
    "v_cvt_pk_bf16_f32 v172, v152, v153\n"
    "v_mfma_f32_16x16x32_bf16 v[128:131], a[136:139], a[8:11], v[128:131]\n"
    "ds_read_b64_tr_b16 a[164:165], v178 offset:33024\n"
    "v_exp_f32 v155, v155\n"
    "v_mfma_f32_16x16x32_bf16 v[128:131], a[140:143], a[12:15], v[128:131]\n"
    "v_mul_f32 v156, s54, v156\n"
    "v_mul_f32 v157, s54, v157\n"
    "v_mfma_f32_16x16x32_bf16 v[132:135], a[144:147], a[0:3], v[248:251]\n"
    "v_exp_f32 v156, v156\n"
    "v_mfma_f32_16x16x32_bf16 v[132:135], a[148:151], a[4:7], v[132:135]\n"
    "v_exp_f32 v157, v157\n"
    "v_mfma_f32_16x16x32_bf16 v[132:135], a[152:155], a[8:11], v[132:135]\n"
    "ds_read_b64_tr_b16 a[166:167], v178 offset:37120\n"
    "v_cvt_pk_bf16_f32 v173, v154, v155\n"
    "v_mfma_f32_16x16x32_bf16 v[132:135], a[156:159], a[12:15], v[132:135]\n"
    "v_mul_f32 v158, s54, v158\n"
    "v_mul_f32 v159, s54, v159\n"
    "v_mfma_f32_16x16x32_bf16 v[136:139], a[128:131], a[16:19], v[252:255]\n"
    "v_exp_f32 v158, v158\n"
    "v_mfma_f32_16x16x32_bf16 v[136:139], a[132:135], a[20:23], v[136:139]\n"
    "v_exp_f32 v159, v159\n"
    "v_mfma_f32_16x16x32_bf16 v[136:139], a[136:139], a[24:27], v[136:139]\n"
    "v_cvt_pk_bf16_f32 v174, v156, v157\n"
    "v_cvt_pk_bf16_f32 v175, v158, v159\n"
    "v_mfma_f32_16x16x32_bf16 v[136:139], a[140:143], a[28:31], v[136:139]\n"
    "buffer_load_dwordx4 v183, s[40:43], s76 offen offset:2176 nt lds\n"
    "v_mul_f32 v128, s54, v128\n"
    "v_mfma_f32_16x16x32_bf16 v[140:143], a[144:147], a[16:19], v[252:255]\n"
    "ds_read_b64_tr_b16 a[168:169], v178 offset:33280\n"
    "v_exp_f32 v128, v128\n"
    "v_mfma_f32_16x16x32_bf16 v[140:143], a[148:151], a[20:23], v[140:143]\n"
    "v_mul_f32 v129, s54, v129\n"
    "v_mul_f32 v130, s54, v130\n"
    "v_mfma_f32_16x16x32_bf16 v[140:143], a[152:155], a[24:27], v[140:143]\n"
    "ds_read_b64_tr_b16 a[170:171], v178 offset:37376\n"
    "v_mul_f32 v131, s54, v131\n"
    "v_mfma_f32_16x16x32_bf16 v[140:143], a[156:159], a[28:31], v[140:143]\n"
    "v_exp_f32 v129, v129\n"
    "v_mfma_f32_16x16x32_bf16 v[64:67], a[192:195], v[168:171], v[64:67]\n"
    "ds_read_b64_tr_b16 a[172:173], v178 offset:33536\n"
    "v_exp_f32 v130, v130\n"
    "v_mfma_f32_16x16x32_bf16 v[68:71], a[196:199], v[168:171], v[68:71]\n"
    "v_exp_f32 v131, v131\n"
    "v_mfma_f32_16x16x32_bf16 v[72:75], a[200:203], v[168:171], v[72:75]\n"
    "ds_read_b64_tr_b16 a[174:175], v178 offset:37632\n"
    "v_cvt_pk_bf16_f32 v160, v128, v129\n"
    "v_mfma_f32_16x16x32_bf16 v[76:79], a[204:207], v[168:171], v[76:79]\n"
    "v_mul_f32 v132, s54, v132\n"
    "v_exp_f32 v132, v132\n"
    "v_mfma_f32_16x16x32_bf16 v[80:83], a[208:211], v[168:171], v[80:83]\n"
    "s_add_u32 s58, s53, 0x2080\n"
    "s_add_u32 m0, s52, 0x24000\n"
    "ds_read_b64_tr_b16 a[176:177], v178 offset:33792\n"
    "v_mfma_f32_16x16x32_bf16 v[84:87], a[212:215], v[168:171], v[84:87]\n"
    "buffer_load_dwordx4 v181, s[44:47], s58 offen offset:0 nt lds\n"
    "v_mul_f32 v133, s54, v133\n"
    "v_mfma_f32_16x16x32_bf16 v[88:91], a[216:219], v[168:171], v[88:91]\n"
    "ds_read_b64_tr_b16 a[178:179], v178 offset:37888\n"
    "v_exp_f32 v133, v133\n"
    "v_mfma_f32_16x16x32_bf16 v[92:95], a[220:223], v[168:171], v[92:95]\n"
    "v_cvt_pk_bf16_f32 v161, v130, v131\n"
    "v_mul_f32 v134, s54, v134\n"
    "v_mfma_f32_16x16x32_bf16 a[232:235], a[240:243], v[168:171], a[232:235]\n"
    "ds_read_b64_tr_b16 a[180:181], v178 offset:34048\n"
    "v_mul_f32 v135, s54, v135\n"
    "v_mfma_f32_16x16x32_bf16 v[96:99], a[192:195], v[172:175], v[96:99]\n"
    "v_exp_f32 v134, v134\n"
    "v_mfma_f32_16x16x32_bf16 v[100:103], a[196:199], v[172:175], v[100:103]\n"
    "ds_read_b64_tr_b16 a[182:183], v178 offset:38144\n"
    "v_exp_f32 v135, v135\n"
    "v_mfma_f32_16x16x32_bf16 v[104:107], a[200:203], v[172:175], v[104:107]\n"
    "v_cvt_pk_bf16_f32 v162, v132, v133\n"
    "v_cvt_pk_bf16_f32 v163, v134, v135\n"
    "v_mfma_f32_16x16x32_bf16 v[108:111], a[204:207], v[172:175], v[108:111]\n"
    "v_mul_f32 v136, s54, v136\n"
    "v_mul_f32 v137, s54, v137\n"
    "v_mfma_f32_16x16x32_bf16 v[112:115], a[208:211], v[172:175], v[112:115]\n"
    "v_exp_f32 v136, v136\n"
    "v_mfma_f32_16x16x32_bf16 v[116:119], a[212:215], v[172:175], v[116:119]\n"
    "ds_read_b64_tr_b16 a[184:185], v178 offset:34304\n"
    "v_exp_f32 v137, v137\n"
    "v_mfma_f32_16x16x32_bf16 v[120:123], a[216:219], v[172:175], v[120:123]\n"
    "v_mul_f32 v138, s54, v138\n"
    "v_mul_f32 v139, s54, v139\n"
    "v_mfma_f32_16x16x32_bf16 v[124:127], a[220:223], v[172:175], v[124:127]\n"
    "s_add_u32 m0, s52, 0x23c00\n"
    "v_exp_f32 v138, v138\n"
    "v_mfma_f32_16x16x32_bf16 a[236:239], a[240:243], v[172:175], a[236:239]\n"
    "buffer_load_dwordx4 v182, s[44:47], s58 offen offset:2048 nt lds\n"
    "v_cvt_pk_bf16_f32 v164, v136, v137\n"
    "v_mfma_f32_16x16x32_bf16 v[144:147], a[128:131], a[32:35], v[196:199]\n"
    "ds_read_b64_tr_b16 a[186:187], v178 offset:38400\n"
    "v_mul_f32 v140, s54, v140\n"
    "v_mfma_f32_16x16x32_bf16 v[144:147], a[132:135], a[36:39], v[144:147]\n"
    "v_exp_f32 v139, v139\n"
    "v_mfma_f32_16x16x32_bf16 v[144:147], a[136:139], a[40:43], v[144:147]\n"
    "ds_read_b64_tr_b16 a[188:189], v178 offset:34560\n"
    "v_exp_f32 v140, v140\n"
    "v_mfma_f32_16x16x32_bf16 v[144:147], a[140:143], a[44:47], v[144:147]\n"
    "v_mul_f32 v141, s54, v141\n"
    "v_cvt_pk_bf16_f32 v165, v138, v139\n"
    "v_mfma_f32_16x16x32_bf16 v[148:151], a[144:147], a[32:35], v[196:199]\n"
    "ds_read_b64_tr_b16 a[190:191], v178 offset:38656\n"
    "v_mul_f32 v142, s54, v142\n"
    "v_mfma_f32_16x16x32_bf16 v[148:151], a[148:151], a[36:39], v[148:151]\n"
    "v_exp_f32 v141, v141\n"
    "v_mfma_f32_16x16x32_bf16 v[148:151], a[152:155], a[40:43], v[148:151]\n"
    "v_exp_f32 v142, v142\n"
    "v_mfma_f32_16x16x32_bf16 v[148:151], a[156:159], a[44:47], v[148:151]\n"
    "v_mul_f32 v143, s54, v143\n"
    "v_cvt_pk_bf16_f32 v166, v140, v141\n"
    "v_mfma_f32_16x16x32_bf16 v[152:155], a[128:131], a[48:51], v[212:215]\n"
    "s_waitcnt lgkmcnt(0)\n"
    "s_add_u32 s58, s53, 0x3080\n"
    "s_add_u32 m0, s52, 0x24800\n"
    "v_mfma_f32_16x16x32_bf16 v[152:155], a[132:135], a[52:55], v[152:155]\n"
    "buffer_load_dwordx4 v181, s[44:47], s58 offen offset:0 nt lds\n"
    "v_mfma_f32_16x16x32_bf16 v[152:155], a[136:139], a[56:59], v[152:155]\n"
    "ds_read_b128 a[128:131], v176 offset:40960\n"
    "v_exp_f32 v143, v143\n"
    "v_mfma_f32_16x16x32_bf16 v[152:155], a[140:143], a[60:63], v[152:155]\n"
    "v_cvt_pk_bf16_f32 v167, v142, v143\n"
    "v_mul_f32 v144, s54, v144\n"
    "v_mfma_f32_16x16x32_bf16 v[156:159], a[144:147], a[48:51], v[212:215]\n"
    "ds_read_b128 a[132:135], v176 offset:41472\n"
    "v_mul_f32 v145, s54, v145\n"
    "v_mfma_f32_16x16x32_bf16 v[156:159], a[148:151], a[52:55], v[156:159]\n"
    "v_exp_f32 v144, v144\n"
    "v_mul_f32 v146, s54, v146\n"
    "v_mfma_f32_16x16x32_bf16 v[156:159], a[152:155], a[56:59], v[156:159]\n"
    "ds_read_b128 a[136:139], v176 offset:41984\n"
    "v_mul_f32 v147, s54, v147\n"
    "v_mfma_f32_16x16x32_bf16 v[156:159], a[156:159], a[60:63], v[156:159]\n"
    "v_exp_f32 v145, v145\n"
    "v_mfma_f32_16x16x32_bf16 v[0:3], a[160:163], v[160:163], v[0:3]\n"
    "ds_read_b128 a[140:143], v176 offset:42496\n"
    "v_cvt_pk_bf16_f32 v168, v144, v145\n"
    "v_mfma_f32_16x16x32_bf16 v[4:7], a[164:167], v[160:163], v[4:7]\n"
    "v_exp_f32 v146, v146\n"
    "v_mfma_f32_16x16x32_bf16 v[8:11], a[168:171], v[160:163], v[8:11]\n"
    "ds_read_b128 a[144:147], v176 offset:45056\n"
    "v_mul_f32 v148, s54, v148\n"
    "v_mfma_f32_16x16x32_bf16 v[12:15], a[172:175], v[160:163], v[12:15]\n"
    "v_exp_f32 v147, v147\n"
    "v_mul_f32 v149, s54, v149\n"
    "v_mfma_f32_16x16x32_bf16 v[16:19], a[176:179], v[160:163], v[16:19]\n"
    "s_add_u32 m0, s52, 0x24400\n"
    "ds_read_b128 a[148:151], v176 offset:45568\n"
    "v_cvt_pk_bf16_f32 v169, v146, v147\n"
    "v_mfma_f32_16x16x32_bf16 v[20:23], a[180:183], v[160:163], v[20:23]\n"
    "buffer_load_dwordx4 v182, s[44:47], s58 offen offset:2048 nt lds\n"
    "v_mul_f32 v150, s54, v150\n"
    "v_mfma_f32_16x16x32_bf16 v[24:27], a[184:187], v[160:163], v[24:27]\n"
    "ds_read_b128 a[152:155], v176 offset:46080\n"
    "v_mul_f32 v151, s54, v151\n"
    "v_mfma_f32_16x16x32_bf16 v[28:31], a[188:191], v[160:163], v[28:31]\n"
    "v_exp_f32 v148, v148\n"
    "v_mfma_f32_16x16x32_bf16 a[224:227], a[240:243], v[160:163], a[224:227]\n"
    "ds_read_b128 a[156:159], v176 offset:46592\n"
    "v_mfma_f32_16x16x32_bf16 v[32:35], a[160:163], v[164:167], v[32:35]\n"
    "v_exp_f32 v149, v149\n"
    "v_mfma_f32_16x16x32_bf16 v[36:39], a[164:167], v[164:167], v[36:39]\n"
    "v_exp_f32 v150, v150\n"
    "v_mfma_f32_16x16x32_bf16 v[40:43], a[168:171], v[164:167], v[40:43]\n"
    "v_exp_f32 v151, v151\n"
    "v_cvt_pk_bf16_f32 v170, v148, v149\n"
    "v_mfma_f32_16x16x32_bf16 v[44:47], a[172:175], v[164:167], v[44:47]\n"
    "s_add_u32 s36, s36, 0x2000000\n"
    "s_addc_u32 s37, s37, 0\n"
    "s_cmp_lt_u32 s57, 3\n"
    "s_cselect_b32 s38, 0x20000, 0\n"
    "v_cvt_pk_bf16_f32 v171, v150, v151\n"
    "v_mfma_f32_16x16x32_bf16 v[48:51], a[176:179], v[164:167], v[48:51]\n"
    "v_mul_f32 v152, s54, v152\n"
    "v_mul_f32 v153, s54, v153\n"
    "v_mfma_f32_16x16x32_bf16 v[52:55], a[180:183], v[164:167], v[52:55]\n"
    "s_add_u32 m0, s52, 0x0\n"
    "ds_read_b64_tr_b16 a[192:193], v178 offset:40960\n"
    "v_mul_f32 v154, s54, v154\n"
    "v_mfma_f32_16x16x32_bf16 v[56:59], a[184:187], v[164:167], v[56:59]\n"
    "buffer_load_dwordx4 v181, s[36:39], 0 offen offset:0 nt lds\n"
    "v_mul_f32 v155, s54, v155\n"
    "v_mfma_f32_16x16x32_bf16 v[60:63], a[188:191], v[164:167], v[60:63]\n"
    "s_waitcnt lgkmcnt(1)\n"
    "ds_read_b64_tr_b16 a[194:195], v178 offset:45056\n"
    "v_exp_f32 v152, v152\n"
    "v_mfma_f32_16x16x32_bf16 a[228:231], a[240:243], v[164:167], a[228:231]\n"
    "v_exp_f32 v153, v153\n"
    "v_mfma_f32_16x16x32_bf16 v[128:131], a[128:131], a[0:3], v[248:251]\n"
    "v_exp_f32 v154, v154\n"
    "v_mfma_f32_16x16x32_bf16 v[128:131], a[132:135], a[4:7], v[128:131]\n"
    "v_exp_f32 v155, v155\n"
    "v_mfma_f32_16x16x32_bf16 v[128:131], a[136:139], a[8:11], v[128:131]\n"
    "ds_read_b64_tr_b16 a[196:197], v178 offset:41216\n"
    "v_cvt_pk_bf16_f32 v172, v152, v153\n"
    "v_mfma_f32_16x16x32_bf16 v[128:131], a[140:143], a[12:15], v[128:131]\n"
    "v_mul_f32 v156, s54, v156\n"
    "v_mul_f32 v157, s54, v157\n"
    "v_mfma_f32_16x16x32_bf16 v[132:135], a[144:147], a[0:3], v[248:251]\n"
    "v_exp_f32 v156, v156\n"
    "v_mfma_f32_16x16x32_bf16 v[132:135], a[148:151], a[4:7], v[132:135]\n"
    "v_exp_f32 v157, v157\n"
    "v_mfma_f32_16x16x32_bf16 v[132:135], a[152:155], a[8:11], v[132:135]\n"
    "ds_read_b64_tr_b16 a[198:199], v178 offset:45312\n"
    "v_cvt_pk_bf16_f32 v173, v154, v155\n"
    "v_mul_f32 v158, s54, v158\n"
    "v_mfma_f32_16x16x32_bf16 v[132:135], a[156:159], a[12:15], v[132:135]\n"
    "v_exp_f32 v158, v158\n"
    "v_mfma_f32_16x16x32_bf16 v[136:139], a[128:131], a[16:19], v[252:255]\n"
    "v_mul_f32 v159, s54, v159\n"
    "v_cvt_pk_bf16_f32 v174, v156, v157\n"
    "v_mfma_f32_16x16x32_bf16 v[136:139], a[132:135], a[20:23], v[136:139]\n"
    "buffer_load_dwordx4 v182, s[36:39], 0 offen offset:2048 nt lds\n"
    "v_mfma_f32_16x16x32_bf16 v[136:139], a[136:139], a[24:27], v[136:139]\n"
    "v_exp_f32 v159, v159\n"
    "v_mfma_f32_16x16x32_bf16 v[136:139], a[140:143], a[28:31], v[136:139]\n"
    "v_cvt_pk_bf16_f32 v175, v158, v159\n"
    "v_mul_f32 v128, s54, v128\n"
    "v_mfma_f32_16x16x32_bf16 v[140:143], a[144:147], a[16:19], v[252:255]\n"
    "ds_read_b64_tr_b16 a[200:201], v178 offset:41472\n"
    "v_mul_f32 v129, s54, v129\n"
    "v_mfma_f32_16x16x32_bf16 v[140:143], a[148:151], a[20:23], v[140:143]\n"
    "v_exp_f32 v128, v128\n"
    "v_mul_f32 v130, s54, v130\n"
    "v_mfma_f32_16x16x32_bf16 v[140:143], a[152:155], a[24:27], v[140:143]\n"
    "ds_read_b64_tr_b16 a[202:203], v178 offset:45568\n"
    "v_mul_f32 v131, s54, v131\n"
    "v_mfma_f32_16x16x32_bf16 v[140:143], a[156:159], a[28:31], v[140:143]\n"
    "v_exp_f32 v129, v129\n"
    "v_mfma_f32_16x16x32_bf16 v[64:67], a[160:163], v[168:171], v[64:67]\n"
    "ds_read_b64_tr_b16 a[204:205], v178 offset:41728\n"
    "v_cvt_pk_bf16_f32 v160, v128, v129\n"
    "v_mfma_f32_16x16x32_bf16 v[68:71], a[164:167], v[168:171], v[68:71]\n"
    "v_exp_f32 v130, v130\n"
    "v_mul_f32 v132, s54, v132\n"
    "v_mfma_f32_16x16x32_bf16 v[72:75], a[168:171], v[168:171], v[72:75]\n"
    "s_add_u32 m0, s52, 0x380\n"
    "ds_read_b64_tr_b16 a[206:207], v178 offset:45824\n"
    "v_mul_f32 v133, s54, v133\n"
    "v_mfma_f32_16x16x32_bf16 v[76:79], a[172:175], v[168:171], v[76:79]\n"
    "buffer_load_dwordx4 v181, s[36:39], 0 offen offset:128 nt lds\n"
    "v_mul_f32 v134, s54, v134\n"
    "v_mfma_f32_16x16x32_bf16 v[80:83], a[176:179], v[168:171], v[80:83]\n"
    "ds_read_b64_tr_b16 a[208:209], v178 offset:41984\n"
    "v_mul_f32 v135, s54, v135\n"
    "v_mfma_f32_16x16x32_bf16 v[84:87], a[180:183], v[168:171], v[84:87]\n"
    "v_exp_f32 v131, v131\n"
    "v_mfma_f32_16x16x32_bf16 v[88:91], a[184:187], v[168:171], v[88:91]\n"
    "ds_read_b64_tr_b16 a[210:211], v178 offset:46080\n"
    "v_cvt_pk_bf16_f32 v161, v130, v131\n"
    "v_mfma_f32_16x16x32_bf16 v[92:95], a[188:191], v[168:171], v[92:95]\n"
    "v_exp_f32 v132, v132\n"
    "v_mfma_f32_16x16x32_bf16 a[232:235], a[240:243], v[168:171], a[232:235]\n"
    "ds_read_b64_tr_b16 a[212:213], v178 offset:42240\n"
    "v_mfma_f32_16x16x32_bf16 v[96:99], a[160:163], v[172:175], v[96:99]\n"
    "v_exp_f32 v133, v133\n"
    "v_mfma_f32_16x16x32_bf16 v[100:103], a[164:167], v[172:175], v[100:103]\n"
    "ds_read_b64_tr_b16 a[214:215], v178 offset:46336\n"
    "v_cvt_pk_bf16_f32 v162, v132, v133\n"
    "v_mfma_f32_16x16x32_bf16 v[104:107], a[168:171], v[172:175], v[104:107]\n"
    "v_exp_f32 v134, v134\n"
    "v_exp_f32 v135, v135\n"
    "v_mfma_f32_16x16x32_bf16 v[108:111], a[172:175], v[172:175], v[108:111]\n"
    "v_cvt_pk_bf16_f32 v163, v134, v135\n"
    "v_mul_f32 v136, s54, v136\n"
    "v_mfma_f32_16x16x32_bf16 v[112:115], a[176:179], v[172:175], v[112:115]\n"
    "v_exp_f32 v136, v136\n"
    "v_mfma_f32_16x16x32_bf16 v[116:119], a[180:183], v[172:175], v[116:119]\n"
    "ds_read_b64_tr_b16 a[216:217], v178 offset:42496\n"
    "v_mul_f32 v137, s54, v137\n"
    "v_mul_f32 v138, s54, v138\n"
    "v_mfma_f32_16x16x32_bf16 v[120:123], a[184:187], v[172:175], v[120:123]\n"
    "buffer_load_dwordx4 v182, s[36:39], 0 offen offset:2176 nt lds\n"
    "v_mul_f32 v139, s54, v139\n"
    "v_mfma_f32_16x16x32_bf16 v[124:127], a[188:191], v[172:175], v[124:127]\n"
    "v_exp_f32 v137, v137\n"
    "v_mfma_f32_16x16x32_bf16 a[236:239], a[240:243], v[172:175], a[236:239]\n"
    "v_exp_f32 v138, v138\n"
    "v_mfma_f32_16x16x32_bf16 v[144:147], a[128:131], a[32:35], v[196:199]\n"
    "ds_read_b64_tr_b16 a[218:219], v178 offset:46592\n"
    "v_cvt_pk_bf16_f32 v164, v136, v137\n"
    "v_mfma_f32_16x16x32_bf16 v[144:147], a[132:135], a[36:39], v[144:147]\n"
    "v_exp_f32 v139, v139\n"
    "v_mfma_f32_16x16x32_bf16 v[144:147], a[136:139], a[40:43], v[144:147]\n"
    "ds_read_b64_tr_b16 a[220:221], v178 offset:42752\n"
    "v_mul_f32 v140, s54, v140\n"
    "v_mul_f32 v141, s54, v141\n"
    "v_mfma_f32_16x16x32_bf16 v[144:147], a[140:143], a[44:47], v[144:147]\n"
    "v_exp_f32 v140, v140\n"
    "v_mfma_f32_16x16x32_bf16 v[148:151], a[144:147], a[32:35], v[196:199]\n"
    "ds_read_b64_tr_b16 a[222:223], v178 offset:46848\n"
    "v_cvt_pk_bf16_f32 v165, v138, v139\n"
    "v_mfma_f32_16x16x32_bf16 v[148:151], a[148:151], a[36:39], v[148:151]\n"
    "s_add_u32 s40, s40, 0x2000000\n"
    "s_addc_u32 s41, s41, 0\n"
    "s_cmp_lt_u32 s57, 3\n"
    "s_cselect_b32 s42, 0x20000, 0\n"
    "v_mfma_f32_16x16x32_bf16 v[148:151], a[152:155], a[40:43], v[148:151]\n"
    "v_exp_f32 v141, v141\n"
    "v_mfma_f32_16x16x32_bf16 v[148:151], a[156:159], a[44:47], v[148:151]\n"
    "v_mul_f32 v142, s54, v142\n"
    "v_mul_f32 v143, s54, v143\n"
    "v_mfma_f32_16x16x32_bf16 v[152:155], a[128:131], a[48:51], v[212:215]\n"
    "s_waitcnt vmcnt(20) lgkmcnt(0)\n"
    "s_barrier\n"
    "v_exp_f32 v142, v142\n"
    "v_cvt_pk_bf16_f32 v166, v140, v141\n"
    "v_mfma_f32_16x16x32_bf16 v[152:155], a[132:135], a[52:55], v[152:155]\n"
    "v_exp_f32 v143, v143\n"
    "v_mfma_f32_16x16x32_bf16 v[152:155], a[136:139], a[56:59], v[152:155]\n"
    "s_add_u32 m0, s52, 0x4000\n"
    "ds_read_b128 a[128:131], v177 offset:0\n"
    "v_cvt_pk_bf16_f32 v167, v142, v143\n"
    "v_mfma_f32_16x16x32_bf16 v[152:155], a[140:143], a[60:63], v[152:155]\n"
    "buffer_load_dwordx4 v183, s[40:43], 0 offen offset:0 nt lds\n"
    "v_mul_f32 v144, s54, v144\n"
    "v_mfma_f32_16x16x32_bf16 v[156:159], a[144:147], a[48:51], v[212:215]\n"
    "ds_read_b128 a[132:135], v177 offset:512\n"
    "v_mul_f32 v145, s54, v145\n"
    "v_mfma_f32_16x16x32_bf16 v[156:159], a[148:151], a[52:55], v[156:159]\n"
    "v_exp_f32 v144, v144\n"
    "v_mul_f32 v146, s54, v146\n"
    "v_mfma_f32_16x16x32_bf16 v[156:159], a[152:155], a[56:59], v[156:159]\n"
    "ds_read_b128 a[136:139], v177 offset:1024\n"
    "v_mul_f32 v147, s54, v147\n"
    "v_mfma_f32_16x16x32_bf16 v[156:159], a[156:159], a[60:63], v[156:159]\n"
    "v_exp_f32 v145, v145\n"
    "v_mfma_f32_16x16x32_bf16 v[0:3], a[192:195], v[160:163], v[0:3]\n"
    "ds_read_b128 a[140:143], v177 offset:1536\n"
    "v_cvt_pk_bf16_f32 v168, v144, v145\n"
    "v_mfma_f32_16x16x32_bf16 v[4:7], a[196:199], v[160:163], v[4:7]\n"
    "v_exp_f32 v146, v146\n"
    "v_mfma_f32_16x16x32_bf16 v[8:11], a[200:203], v[160:163], v[8:11]\n"
    "ds_read_b128 a[144:147], v177 offset:4096\n"
    "v_mul_f32 v148, s54, v148\n"
    "v_mfma_f32_16x16x32_bf16 v[12:15], a[204:207], v[160:163], v[12:15]\n"
    "v_exp_f32 v147, v147\n"
    "v_mul_f32 v149, s54, v149\n"
    "v_mfma_f32_16x16x32_bf16 v[16:19], a[208:211], v[160:163], v[16:19]\n"
    "ds_read_b128 a[148:151], v177 offset:4608\n"
    "v_cvt_pk_bf16_f32 v169, v146, v147\n"
    "v_mfma_f32_16x16x32_bf16 v[20:23], a[212:215], v[160:163], v[20:23]\n"
    "v_exp_f32 v148, v148\n"
    "v_mfma_f32_16x16x32_bf16 v[24:27], a[216:219], v[160:163], v[24:27]\n"
    "ds_read_b128 a[152:155], v177 offset:5120\n"
    "v_mul_f32 v150, s54, v150\n"
    "v_mfma_f32_16x16x32_bf16 v[28:31], a[220:223], v[160:163], v[28:31]\n"
    "buffer_load_dwordx4 v183, s[40:43], 0 offen offset:2048 nt lds\n"
    "v_mul_f32 v151, s54, v151\n"
    "v_mfma_f32_16x16x32_bf16 a[224:227], a[240:243], v[160:163], a[224:227]\n"
    "ds_read_b128 a[156:159], v177 offset:5632\n"
    "v_mfma_f32_16x16x32_bf16 v[32:35], a[192:195], v[164:167], v[32:35]\n"
    "v_exp_f32 v149, v149\n"
    "v_mfma_f32_16x16x32_bf16 v[36:39], a[196:199], v[164:167], v[36:39]\n"
    "v_exp_f32 v150, v150\n"
    "v_mfma_f32_16x16x32_bf16 v[40:43], a[200:203], v[164:167], v[40:43]\n"
    "v_exp_f32 v151, v151\n"
    "v_cvt_pk_bf16_f32 v170, v148, v149\n"
    "v_mfma_f32_16x16x32_bf16 v[44:47], a[204:207], v[164:167], v[44:47]\n"
    "v_cvt_pk_bf16_f32 v171, v150, v151\n"
    "v_mul_f32 v152, s54, v152\n"
    "v_mfma_f32_16x16x32_bf16 v[48:51], a[208:211], v[164:167], v[48:51]\n"
    "v_exp_f32 v152, v152\n"
    "v_mfma_f32_16x16x32_bf16 v[52:55], a[212:215], v[164:167], v[52:55]\n"
    "ds_read_b64_tr_b16 a[160:161], v179 offset:0\n"
    "v_mul_f32 v153, s54, v153\n"
    "v_mfma_f32_16x16x32_bf16 v[56:59], a[216:219], v[164:167], v[56:59]\n"
    "v_exp_f32 v153, v153\n"
    "v_mfma_f32_16x16x32_bf16 v[60:63], a[220:223], v[164:167], v[60:63]\n"
    "s_waitcnt lgkmcnt(1)\n"
    "s_add_u32 m0, s52, 0x4380\n"
    "ds_read_b64_tr_b16 a[162:163], v179 offset:4096\n"
    "v_mfma_f32_16x16x32_bf16 a[228:231], a[240:243], v[164:167], a[228:231]\n"
    "buffer_load_dwordx4 v183, s[40:43], 0 offen offset:128 nt lds\n"
    "v_mul_f32 v154, s54, v154\n"
    "v_mfma_f32_16x16x32_bf16 v[128:131], a[128:131], a[0:3], v[248:251]\n"
    "v_exp_f32 v154, v154\n"
    "v_mfma_f32_16x16x32_bf16 v[128:131], a[132:135], a[4:7], v[128:131]\n"
    "v_mul_f32 v155, s54, v155\n"
    "v_cvt_pk_bf16_f32 v172, v152, v153\n"
    "v_mfma_f32_16x16x32_bf16 v[128:131], a[136:139], a[8:11], v[128:131]\n"
    "ds_read_b64_tr_b16 a[164:165], v179 offset:256\n"
    "v_exp_f32 v155, v155\n"
    "v_mfma_f32_16x16x32_bf16 v[128:131], a[140:143], a[12:15], v[128:131]\n"
    "v_mul_f32 v156, s54, v156\n"
    "v_mul_f32 v157, s54, v157\n"
    "v_mfma_f32_16x16x32_bf16 v[132:135], a[144:147], a[0:3], v[248:251]\n"
    "v_exp_f32 v156, v156\n"
    "v_mfma_f32_16x16x32_bf16 v[132:135], a[148:151], a[4:7], v[132:135]\n"
    "v_exp_f32 v157, v157\n"
    "v_mfma_f32_16x16x32_bf16 v[132:135], a[152:155], a[8:11], v[132:135]\n"
    "ds_read_b64_tr_b16 a[166:167], v179 offset:4352\n"
    "v_cvt_pk_bf16_f32 v173, v154, v155\n"
    "v_mfma_f32_16x16x32_bf16 v[132:135], a[156:159], a[12:15], v[132:135]\n"
    "v_mul_f32 v158, s54, v158\n"
    "v_mul_f32 v159, s54, v159\n"
    "v_mfma_f32_16x16x32_bf16 v[136:139], a[128:131], a[16:19], v[252:255]\n"
    "v_exp_f32 v158, v158\n"
    "v_mfma_f32_16x16x32_bf16 v[136:139], a[132:135], a[20:23], v[136:139]\n"
    "v_exp_f32 v159, v159\n"
    "v_mfma_f32_16x16x32_bf16 v[136:139], a[136:139], a[24:27], v[136:139]\n"
    "v_cvt_pk_bf16_f32 v174, v156, v157\n"
    "v_cvt_pk_bf16_f32 v175, v158, v159\n"
    "v_mfma_f32_16x16x32_bf16 v[136:139], a[140:143], a[28:31], v[136:139]\n"
    "buffer_load_dwordx4 v183, s[40:43], 0 offen offset:2176 nt lds\n"
    "v_mul_f32 v128, s54, v128\n"
    "v_mfma_f32_16x16x32_bf16 v[140:143], a[144:147], a[16:19], v[252:255]\n"
    "ds_read_b64_tr_b16 a[168:169], v179 offset:512\n"
    "v_mul_f32 v129, s54, v129\n"
    "v_mfma_f32_16x16x32_bf16 v[140:143], a[148:151], a[20:23], v[140:143]\n"
    "v_exp_f32 v128, v128\n"
    "v_mul_f32 v130, s54, v130\n"
    "v_mfma_f32_16x16x32_bf16 v[140:143], a[152:155], a[24:27], v[140:143]\n"
    "ds_read_b64_tr_b16 a[170:171], v179 offset:4608\n"
    "v_mul_f32 v131, s54, v131\n"
    "v_mfma_f32_16x16x32_bf16 v[140:143], a[156:159], a[28:31], v[140:143]\n"
    "v_exp_f32 v129, v129\n"
    "v_mfma_f32_16x16x32_bf16 v[64:67], a[192:195], v[168:171], v[64:67]\n"
    "ds_read_b64_tr_b16 a[172:173], v179 offset:768\n"
    "v_cvt_pk_bf16_f32 v160, v128, v129\n"
    "v_mfma_f32_16x16x32_bf16 v[68:71], a[196:199], v[168:171], v[68:71]\n"
    "v_exp_f32 v130, v130\n"
    "v_mul_f32 v132, s54, v132\n"
    "v_mfma_f32_16x16x32_bf16 v[72:75], a[200:203], v[168:171], v[72:75]\n"
    "ds_read_b64_tr_b16 a[174:175], v179 offset:4864\n"
    "v_mul_f32 v133, s54, v133\n"
    "v_mfma_f32_16x16x32_bf16 v[76:79], a[204:207], v[168:171], v[76:79]\n"
    "v_exp_f32 v131, v131\n"
    "v_mfma_f32_16x16x32_bf16 v[80:83], a[208:211], v[168:171], v[80:83]\n"
    "s_add_u32 m0, s52, 0x8000\n"
    "ds_read_b64_tr_b16 a[176:177], v179 offset:1024\n"
    "v_cvt_pk_bf16_f32 v161, v130, v131\n"
    "v_mfma_f32_16x16x32_bf16 v[84:87], a[212:215], v[168:171], v[84:87]\n"
    "buffer_load_dwordx4 v181, s[36:39], s70 offen offset:0 nt lds\n"
    "v_exp_f32 v132, v132\n"
    "v_mfma_f32_16x16x32_bf16 v[88:91], a[216:219], v[168:171], v[88:91]\n"
    "ds_read_b64_tr_b16 a[178:179], v179 offset:5120\n"
    "v_mul_f32 v134, s54, v134\n"
    "v_mfma_f32_16x16x32_bf16 v[92:95], a[220:223], v[168:171], v[92:95]\n"
    "v_exp_f32 v133, v133\n"
    "v_mfma_f32_16x16x32_bf16 a[232:235], a[240:243], v[168:171], a[232:235]\n"
    "ds_read_b64_tr_b16 a[180:181], v179 offset:1280\n"
    "v_mul_f32 v135, s54, v135\n"
    "v_mfma_f32_16x16x32_bf16 v[96:99], a[192:195], v[172:175], v[96:99]\n"
    "v_exp_f32 v134, v134\n"
    "v_cvt_pk_bf16_f32 v162, v132, v133\n"
    "v_mfma_f32_16x16x32_bf16 v[100:103], a[196:199], v[172:175], v[100:103]\n"
    "ds_read_b64_tr_b16 a[182:183], v179 offset:5376\n"
    "v_mfma_f32_16x16x32_bf16 v[104:107], a[200:203], v[172:175], v[104:107]\n"
    "v_exp_f32 v135, v135\n"
    "v_mfma_f32_16x16x32_bf16 v[108:111], a[204:207], v[172:175], v[108:111]\n"
    "v_cvt_pk_bf16_f32 v163, v134, v135\n"
    "v_mul_f32 v136, s54, v136\n"
    "v_mfma_f32_16x16x32_bf16 v[112:115], a[208:211], v[172:175], v[112:115]\n"
    "v_exp_f32 v136, v136\n"
    "v_mfma_f32_16x16x32_bf16 v[116:119], a[212:215], v[172:175], v[116:119]\n"
    "ds_read_b64_tr_b16 a[184:185], v179 offset:1536\n"
    "v_mul_f32 v137, s54, v137\n"
    "v_mul_f32 v138, s54, v138\n"
    "v_mfma_f32_16x16x32_bf16 v[120:123], a[216:219], v[172:175], v[120:123]\n"
    "v_exp_f32 v137, v137\n"
    "v_mfma_f32_16x16x32_bf16 v[124:127], a[220:223], v[172:175], v[124:127]\n"
    "v_exp_f32 v138, v138\n"
    "v_mfma_f32_16x16x32_bf16 a[236:239], a[240:243], v[172:175], a[236:239]\n"
    "buffer_load_dwordx4 v182, s[36:39], s70 offen offset:2048 nt lds\n"
    "v_mul_f32 v139, s54, v139\n"
    "v_mfma_f32_16x16x32_bf16 v[144:147], a[128:131], a[32:35], v[196:199]\n"
    "ds_read_b64_tr_b16 a[186:187], v179 offset:5632\n"
    "v_cvt_pk_bf16_f32 v164, v136, v137\n"
    "v_mfma_f32_16x16x32_bf16 v[144:147], a[132:135], a[36:39], v[144:147]\n"
    "v_exp_f32 v139, v139\n"
    "v_mfma_f32_16x16x32_bf16 v[144:147], a[136:139], a[40:43], v[144:147]\n"
    "ds_read_b64_tr_b16 a[188:189], v179 offset:1792\n"
    "v_mul_f32 v140, s54, v140\n"
    "v_mul_f32 v141, s54, v141\n"
    "v_mfma_f32_16x16x32_bf16 v[144:147], a[140:143], a[44:47], v[144:147]\n"
    "v_exp_f32 v140, v140\n"
    "v_mfma_f32_16x16x32_bf16 v[148:151], a[144:147], a[32:35], v[196:199]\n"
    "ds_read_b64_tr_b16 a[190:191], v179 offset:5888\n"
    "v_cvt_pk_bf16_f32 v165, v138, v139\n"
    "v_mfma_f32_16x16x32_bf16 v[148:151], a[148:151], a[36:39], v[148:151]\n"
    "v_exp_f32 v141, v141\n"
    "v_mfma_f32_16x16x32_bf16 v[148:151], a[152:155], a[40:43], v[148:151]\n"
    "v_mul_f32 v142, s54, v142\n"
    "v_mul_f32 v143, s54, v143\n"
    "v_mfma_f32_16x16x32_bf16 v[148:151], a[156:159], a[44:47], v[148:151]\n"
    "v_exp_f32 v142, v142\n"
    "v_mfma_f32_16x16x32_bf16 v[152:155], a[128:131], a[48:51], v[212:215]\n"
    "s_waitcnt lgkmcnt(0)\n"
    "s_add_u32 m0, s52, 0x8380\n"
    "v_exp_f32 v143, v143\n"
    "v_mfma_f32_16x16x32_bf16 v[152:155], a[132:135], a[52:55], v[152:155]\n"
    "buffer_load_dwordx4 v181, s[36:39], s70 offen offset:128 nt lds\n"
    "v_cvt_pk_bf16_f32 v166, v140, v141\n"
    "v_mfma_f32_16x16x32_bf16 v[152:155], a[136:139], a[56:59], v[152:155]\n"
    "ds_read_b128 a[128:131], v177 offset:8192\n"
    "v_cvt_pk_bf16_f32 v167, v142, v143\n"
    "v_mfma_f32_16x16x32_bf16 v[152:155], a[140:143], a[60:63], v[152:155]\n"
    "v_mul_f32 v144, s54, v144\n"
    "v_mul_f32 v145, s54, v145\n"
    "v_mfma_f32_16x16x32_bf16 v[156:159], a[144:147], a[48:51], v[212:215]\n"
    "ds_read_b128 a[132:135], v177 offset:8704\n"
    "v_mul_f32 v146, s54, v146\n"
    "v_mfma_f32_16x16x32_bf16 v[156:159], a[148:151], a[52:55], v[156:159]\n"
    "v_exp_f32 v144, v144\n"
    "v_mfma_f32_16x16x32_bf16 v[156:159], a[152:155], a[56:59], v[156:159]\n"
    "ds_read_b128 a[136:139], v177 offset:9216\n"
    "v_mul_f32 v147, s54, v147\n"
    "v_mfma_f32_16x16x32_bf16 v[156:159], a[156:159], a[60:63], v[156:159]\n"
    "v_exp_f32 v145, v145\n"
    "v_mul_f32 v148, s54, v148\n"
    "v_mfma_f32_16x16x32_bf16 v[0:3], a[160:163], v[160:163], v[0:3]\n"
    "ds_read_b128 a[140:143], v177 offset:9728\n"
    "v_cvt_pk_bf16_f32 v168, v144, v145\n"
    "v_mfma_f32_16x16x32_bf16 v[4:7], a[164:167], v[160:163], v[4:7]\n"
    "v_exp_f32 v146, v146\n"
    "v_mfma_f32_16x16x32_bf16 v[8:11], a[168:171], v[160:163], v[8:11]\n"
    "ds_read_b128 a[144:147], v177 offset:12288\n"
    "v_mul_f32 v149, s54, v149\n"
    "v_mfma_f32_16x16x32_bf16 v[12:15], a[172:175], v[160:163], v[12:15]\n"
    "v_exp_f32 v147, v147\n"
    "v_mfma_f32_16x16x32_bf16 v[16:19], a[176:179], v[160:163], v[16:19]\n"
    "ds_read_b128 a[148:151], v177 offset:12800\n"
    "v_cvt_pk_bf16_f32 v169, v146, v147\n"
    "v_mfma_f32_16x16x32_bf16 v[20:23], a[180:183], v[160:163], v[20:23]\n"
    "buffer_load_dwordx4 v182, s[36:39], s70 offen offset:2176 nt lds\n"
    "v_mul_f32 v150, s54, v150\n"
    "v_mfma_f32_16x16x32_bf16 v[24:27], a[184:187], v[160:163], v[24:27]\n"
    "ds_read_b128 a[152:155], v177 offset:13312\n"
    "v_mul_f32 v151, s54, v151\n"
    "v_mfma_f32_16x16x32_bf16 v[28:31], a[188:191], v[160:163], v[28:31]\n"
    "v_exp_f32 v148, v148\n"
    "v_mfma_f32_16x16x32_bf16 a[224:227], a[240:243], v[160:163], a[224:227]\n"
    "ds_read_b128 a[156:159], v177 offset:13824\n"
    "v_mfma_f32_16x16x32_bf16 v[32:35], a[160:163], v[164:167], v[32:35]\n"
    "v_exp_f32 v149, v149\n"
    "v_mfma_f32_16x16x32_bf16 v[36:39], a[164:167], v[164:167], v[36:39]\n"
    "v_exp_f32 v150, v150\n"
    "v_mfma_f32_16x16x32_bf16 v[40:43], a[168:171], v[164:167], v[40:43]\n"
    "v_exp_f32 v151, v151\n"
    "v_cvt_pk_bf16_f32 v170, v148, v149\n"
    "v_mfma_f32_16x16x32_bf16 v[44:47], a[172:175], v[164:167], v[44:47]\n"
    "v_cvt_pk_bf16_f32 v171, v150, v151\n"
    "v_mul_f32 v152, s54, v152\n"
    "v_mfma_f32_16x16x32_bf16 v[48:51], a[176:179], v[164:167], v[48:51]\n"
    "v_exp_f32 v152, v152\n"
    "v_mfma_f32_16x16x32_bf16 v[52:55], a[180:183], v[164:167], v[52:55]\n"
    "s_add_u32 m0, s52, 0xc000\n"
    "ds_read_b64_tr_b16 a[192:193], v179 offset:8192\n"
    "v_mul_f32 v153, s54, v153\n"
    "v_mfma_f32_16x16x32_bf16 v[56:59], a[184:187], v[164:167], v[56:59]\n"
    "buffer_load_dwordx4 v183, s[40:43], s70 offen offset:0 nt lds\n"
    "v_mul_f32 v154, s54, v154\n"
    "v_mfma_f32_16x16x32_bf16 v[60:63], a[188:191], v[164:167], v[60:63]\n"
    "s_waitcnt lgkmcnt(1)\n"
    "ds_read_b64_tr_b16 a[194:195], v179 offset:12288\n"
    "v_mul_f32 v155, s54, v155\n"
    "v_mfma_f32_16x16x32_bf16 a[228:231], a[240:243], v[164:167], a[228:231]\n"
    "v_exp_f32 v153, v153\n"
    "v_mfma_f32_16x16x32_bf16 v[128:131], a[128:131], a[0:3], v[248:251]\n"
    "v_exp_f32 v154, v154\n"
    "v_mfma_f32_16x16x32_bf16 v[128:131], a[132:135], a[4:7], v[128:131]\n"
    "v_exp_f32 v155, v155\n"
    "v_mfma_f32_16x16x32_bf16 v[128:131], a[136:139], a[8:11], v[128:131]\n"
    "ds_read_b64_tr_b16 a[196:197], v179 offset:8448\n"
    "v_cvt_pk_bf16_f32 v172, v152, v153\n"
    "v_mul_f32 v156, s54, v156\n"
    "v_mfma_f32_16x16x32_bf16 v[128:131], a[140:143], a[12:15], v[128:131]\n"
    "v_exp_f32 v156, v156\n"
    "v_mfma_f32_16x16x32_bf16 v[132:135], a[144:147], a[0:3], v[248:251]\n"
    "v_mul_f32 v157, s54, v157\n"
    "v_cvt_pk_bf16_f32 v173, v154, v155\n"
    "v_mfma_f32_16x16x32_bf16 v[132:135], a[148:151], a[4:7], v[132:135]\n"
    "v_exp_f32 v157, v157\n"
    "v_mfma_f32_16x16x32_bf16 v[132:135], a[152:155], a[8:11], v[132:135]\n"
    "ds_read_b64_tr_b16 a[198:199], v179 offset:12544\n"
    "v_mul_f32 v158, s54, v158\n"
    "v_mfma_f32_16x16x32_bf16 v[132:135], a[156:159], a[12:15], v[132:135]\n"
    "v_exp_f32 v158, v158\n"
    "v_mfma_f32_16x16x32_bf16 v[136:139], a[128:131], a[16:19], v[252:255]\n"
    "v_mul_f32 v159, s54, v159\n"
    "v_cvt_pk_bf16_f32 v174, v156, v157\n"
    "v_mfma_f32_16x16x32_bf16 v[136:139], a[132:135], a[20:23], v[136:139]\n"
    "buffer_load_dwordx4 v183, s[40:43], s70 offen offset:2048 nt lds\n"
    "v_mfma_f32_16x16x32_bf16 v[136:139], a[136:139], a[24:27], v[136:139]\n"
    "v_exp_f32 v159, v159\n"
    "v_mfma_f32_16x16x32_bf16 v[136:139], a[140:143], a[28:31], v[136:139]\n"
    "v_cvt_pk_bf16_f32 v175, v158, v159\n"
    "v_mul_f32 v128, s54, v128\n"
    "v_mfma_f32_16x16x32_bf16 v[140:143], a[144:147], a[16:19], v[252:255]\n"
    "ds_read_b64_tr_b16 a[200:201], v179 offset:8704\n"
    "v_mul_f32 v129, s54, v129\n"
    "v_mfma_f32_16x16x32_bf16 v[140:143], a[148:151], a[20:23], v[140:143]\n"
    "v_exp_f32 v128, v128\n"
    "v_mul_f32 v130, s54, v130\n"
    "v_mfma_f32_16x16x32_bf16 v[140:143], a[152:155], a[24:27], v[140:143]\n"
    "ds_read_b64_tr_b16 a[202:203], v179 offset:12800\n"
    "v_mul_f32 v131, s54, v131\n"
    "v_mfma_f32_16x16x32_bf16 v[140:143], a[156:159], a[28:31], v[140:143]\n"
    "v_exp_f32 v129, v129\n"
    "v_mfma_f32_16x16x32_bf16 v[64:67], a[160:163], v[168:171], v[64:67]\n"
    "ds_read_b64_tr_b16 a[204:205], v179 offset:8960\n"
    "v_cvt_pk_bf16_f32 v160, v128, v129\n"
    "v_mfma_f32_16x16x32_bf16 v[68:71], a[164:167], v[168:171], v[68:71]\n"
    "v_exp_f32 v130, v130\n"
    "v_mul_f32 v132, s54, v132\n"
    "v_mfma_f32_16x16x32_bf16 v[72:75], a[168:171], v[168:171], v[72:75]\n"
    "s_add_u32 m0, s52, 0xc380\n"
    "ds_read_b64_tr_b16 a[206:207], v179 offset:13056\n"
    "v_mul_f32 v133, s54, v133\n"
    "v_mfma_f32_16x16x32_bf16 v[76:79], a[172:175], v[168:171], v[76:79]\n"
    "buffer_load_dwordx4 v183, s[40:43], s70 offen offset:128 nt lds\n"
    "v_mul_f32 v134, s54, v134\n"
    "v_mfma_f32_16x16x32_bf16 v[80:83], a[176:179], v[168:171], v[80:83]\n"
    "ds_read_b64_tr_b16 a[208:209], v179 offset:9216\n"
    "v_mul_f32 v135, s54, v135\n"
    "v_mfma_f32_16x16x32_bf16 v[84:87], a[180:183], v[168:171], v[84:87]\n"
    "v_exp_f32 v131, v131\n"
    "v_mfma_f32_16x16x32_bf16 v[88:91], a[184:187], v[168:171], v[88:91]\n"
    "ds_read_b64_tr_b16 a[210:211], v179 offset:13312\n"
    "v_cvt_pk_bf16_f32 v161, v130, v131\n"
    "v_mfma_f32_16x16x32_bf16 v[92:95], a[188:191], v[168:171], v[92:95]\n"
    "v_exp_f32 v132, v132\n"
    "v_mfma_f32_16x16x32_bf16 a[232:235], a[240:243], v[168:171], a[232:235]\n"
    "ds_read_b64_tr_b16 a[212:213], v179 offset:9472\n"
    "v_mfma_f32_16x16x32_bf16 v[96:99], a[160:163], v[172:175], v[96:99]\n"
    "v_exp_f32 v133, v133\n"
    "v_mfma_f32_16x16x32_bf16 v[100:103], a[164:167], v[172:175], v[100:103]\n"
    "ds_read_b64_tr_b16 a[214:215], v179 offset:13568\n"
    "v_cvt_pk_bf16_f32 v162, v132, v133\n"
    "v_mfma_f32_16x16x32_bf16 v[104:107], a[168:171], v[172:175], v[104:107]\n"
    "v_exp_f32 v134, v134\n"
    "v_exp_f32 v135, v135\n"
    "v_mfma_f32_16x16x32_bf16 v[108:111], a[172:175], v[172:175], v[108:111]\n"
    "v_cvt_pk_bf16_f32 v163, v134, v135\n"
    "v_mul_f32 v136, s54, v136\n"
    "v_mfma_f32_16x16x32_bf16 v[112:115], a[176:179], v[172:175], v[112:115]\n"
    "v_exp_f32 v136, v136\n"
    "v_mfma_f32_16x16x32_bf16 v[116:119], a[180:183], v[172:175], v[116:119]\n"
    "ds_read_b64_tr_b16 a[216:217], v179 offset:9728\n"
    "v_mul_f32 v137, s54, v137\n"
    "v_mfma_f32_16x16x32_bf16 v[120:123], a[184:187], v[172:175], v[120:123]\n"
    "buffer_load_dwordx4 v183, s[40:43], s70 offen offset:2176 nt lds\n"
    "v_mul_f32 v138, s54, v138\n"
    "v_mfma_f32_16x16x32_bf16 v[124:127], a[188:191], v[172:175], v[124:127]\n"
    "v_exp_f32 v137, v137\n"
    "v_mfma_f32_16x16x32_bf16 a[236:239], a[240:243], v[172:175], a[236:239]\n"
    "v_exp_f32 v138, v138\n"
    "v_mfma_f32_16x16x32_bf16 v[144:147], a[128:131], a[32:35], v[196:199]\n"
    "ds_read_b64_tr_b16 a[218:219], v179 offset:13824\n"
    "v_mul_f32 v139, s54, v139\n"
    "v_mfma_f32_16x16x32_bf16 v[144:147], a[132:135], a[36:39], v[144:147]\n"
    "v_exp_f32 v139, v139\n"
    "v_mfma_f32_16x16x32_bf16 v[144:147], a[136:139], a[40:43], v[144:147]\n"
    "ds_read_b64_tr_b16 a[220:221], v179 offset:9984\n"
    "v_cvt_pk_bf16_f32 v164, v136, v137\n"
    "v_mul_f32 v140, s54, v140\n"
    "v_mfma_f32_16x16x32_bf16 v[144:147], a[140:143], a[44:47], v[144:147]\n"
    "v_exp_f32 v140, v140\n"
    "v_mfma_f32_16x16x32_bf16 v[148:151], a[144:147], a[32:35], v[196:199]\n"
    "ds_read_b64_tr_b16 a[222:223], v179 offset:14080\n"
    "v_mul_f32 v141, s54, v141\n"
    "v_mfma_f32_16x16x32_bf16 v[148:151], a[148:151], a[36:39], v[148:151]\n"
    "v_exp_f32 v141, v141\n"
    "v_mfma_f32_16x16x32_bf16 v[148:151], a[152:155], a[40:43], v[148:151]\n"
    "v_cvt_pk_bf16_f32 v165, v138, v139\n"
    "v_mul_f32 v142, s54, v142\n"
    "v_mfma_f32_16x16x32_bf16 v[148:151], a[156:159], a[44:47], v[148:151]\n"
    "v_exp_f32 v142, v142\n"
    "v_mfma_f32_16x16x32_bf16 v[152:155], a[128:131], a[48:51], v[212:215]\n"
    "s_waitcnt vmcnt(20) lgkmcnt(0)\n"
    "s_barrier\n"
    "v_mul_f32 v143, s54, v143\n"
    "v_cvt_pk_bf16_f32 v166, v140, v141\n"
    "v_mfma_f32_16x16x32_bf16 v[152:155], a[132:135], a[52:55], v[152:155]\n"
    "v_exp_f32 v143, v143\n"
    "v_mfma_f32_16x16x32_bf16 v[152:155], a[136:139], a[56:59], v[152:155]\n"
    "s_add_u32 m0, s52, 0x10000\n"
    "ds_read_b128 a[128:131], v177 offset:32768\n"
    "v_cvt_pk_bf16_f32 v167, v142, v143\n"
    "v_mfma_f32_16x16x32_bf16 v[152:155], a[140:143], a[60:63], v[152:155]\n"
    "buffer_load_dwordx4 v181, s[36:39], s71 offen offset:0 nt lds\n"
    "v_mul_f32 v144, s54, v144\n"
    "v_mfma_f32_16x16x32_bf16 v[156:159], a[144:147], a[48:51], v[212:215]\n"
    "ds_read_b128 a[132:135], v177 offset:33280\n"
    "v_mul_f32 v145, s54, v145\n"
    "v_mfma_f32_16x16x32_bf16 v[156:159], a[148:151], a[52:55], v[156:159]\n"
    "v_exp_f32 v144, v144\n"
    "v_mul_f32 v146, s54, v146\n"
    "v_mfma_f32_16x16x32_bf16 v[156:159], a[152:155], a[56:59], v[156:159]\n"
    "ds_read_b128 a[136:139], v177 offset:33792\n"
    "v_mul_f32 v147, s54, v147\n"
    "v_mfma_f32_16x16x32_bf16 v[156:159], a[156:159], a[60:63], v[156:159]\n"
    "v_exp_f32 v145, v145\n"
    "v_mfma_f32_16x16x32_bf16 v[0:3], a[192:195], v[160:163], v[0:3]\n"
    "ds_read_b128 a[140:143], v177 offset:34304\n"
    "v_cvt_pk_bf16_f32 v168, v144, v145\n"
    "v_mfma_f32_16x16x32_bf16 v[4:7], a[196:199], v[160:163], v[4:7]\n"
    "v_exp_f32 v146, v146\n"
    "v_mfma_f32_16x16x32_bf16 v[8:11], a[200:203], v[160:163], v[8:11]\n"
    "ds_read_b128 a[144:147], v177 offset:36864\n"
    "v_mul_f32 v148, s54, v148\n"
    "v_mfma_f32_16x16x32_bf16 v[12:15], a[204:207], v[160:163], v[12:15]\n"
    "v_exp_f32 v147, v147\n"
    "v_mul_f32 v149, s54, v149\n"
    "v_mfma_f32_16x16x32_bf16 v[16:19], a[208:211], v[160:163], v[16:19]\n"
    "ds_read_b128 a[148:151], v177 offset:37376\n"
    "v_cvt_pk_bf16_f32 v169, v146, v147\n"
    "v_mfma_f32_16x16x32_bf16 v[20:23], a[212:215], v[160:163], v[20:23]\n"
    "v_exp_f32 v148, v148\n"
    "v_mfma_f32_16x16x32_bf16 v[24:27], a[216:219], v[160:163], v[24:27]\n"
    "ds_read_b128 a[152:155], v177 offset:37888\n"
    "v_mul_f32 v150, s54, v150\n"
    "v_mfma_f32_16x16x32_bf16 v[28:31], a[220:223], v[160:163], v[28:31]\n"
    "buffer_load_dwordx4 v182, s[36:39], s71 offen offset:2048 nt lds\n"
    "v_mul_f32 v151, s54, v151\n"
    "v_mfma_f32_16x16x32_bf16 a[224:227], a[240:243], v[160:163], a[224:227]\n"
    "ds_read_b128 a[156:159], v177 offset:38400\n"
    "v_mfma_f32_16x16x32_bf16 v[32:35], a[192:195], v[164:167], v[32:35]\n"
    "v_exp_f32 v149, v149\n"
    "v_mfma_f32_16x16x32_bf16 v[36:39], a[196:199], v[164:167], v[36:39]\n"
    "v_exp_f32 v150, v150\n"
    "v_mfma_f32_16x16x32_bf16 v[40:43], a[200:203], v[164:167], v[40:43]\n"
    "v_exp_f32 v151, v151\n"
    "v_cvt_pk_bf16_f32 v170, v148, v149\n"
    "v_mfma_f32_16x16x32_bf16 v[44:47], a[204:207], v[164:167], v[44:47]\n"
    "v_cvt_pk_bf16_f32 v171, v150, v151\n"
    "v_mul_f32 v152, s54, v152\n"
    "v_mfma_f32_16x16x32_bf16 v[48:51], a[208:211], v[164:167], v[48:51]\n"
    "v_exp_f32 v152, v152\n"
    "v_mfma_f32_16x16x32_bf16 v[52:55], a[212:215], v[164:167], v[52:55]\n"
    "ds_read_b64_tr_b16 a[160:161], v179 offset:32768\n"
    "v_mul_f32 v153, s54, v153\n"
    "v_mfma_f32_16x16x32_bf16 v[56:59], a[216:219], v[164:167], v[56:59]\n"
    "v_exp_f32 v153, v153\n"
    "v_mfma_f32_16x16x32_bf16 v[60:63], a[220:223], v[164:167], v[60:63]\n"
    "s_waitcnt lgkmcnt(1)\n"
    "s_add_u32 m0, s52, 0x10380\n"
    "ds_read_b64_tr_b16 a[162:163], v179 offset:36864\n"
    "v_mfma_f32_16x16x32_bf16 a[228:231], a[240:243], v[164:167], a[228:231]\n"
    "buffer_load_dwordx4 v181, s[36:39], s71 offen offset:128 nt lds\n"
    "v_mul_f32 v154, s54, v154\n"
    "v_mfma_f32_16x16x32_bf16 v[128:131], a[128:131], a[0:3], v[248:251]\n"
    "v_exp_f32 v154, v154\n"
    "v_mfma_f32_16x16x32_bf16 v[128:131], a[132:135], a[4:7], v[128:131]\n"
    "v_mul_f32 v155, s54, v155\n"
    "v_cvt_pk_bf16_f32 v172, v152, v153\n"
    "v_mfma_f32_16x16x32_bf16 v[128:131], a[136:139], a[8:11], v[128:131]\n"
    "ds_read_b64_tr_b16 a[164:165], v179 offset:33024\n"
    "v_exp_f32 v155, v155\n"
    "v_mfma_f32_16x16x32_bf16 v[128:131], a[140:143], a[12:15], v[128:131]\n"
    "v_mul_f32 v156, s54, v156\n"
    "v_mul_f32 v157, s54, v157\n"
    "v_mfma_f32_16x16x32_bf16 v[132:135], a[144:147], a[0:3], v[248:251]\n"
    "v_exp_f32 v156, v156\n"
    "v_mfma_f32_16x16x32_bf16 v[132:135], a[148:151], a[4:7], v[132:135]\n"
    "v_exp_f32 v157, v157\n"
    "v_mfma_f32_16x16x32_bf16 v[132:135], a[152:155], a[8:11], v[132:135]\n"
    "ds_read_b64_tr_b16 a[166:167], v179 offset:37120\n"
    "v_cvt_pk_bf16_f32 v173, v154, v155\n"
    "v_mfma_f32_16x16x32_bf16 v[132:135], a[156:159], a[12:15], v[132:135]\n"
    "v_mul_f32 v158, s54, v158\n"
    "v_mul_f32 v159, s54, v159\n"
    "v_mfma_f32_16x16x32_bf16 v[136:139], a[128:131], a[16:19], v[252:255]\n"
    "v_exp_f32 v158, v158\n"
    "v_mfma_f32_16x16x32_bf16 v[136:139], a[132:135], a[20:23], v[136:139]\n"
    "v_exp_f32 v159, v159\n"
    "v_mfma_f32_16x16x32_bf16 v[136:139], a[136:139], a[24:27], v[136:139]\n"
    "v_cvt_pk_bf16_f32 v174, v156, v157\n"
    "v_cvt_pk_bf16_f32 v175, v158, v159\n"
    "v_mfma_f32_16x16x32_bf16 v[136:139], a[140:143], a[28:31], v[136:139]\n"
    "buffer_load_dwordx4 v182, s[36:39], s71 offen offset:2176 nt lds\n"
    "v_mul_f32 v128, s54, v128\n"
    "v_mfma_f32_16x16x32_bf16 v[140:143], a[144:147], a[16:19], v[252:255]\n"
    "ds_read_b64_tr_b16 a[168:169], v179 offset:33280\n"
    "v_mul_f32 v129, s54, v129\n"
    "v_mfma_f32_16x16x32_bf16 v[140:143], a[148:151], a[20:23], v[140:143]\n"
    "v_exp_f32 v128, v128\n"
    "v_mul_f32 v130, s54, v130\n"
    "v_mfma_f32_16x16x32_bf16 v[140:143], a[152:155], a[24:27], v[140:143]\n"
    "ds_read_b64_tr_b16 a[170:171], v179 offset:37376\n"
    "v_mul_f32 v131, s54, v131\n"
    "v_mfma_f32_16x16x32_bf16 v[140:143], a[156:159], a[28:31], v[140:143]\n"
    "v_exp_f32 v129, v129\n"
    "v_mfma_f32_16x16x32_bf16 v[64:67], a[192:195], v[168:171], v[64:67]\n"
    "ds_read_b64_tr_b16 a[172:173], v179 offset:33536\n"
    "v_cvt_pk_bf16_f32 v160, v128, v129\n"
    "v_mfma_f32_16x16x32_bf16 v[68:71], a[196:199], v[168:171], v[68:71]\n"
    "v_exp_f32 v130, v130\n"
    "v_mul_f32 v132, s54, v132\n"
    "v_mfma_f32_16x16x32_bf16 v[72:75], a[200:203], v[168:171], v[72:75]\n"
    "ds_read_b64_tr_b16 a[174:175], v179 offset:37632\n"
    "v_mul_f32 v133, s54, v133\n"
    "v_mfma_f32_16x16x32_bf16 v[76:79], a[204:207], v[168:171], v[76:79]\n"
    "v_exp_f32 v131, v131\n"
    "v_mfma_f32_16x16x32_bf16 v[80:83], a[208:211], v[168:171], v[80:83]\n"
    "s_add_u32 m0, s52, 0x14000\n"
    "ds_read_b64_tr_b16 a[176:177], v179 offset:33792\n"
    "v_cvt_pk_bf16_f32 v161, v130, v131\n"
    "v_mfma_f32_16x16x32_bf16 v[84:87], a[212:215], v[168:171], v[84:87]\n"
    "buffer_load_dwordx4 v183, s[40:43], s71 offen offset:0 nt lds\n"
    "v_exp_f32 v132, v132\n"
    "v_mfma_f32_16x16x32_bf16 v[88:91], a[216:219], v[168:171], v[88:91]\n"
    "ds_read_b64_tr_b16 a[178:179], v179 offset:37888\n"
    "v_mul_f32 v134, s54, v134\n"
    "v_mfma_f32_16x16x32_bf16 v[92:95], a[220:223], v[168:171], v[92:95]\n"
    "v_exp_f32 v133, v133\n"
    "v_mfma_f32_16x16x32_bf16 a[232:235], a[240:243], v[168:171], a[232:235]\n"
    "ds_read_b64_tr_b16 a[180:181], v179 offset:34048\n"
    "v_mul_f32 v135, s54, v135\n"
    "v_mfma_f32_16x16x32_bf16 v[96:99], a[192:195], v[172:175], v[96:99]\n"
    "v_exp_f32 v134, v134\n"
    "v_cvt_pk_bf16_f32 v162, v132, v133\n"
    "v_mfma_f32_16x16x32_bf16 v[100:103], a[196:199], v[172:175], v[100:103]\n"
    "ds_read_b64_tr_b16 a[182:183], v179 offset:38144\n"
    "v_mfma_f32_16x16x32_bf16 v[104:107], a[200:203], v[172:175], v[104:107]\n"
    "v_exp_f32 v135, v135\n"
    "v_mfma_f32_16x16x32_bf16 v[108:111], a[204:207], v[172:175], v[108:111]\n"
    "v_cvt_pk_bf16_f32 v163, v134, v135\n"
    "v_mul_f32 v136, s54, v136\n"
    "v_mfma_f32_16x16x32_bf16 v[112:115], a[208:211], v[172:175], v[112:115]\n"
    "v_exp_f32 v136, v136\n"
    "v_mfma_f32_16x16x32_bf16 v[116:119], a[212:215], v[172:175], v[116:119]\n"
    "ds_read_b64_tr_b16 a[184:185], v179 offset:34304\n"
    "v_mul_f32 v137, s54, v137\n"
    "v_mul_f32 v138, s54, v138\n"
    "v_mfma_f32_16x16x32_bf16 v[120:123], a[216:219], v[172:175], v[120:123]\n"
    "v_exp_f32 v137, v137\n"
    "v_mfma_f32_16x16x32_bf16 v[124:127], a[220:223], v[172:175], v[124:127]\n"
    "v_exp_f32 v138, v138\n"
    "v_mfma_f32_16x16x32_bf16 a[236:239], a[240:243], v[172:175], a[236:239]\n"
    "buffer_load_dwordx4 v183, s[40:43], s71 offen offset:2048 nt lds\n"
    "v_mul_f32 v139, s54, v139\n"
    "v_mfma_f32_16x16x32_bf16 v[144:147], a[128:131], a[32:35], v[196:199]\n"
    "ds_read_b64_tr_b16 a[186:187], v179 offset:38400\n"
    "v_cvt_pk_bf16_f32 v164, v136, v137\n"
    "v_mfma_f32_16x16x32_bf16 v[144:147], a[132:135], a[36:39], v[144:147]\n"
    "v_exp_f32 v139, v139\n"
    "v_mfma_f32_16x16x32_bf16 v[144:147], a[136:139], a[40:43], v[144:147]\n"
    "ds_read_b64_tr_b16 a[188:189], v179 offset:34560\n"
    "v_mul_f32 v140, s54, v140\n"
    "v_mul_f32 v141, s54, v141\n"
    "v_mfma_f32_16x16x32_bf16 v[144:147], a[140:143], a[44:47], v[144:147]\n"
    "v_exp_f32 v140, v140\n"
    "v_mfma_f32_16x16x32_bf16 v[148:151], a[144:147], a[32:35], v[196:199]\n"
    "ds_read_b64_tr_b16 a[190:191], v179 offset:38656\n"
    "v_cvt_pk_bf16_f32 v165, v138, v139\n"
    "v_mfma_f32_16x16x32_bf16 v[148:151], a[148:151], a[36:39], v[148:151]\n"
    "v_exp_f32 v141, v141\n"
    "v_mfma_f32_16x16x32_bf16 v[148:151], a[152:155], a[40:43], v[148:151]\n"
    "v_mul_f32 v142, s54, v142\n"
    "v_mul_f32 v143, s54, v143\n"
    "v_mfma_f32_16x16x32_bf16 v[148:151], a[156:159], a[44:47], v[148:151]\n"
    "v_exp_f32 v142, v142\n"
    "v_mfma_f32_16x16x32_bf16 v[152:155], a[128:131], a[48:51], v[212:215]\n"
    "s_waitcnt lgkmcnt(0)\n"
    "s_add_u32 m0, s52, 0x14380\n"
    "v_exp_f32 v143, v143\n"
    "v_mfma_f32_16x16x32_bf16 v[152:155], a[132:135], a[52:55], v[152:155]\n"
    "buffer_load_dwordx4 v183, s[40:43], s71 offen offset:128 nt lds\n"
    "v_cvt_pk_bf16_f32 v166, v140, v141\n"
    "v_mfma_f32_16x16x32_bf16 v[152:155], a[136:139], a[56:59], v[152:155]\n"
    "ds_read_b128 a[128:131], v177 offset:40960\n"
    "v_cvt_pk_bf16_f32 v167, v142, v143\n"
    "v_mfma_f32_16x16x32_bf16 v[152:155], a[140:143], a[60:63], v[152:155]\n"
    "v_mul_f32 v144, s54, v144\n"
    "v_mul_f32 v145, s54, v145\n"
    "v_mfma_f32_16x16x32_bf16 v[156:159], a[144:147], a[48:51], v[212:215]\n"
    "ds_read_b128 a[132:135], v177 offset:41472\n"
    "v_mul_f32 v146, s54, v146\n"
    "v_mfma_f32_16x16x32_bf16 v[156:159], a[148:151], a[52:55], v[156:159]\n"
    "v_exp_f32 v144, v144\n"
    "v_mfma_f32_16x16x32_bf16 v[156:159], a[152:155], a[56:59], v[156:159]\n"
    "ds_read_b128 a[136:139], v177 offset:41984\n"
    "v_mul_f32 v147, s54, v147\n"
    "v_mfma_f32_16x16x32_bf16 v[156:159], a[156:159], a[60:63], v[156:159]\n"
    "v_exp_f32 v145, v145\n"
    "v_mul_f32 v148, s54, v148\n"
    "v_mfma_f32_16x16x32_bf16 v[0:3], a[160:163], v[160:163], v[0:3]\n"
    "ds_read_b128 a[140:143], v177 offset:42496\n"
    "v_cvt_pk_bf16_f32 v168, v144, v145\n"
    "v_mfma_f32_16x16x32_bf16 v[4:7], a[164:167], v[160:163], v[4:7]\n"
    "v_exp_f32 v146, v146\n"
    "v_mfma_f32_16x16x32_bf16 v[8:11], a[168:171], v[160:163], v[8:11]\n"
    "ds_read_b128 a[144:147], v177 offset:45056\n"
    "v_mul_f32 v149, s54, v149\n"
    "v_mfma_f32_16x16x32_bf16 v[12:15], a[172:175], v[160:163], v[12:15]\n"
    "v_exp_f32 v147, v147\n"
    "v_mfma_f32_16x16x32_bf16 v[16:19], a[176:179], v[160:163], v[16:19]\n"
    "ds_read_b128 a[148:151], v177 offset:45568\n"
    "v_cvt_pk_bf16_f32 v169, v146, v147\n"
    "v_mfma_f32_16x16x32_bf16 v[20:23], a[180:183], v[160:163], v[20:23]\n"
    "buffer_load_dwordx4 v183, s[40:43], s71 offen offset:2176 nt lds\n"
    "v_mul_f32 v150, s54, v150\n"
    "v_mfma_f32_16x16x32_bf16 v[24:27], a[184:187], v[160:163], v[24:27]\n"
    "ds_read_b128 a[152:155], v177 offset:46080\n"
    "v_mul_f32 v151, s54, v151\n"
    "v_mfma_f32_16x16x32_bf16 v[28:31], a[188:191], v[160:163], v[28:31]\n"
    "v_exp_f32 v148, v148\n"
    "v_mfma_f32_16x16x32_bf16 a[224:227], a[240:243], v[160:163], a[224:227]\n"
    "ds_read_b128 a[156:159], v177 offset:46592\n"
    "v_mfma_f32_16x16x32_bf16 v[32:35], a[160:163], v[164:167], v[32:35]\n"
    "v_exp_f32 v149, v149\n"
    "v_mfma_f32_16x16x32_bf16 v[36:39], a[164:167], v[164:167], v[36:39]\n"
    "v_exp_f32 v150, v150\n"
    "v_mfma_f32_16x16x32_bf16 v[40:43], a[168:171], v[164:167], v[40:43]\n"
    "v_exp_f32 v151, v151\n"
    "v_cvt_pk_bf16_f32 v170, v148, v149\n"
    "v_mfma_f32_16x16x32_bf16 v[44:47], a[172:175], v[164:167], v[44:47]\n"
    "v_cvt_pk_bf16_f32 v171, v150, v151\n"
    "v_mul_f32 v152, s54, v152\n"
    "v_mfma_f32_16x16x32_bf16 v[48:51], a[176:179], v[164:167], v[48:51]\n"
    "v_exp_f32 v152, v152\n"
    "v_mfma_f32_16x16x32_bf16 v[52:55], a[180:183], v[164:167], v[52:55]\n"
    "ds_read_b64_tr_b16 a[192:193], v179 offset:40960\n"
    "v_mul_f32 v153, s54, v153\n"
    "v_mfma_f32_16x16x32_bf16 v[56:59], a[184:187], v[164:167], v[56:59]\n"
    "v_exp_f32 v153, v153\n"
    "v_mfma_f32_16x16x32_bf16 v[60:63], a[188:191], v[164:167], v[60:63]\n"
    "s_waitcnt lgkmcnt(1)\n"
    "ds_read_b64_tr_b16 a[194:195], v179 offset:45056\n"
    "v_mul_f32 v154, s54, v154\n"
    "v_mfma_f32_16x16x32_bf16 a[228:231], a[240:243], v[164:167], a[228:231]\n"
    "v_exp_f32 v154, v154\n"
    "v_mfma_f32_16x16x32_bf16 v[128:131], a[128:131], a[0:3], v[248:251]\n"
    "v_mul_f32 v155, s54, v155\n"
    "v_cvt_pk_bf16_f32 v172, v152, v153\n"
    "v_mfma_f32_16x16x32_bf16 v[128:131], a[132:135], a[4:7], v[128:131]\n"
    "v_exp_f32 v155, v155\n"
    "v_mfma_f32_16x16x32_bf16 v[128:131], a[136:139], a[8:11], v[128:131]\n"
    "ds_read_b64_tr_b16 a[196:197], v179 offset:41216\n"
    "v_mul_f32 v156, s54, v156\n"
    "v_mfma_f32_16x16x32_bf16 v[128:131], a[140:143], a[12:15], v[128:131]\n"
    "v_exp_f32 v156, v156\n"
    "v_mfma_f32_16x16x32_bf16 v[132:135], a[144:147], a[0:3], v[248:251]\n"
    "v_mul_f32 v157, s54, v157\n"
    "v_cvt_pk_bf16_f32 v173, v154, v155\n"
    "v_mfma_f32_16x16x32_bf16 v[132:135], a[148:151], a[4:7], v[132:135]\n"
    "v_exp_f32 v157, v157\n"
    "v_mfma_f32_16x16x32_bf16 v[132:135], a[152:155], a[8:11], v[132:135]\n"
    "ds_read_b64_tr_b16 a[198:199], v179 offset:45312\n"
    "v_mul_f32 v158, s54, v158\n"
    "v_mfma_f32_16x16x32_bf16 v[132:135], a[156:159], a[12:15], v[132:135]\n"
    "v_exp_f32 v158, v158\n"
    "v_mfma_f32_16x16x32_bf16 v[136:139], a[128:131], a[16:19], v[252:255]\n"
    "v_mul_f32 v159, s54, v159\n"
    "v_cvt_pk_bf16_f32 v174, v156, v157\n"
    "v_mfma_f32_16x16x32_bf16 v[136:139], a[132:135], a[20:23], v[136:139]\n"
    "v_exp_f32 v159, v159\n"
    "v_mfma_f32_16x16x32_bf16 v[136:139], a[136:139], a[24:27], v[136:139]\n"
    "v_cvt_pk_bf16_f32 v175, v158, v159\n"
    "v_mfma_f32_16x16x32_bf16 v[136:139], a[140:143], a[28:31], v[136:139]\n"
    "v_mul_f32 v128, s54, v128\n"
    "v_mul_f32 v129, s54, v129\n"
    "v_mfma_f32_16x16x32_bf16 v[140:143], a[144:147], a[16:19], v[252:255]\n"
    "ds_read_b64_tr_b16 a[200:201], v179 offset:41472\n"
    "v_mul_f32 v130, s54, v130\n"
    "v_mfma_f32_16x16x32_bf16 v[140:143], a[148:151], a[20:23], v[140:143]\n"
    "v_exp_f32 v128, v128\n"
    "v_mfma_f32_16x16x32_bf16 v[140:143], a[152:155], a[24:27], v[140:143]\n"
    "ds_read_b64_tr_b16 a[202:203], v179 offset:45568\n"
    "v_mul_f32 v131, s54, v131\n"
    "v_mfma_f32_16x16x32_bf16 v[140:143], a[156:159], a[28:31], v[140:143]\n"
    "v_exp_f32 v129, v129\n"
    "v_mul_f32 v132, s54, v132\n"
    "v_mfma_f32_16x16x32_bf16 v[64:67], a[160:163], v[168:171], v[64:67]\n"
    "ds_read_b64_tr_b16 a[204:205], v179 offset:41728\n"
    "v_cvt_pk_bf16_f32 v160, v128, v129\n"
    "v_mfma_f32_16x16x32_bf16 v[68:71], a[164:167], v[168:171], v[68:71]\n"
    "v_exp_f32 v130, v130\n"
    "v_accvgpr_mov_b32 a0, a64\n"
    "v_mfma_f32_16x16x32_bf16 v[72:75], a[168:171], v[168:171], v[72:75]\n"
    "ds_read_b64_tr_b16 a[206:207], v179 offset:45824\n"
    "v_mul_f32 v133, s54, v133\n"
    "v_mfma_f32_16x16x32_bf16 v[76:79], a[172:175], v[168:171], v[76:79]\n"
    "v_exp_f32 v131, v131\n"
    "v_accvgpr_mov_b32 a1, a65\n"
    "v_mfma_f32_16x16x32_bf16 v[80:83], a[176:179], v[168:171], v[80:83]\n"
    "ds_read_b64_tr_b16 a[208:209], v179 offset:41984\n"
    "v_cvt_pk_bf16_f32 v161, v130, v131\n"
    "v_accvgpr_mov_b32 a2, a66\n"
    "v_mfma_f32_16x16x32_bf16 v[84:87], a[180:183], v[168:171], v[84:87]\n"
    "v_exp_f32 v132, v132\n"
    "v_mfma_f32_16x16x32_bf16 v[88:91], a[184:187], v[168:171], v[88:91]\n"
    "ds_read_b64_tr_b16 a[210:211], v179 offset:46080\n"
    "v_mul_f32 v134, s54, v134\n"
    "v_accvgpr_mov_b32 a3, a67\n"
    "v_mfma_f32_16x16x32_bf16 v[92:95], a[188:191], v[168:171], v[92:95]\n"
    "v_exp_f32 v133, v133\n"
    "v_mul_f32 v135, s54, v135\n"
    "v_accvgpr_mov_b32 a4, a68\n"
    "v_mfma_f32_16x16x32_bf16 a[232:235], a[240:243], v[168:171], a[232:235]\n"
    "ds_read_b64_tr_b16 a[212:213], v179 offset:42240\n"
    "v_cvt_pk_bf16_f32 v162, v132, v133\n"
    "v_mfma_f32_16x16x32_bf16 v[96:99], a[160:163], v[172:175], v[96:99]\n"
    "v_exp_f32 v134, v134\n"
    "v_accvgpr_mov_b32 a5, a69\n"
    "v_mfma_f32_16x16x32_bf16 v[100:103], a[164:167], v[172:175], v[100:103]\n"
    "ds_read_b64_tr_b16 a[214:215], v179 offset:46336\n"
    "v_accvgpr_mov_b32 a6, a70\n"
    "v_mfma_f32_16x16x32_bf16 v[104:107], a[168:171], v[172:175], v[104:107]\n"
    "v_exp_f32 v135, v135\n"
    "v_accvgpr_mov_b32 a7, a71\n"
    "v_mfma_f32_16x16x32_bf16 v[108:111], a[172:175], v[172:175], v[108:111]\n"
    "v_cvt_pk_bf16_f32 v163, v134, v135\n"
    "v_mul_f32 v136, s54, v136\n"
    "v_accvgpr_mov_b32 a8, a72\n"
    "v_mfma_f32_16x16x32_bf16 v[112:115], a[176:179], v[172:175], v[112:115]\n"
    "v_exp_f32 v136, v136\n"
    "v_mfma_f32_16x16x32_bf16 v[116:119], a[180:183], v[172:175], v[116:119]\n"
    "ds_read_b64_tr_b16 a[216:217], v179 offset:42496\n"
    "v_mul_f32 v137, s54, v137\n"
    "v_accvgpr_mov_b32 a9, a73\n"
    "v_mfma_f32_16x16x32_bf16 v[120:123], a[184:187], v[172:175], v[120:123]\n"
    "v_exp_f32 v137, v137\n"
    "v_accvgpr_mov_b32 a10, a74\n"
    "v_mfma_f32_16x16x32_bf16 v[124:127], a[188:191], v[172:175], v[124:127]\n"
    "v_mul_f32 v138, s54, v138\n"
    "v_mul_f32 v139, s54, v139\n"
    "v_mfma_f32_16x16x32_bf16 a[236:239], a[240:243], v[172:175], a[236:239]\n"
    "v_exp_f32 v138, v138\n"
    "v_accvgpr_mov_b32 a11, a75\n"
    "v_mfma_f32_16x16x32_bf16 v[144:147], a[128:131], a[32:35], v[196:199]\n"
    "ds_read_b64_tr_b16 a[218:219], v179 offset:46592\n"
    "v_cvt_pk_bf16_f32 v164, v136, v137\n"
    "v_accvgpr_mov_b32 a12, a76\n"
    "v_mfma_f32_16x16x32_bf16 v[144:147], a[132:135], a[36:39], v[144:147]\n"
    "v_exp_f32 v139, v139\n"
    "v_mfma_f32_16x16x32_bf16 v[144:147], a[136:139], a[40:43], v[144:147]\n"
    "ds_read_b64_tr_b16 a[220:221], v179 offset:42752\n"
    "v_mul_f32 v140, s54, v140\n"
    "v_accvgpr_mov_b32 a13, a77\n"
    "v_mfma_f32_16x16x32_bf16 v[144:147], a[140:143], a[44:47], v[144:147]\n"
    "v_exp_f32 v140, v140\n"
    "v_accvgpr_mov_b32 a14, a78\n"
    "v_mfma_f32_16x16x32_bf16 v[148:151], a[144:147], a[32:35], v[196:199]\n"
    "ds_read_b64_tr_b16 a[222:223], v179 offset:46848\n"
    "v_mul_f32 v141, s54, v141\n"
    "v_mfma_f32_16x16x32_bf16 v[148:151], a[148:151], a[36:39], v[148:151]\n"
    "v_exp_f32 v141, v141\n"
    "v_accvgpr_mov_b32 a15, a79\n"
    "v_mfma_f32_16x16x32_bf16 v[148:151], a[152:155], a[40:43], v[148:151]\n"
    "v_cvt_pk_bf16_f32 v165, v138, v139\n"
    "v_mul_f32 v142, s54, v142\n"
    "v_accvgpr_mov_b32 a16, a80\n"
    "v_mfma_f32_16x16x32_bf16 v[148:151], a[156:159], a[44:47], v[148:151]\n"
    "v_exp_f32 v142, v142\n"
    "v_mfma_f32_16x16x32_bf16 v[152:155], a[128:131], a[48:51], v[212:215]\n"
    "s_waitcnt vmcnt(16) lgkmcnt(0)\n"
    "s_barrier\n"
    "v_mul_f32 v143, s54, v143\n"
    "v_cvt_pk_bf16_f32 v166, v140, v141\n"
    "v_accvgpr_mov_b32 a17, a81\n"
    "v_mfma_f32_16x16x32_bf16 v[152:155], a[132:135], a[52:55], v[152:155]\n"
    "v_exp_f32 v143, v143\n"
    "v_accvgpr_mov_b32 a18, a82\n"
    "v_mfma_f32_16x16x32_bf16 v[152:155], a[136:139], a[56:59], v[152:155]\n"
    "ds_read_b128 a[128:131], v176 offset:0\n"
    "v_cvt_pk_bf16_f32 v167, v142, v143\n"
    "v_mfma_f32_16x16x32_bf16 v[152:155], a[140:143], a[60:63], v[152:155]\n"
    "v_mul_f32 v144, s54, v144\n"
    "v_mul_f32 v145, s54, v145\n"
    "v_accvgpr_mov_b32 a19, a83\n"
    "v_mfma_f32_16x16x32_bf16 v[156:159], a[144:147], a[48:51], v[212:215]\n"
    "ds_read_b128 a[132:135], v176 offset:512\n"
    "v_exp_f32 v144, v144\n"
    "v_accvgpr_mov_b32 a20, a84\n"
    "v_mfma_f32_16x16x32_bf16 v[156:159], a[148:151], a[52:55], v[156:159]\n"
    "v_exp_f32 v145, v145\n"
    "v_mfma_f32_16x16x32_bf16 v[156:159], a[152:155], a[56:59], v[156:159]\n"
    "ds_read_b128 a[136:139], v176 offset:1024\n"
    "v_mul_f32 v146, s54, v146\n"
    "v_mul_f32 v147, s54, v147\n"
    "v_accvgpr_mov_b32 a21, a85\n"
    "v_mfma_f32_16x16x32_bf16 v[156:159], a[156:159], a[60:63], v[156:159]\n"
    "v_exp_f32 v146, v146\n"
    "v_accvgpr_mov_b32 a22, a86\n"
    "v_mfma_f32_16x16x32_bf16 v[0:3], a[192:195], v[160:163], v[0:3]\n"
    "ds_read_b128 a[140:143], v176 offset:1536\n"
    "v_cvt_pk_bf16_f32 v168, v144, v145\n"
    "v_mfma_f32_16x16x32_bf16 v[4:7], a[196:199], v[160:163], v[4:7]\n"
    "ds_read_b128 a[32:35], v180 offset:0\n"
    "v_exp_f32 v147, v147\n"
    "v_accvgpr_mov_b32 a23, a87\n"
    "v_mfma_f32_16x16x32_bf16 v[8:11], a[200:203], v[160:163], v[8:11]\n"
    "ds_read_b128 a[144:147], v176 offset:4096\n"
    "v_mul_f32 v148, s54, v148\n"
    "v_accvgpr_mov_b32 a24, a88\n"
    "v_mfma_f32_16x16x32_bf16 v[12:15], a[204:207], v[160:163], v[12:15]\n"
    "ds_read_b128 a[36:39], v180 offset:512\n"
    "v_exp_f32 v148, v148\n"
    "v_mfma_f32_16x16x32_bf16 v[16:19], a[208:211], v[160:163], v[16:19]\n"
    "ds_read_b128 a[148:151], v176 offset:4608\n"
    "v_mul_f32 v149, s54, v149\n"
    "v_accvgpr_mov_b32 a25, a89\n"
    "v_mfma_f32_16x16x32_bf16 v[20:23], a[212:215], v[160:163], v[20:23]\n"
    "ds_read_b128 a[48:51], v180 offset:2048\n"
    "v_exp_f32 v149, v149\n"
    "v_accvgpr_mov_b32 a26, a90\n"
    "v_mfma_f32_16x16x32_bf16 v[24:27], a[216:219], v[160:163], v[24:27]\n"
    "ds_read_b128 a[152:155], v176 offset:5120\n"
    "v_cvt_pk_bf16_f32 v169, v146, v147\n"
    "v_mfma_f32_16x16x32_bf16 v[28:31], a[220:223], v[160:163], v[28:31]\n"
    "v_mul_f32 v150, s54, v150\n"
    "v_mul_f32 v151, s54, v151\n"
    "v_accvgpr_mov_b32 a27, a91\n"
    "v_mfma_f32_16x16x32_bf16 a[224:227], a[240:243], v[160:163], a[224:227]\n"
    "ds_read_b128 a[156:159], v176 offset:5632\n"
    "v_exp_f32 v150, v150\n"
    "v_accvgpr_mov_b32 a28, a92\n"
    "v_mfma_f32_16x16x32_bf16 v[32:35], a[192:195], v[164:167], v[32:35]\n"
    "ds_read_b128 a[52:55], v180 offset:2560\n"
    "v_cvt_pk_bf16_f32 v170, v148, v149\n"
    "v_mfma_f32_16x16x32_bf16 v[36:39], a[196:199], v[164:167], v[36:39]\n"
    "ds_read_b128 a[40:43], v180 offset:16384\n"
    "v_exp_f32 v151, v151\n"
    "v_accvgpr_mov_b32 a29, a93\n"
    "v_mfma_f32_16x16x32_bf16 v[40:43], a[200:203], v[164:167], v[40:43]\n"
    "ds_read_b128 a[44:47], v180 offset:16896\n"
    "v_cvt_pk_bf16_f32 v171, v150, v151\n"
    "v_accvgpr_mov_b32 a30, a94\n"
    "v_mfma_f32_16x16x32_bf16 v[44:47], a[204:207], v[164:167], v[44:47]\n"
    "ds_read_b128 a[56:59], v180 offset:18432\n"
    "v_mul_f32 v152, s54, v152\n"
    "v_mul_f32 v153, s54, v153\n"
    "v_mfma_f32_16x16x32_bf16 v[48:51], a[208:211], v[164:167], v[48:51]\n"
    "ds_read_b128 a[60:63], v180 offset:18944\n"
    "v_exp_f32 v152, v152\n"
    "v_accvgpr_mov_b32 a31, a95\n"
    "v_mfma_f32_16x16x32_bf16 v[52:55], a[212:215], v[164:167], v[52:55]\n"
    "ds_read_b64_tr_b16 a[160:161], v178 offset:0\n"
    "v_exp_f32 v153, v153\n"
    "v_mfma_f32_16x16x32_bf16 v[56:59], a[216:219], v[164:167], v[56:59]\n"
    "v_mul_f32 v154, s54, v154\n"
    "v_exp_f32 v154, v154\n"
    "v_mfma_f32_16x16x32_bf16 v[60:63], a[220:223], v[164:167], v[60:63]\n"
    "s_waitcnt lgkmcnt(6)\n"
    "ds_read_b64_tr_b16 a[162:163], v178 offset:4096\n"
    "v_mul_f32 v155, s54, v155\n"
    "v_exp_f32 v155, v155\n"
    "v_mfma_f32_16x16x32_bf16 a[228:231], a[240:243], v[164:167], a[228:231]\n"
    "v_cvt_pk_bf16_f32 v172, v152, v153\n"
    "v_mul_f32 v156, s54, v156\n"
    "v_mul_f32 v157, s54, v157\n"
    "s_cmp_eq_u32 s57, 3\n"
    "s_cbranch_scc1 Lmha_TAIL\n"
    "s_branch Lmha_P0\n"
    "Lmha_TAIL:\n"
    "v_mfma_f32_16x16x32_bf16 v[64:67], a[192:195], v[168:171], v[64:67]\n"
    "s_add_u32 s48, s48, 0x1000000\n"
    "s_addc_u32 s49, s49, 0\n"
    "v_exp_f32 v156, v156\n"
    "v_cvt_pk_bf16_f32 v173, v154, v155\n"
    "v_subrev_u32 v187, 0xc000, v187\n"
    "v_subrev_u32 v188, 0xc000, v188\n"
    "v_subrev_u32 v189, 0xc000, v189\n"
    "v_subrev_u32 v190, 0xc000, v190\n"
    "v_subrev_u32 v191, 0xc000, v191\n"
    "v_subrev_u32 v192, 0xc000, v192\n"
    "v_subrev_u32 v193, 0xc000, v193\n"
    "v_subrev_u32 v194, 0xc000, v194\n"
    "v_subrev_u32 v185, 0xc000, v185\n"
    "v_subrev_u32 v186, 0xc000, v186\n"
    "v_accvgpr_read_b32 v200, a224\n"
    "v_mfma_f32_16x16x32_bf16 v[68:71], a[196:199], v[168:171], v[68:71]\n"
    "s_mov_b32 s50, 0x10000\n"
    "v_mul_f32 v158, s54, v158\n"
    "v_cmp_nle_f32 vcc, v200, v206\n"
    "v_rcp_f32 v200, v200\n"
    "v_mfma_f32_16x16x32_bf16 v[72:75], a[200:203], v[168:171], v[72:75]\n"
    "s_or_b64 s[60:61], s[60:61], vcc\n"
    "v_mul_f32 v159, s54, v159\n"
    "v_exp_f32 v157, v157\n"
    "v_mul_f32 v0, v0, v200\n"
    "v_mul_f32 v1, v1, v200\n"
    "v_mul_f32 v2, v2, v200\n"
    "v_mul_f32 v3, v3, v200\n"
    "v_cvt_pk_bf16_f32 v208, v0, v1\n"
    "v_cvt_pk_bf16_f32 v209, v2, v3\n"
    "v_mfma_f32_16x16x32_bf16 v[76:79], a[204:207], v[168:171], v[76:79]\n"
    "ds_write_b64 v187, v[208:209] offset:0\n"
    "v_cvt_pk_bf16_f32 v174, v156, v157\n"
    "v_accvgpr_read_b32 v201, a228\n"
    "v_mul_f32 v4, v4, v200\n"
    "v_mul_f32 v5, v5, v200\n"
    "v_mul_f32 v6, v6, v200\n"
    "v_mul_f32 v7, v7, v200\n"
    "v_cvt_pk_bf16_f32 v210, v4, v5\n"
    "v_cvt_pk_bf16_f32 v211, v6, v7\n"
    "v_mfma_f32_16x16x32_bf16 v[80:83], a[208:211], v[168:171], v[80:83]\n"
    "ds_write_b64 v188, v[210:211] offset:0\n"
    "v_exp_f32 v158, v158\n"
    "v_exp_f32 v159, v159\n"
    "v_cmp_nle_f32 vcc, v201, v206\n"
    "v_rcp_f32 v201, v201\n"
    "v_mul_f32 v8, v8, v200\n"
    "v_mul_f32 v9, v9, v200\n"
    "v_mul_f32 v10, v10, v200\n"
    "v_mul_f32 v11, v11, v200\n"
    "v_cvt_pk_bf16_f32 v208, v8, v9\n"
    "v_cvt_pk_bf16_f32 v209, v10, v11\n"
    "v_mfma_f32_16x16x32_bf16 v[84:87], a[212:215], v[168:171], v[84:87]\n"
    "s_or_b64 s[60:61], s[60:61], vcc\n"
    "ds_write_b64 v189, v[208:209] offset:0\n"
    "v_cvt_pk_bf16_f32 v175, v158, v159\n"
    "v_mul_f32 v12, v12, v200\n"
    "v_mul_f32 v13, v13, v200\n"
    "v_mul_f32 v14, v14, v200\n"
    "v_mul_f32 v15, v15, v200\n"
    "v_cvt_pk_bf16_f32 v210, v12, v13\n"
    "v_cvt_pk_bf16_f32 v211, v14, v15\n"
    "v_mfma_f32_16x16x32_bf16 v[88:91], a[216:219], v[168:171], v[88:91]\n"
    "ds_write_b64 v190, v[210:211] offset:0\n"
    "v_mul_f32 v16, v16, v200\n"
    "v_mul_f32 v17, v17, v200\n"
    "v_mul_f32 v18, v18, v200\n"
    "v_mul_f32 v19, v19, v200\n"
    "v_cvt_pk_bf16_f32 v208, v16, v17\n"
    "v_cvt_pk_bf16_f32 v209, v18, v19\n"
    "v_mfma_f32_16x16x32_bf16 v[92:95], a[220:223], v[168:171], v[92:95]\n"
    "ds_write_b64 v191, v[208:209] offset:0\n"
    "v_mul_f32 v20, v20, v200\n"
    "v_mul_f32 v21, v21, v200\n"
    "v_mul_f32 v22, v22, v200\n"
    "v_mul_f32 v23, v23, v200\n"
    "v_cvt_pk_bf16_f32 v210, v20, v21\n"
    "v_cvt_pk_bf16_f32 v211, v22, v23\n"
    "v_mfma_f32_16x16x32_bf16 a[232:235], a[240:243], v[168:171], a[232:235]\n"
    "ds_write_b64 v192, v[210:211] offset:0\n"
    "v_mul_f32 v24, v24, v200\n"
    "v_mul_f32 v25, v25, v200\n"
    "v_mul_f32 v26, v26, v200\n"
    "v_mul_f32 v27, v27, v200\n"
    "v_cvt_pk_bf16_f32 v208, v24, v25\n"
    "v_cvt_pk_bf16_f32 v209, v26, v27\n"
    "v_mfma_f32_16x16x32_bf16 v[96:99], a[192:195], v[172:175], v[96:99]\n"
    "ds_write_b64 v193, v[208:209] offset:0\n"
    "v_mul_f32 v28, v28, v200\n"
    "v_mul_f32 v29, v29, v200\n"
    "v_mul_f32 v30, v30, v200\n"
    "v_mul_f32 v31, v31, v200\n"
    "v_cvt_pk_bf16_f32 v210, v28, v29\n"
    "v_cvt_pk_bf16_f32 v211, v30, v31\n"
    "v_mfma_f32_16x16x32_bf16 v[100:103], a[196:199], v[172:175], v[100:103]\n"
    "ds_write_b64 v194, v[210:211] offset:0\n"
    "v_mul_f32 v32, v32, v201\n"
    "v_mul_f32 v33, v33, v201\n"
    "v_mul_f32 v34, v34, v201\n"
    "v_mul_f32 v35, v35, v201\n"
    "v_cvt_pk_bf16_f32 v208, v32, v33\n"
    "v_cvt_pk_bf16_f32 v209, v34, v35\n"
    "v_mfma_f32_16x16x32_bf16 v[104:107], a[200:203], v[172:175], v[104:107]\n"
    "ds_write_b64 v187, v[208:209] offset:16384\n"
    "v_mul_f32 v36, v36, v201\n"
    "v_mul_f32 v37, v37, v201\n"
    "v_mul_f32 v38, v38, v201\n"
    "v_mul_f32 v39, v39, v201\n"
    "v_cvt_pk_bf16_f32 v210, v36, v37\n"
    "v_cvt_pk_bf16_f32 v211, v38, v39\n"
    "v_mfma_f32_16x16x32_bf16 v[108:111], a[204:207], v[172:175], v[108:111]\n"
    "s_waitcnt lgkmcnt(0)\n"
    "ds_read_b128 v[216:219], v185 offset:0\n"
    "ds_read_b128 v[220:223], v186 offset:1024\n"
    "ds_write_b64 v188, v[210:211] offset:16384\n"
    "v_accvgpr_read_b32 v202, a232\n"
    "v_mul_f32 v40, v40, v201\n"
    "v_mul_f32 v41, v41, v201\n"
    "v_mul_f32 v42, v42, v201\n"
    "v_mul_f32 v43, v43, v201\n"
    "v_cvt_pk_bf16_f32 v208, v40, v41\n"
    "v_cvt_pk_bf16_f32 v209, v42, v43\n"
    "v_mfma_f32_16x16x32_bf16 v[112:115], a[208:211], v[172:175], v[112:115]\n"
    "ds_read_b128 v[224:227], v185 offset:2048\n"
    "ds_read_b128 v[228:231], v186 offset:3072\n"
    "ds_write_b64 v189, v[208:209] offset:16384\n"
    "v_cmp_nle_f32 vcc, v202, v206\n"
    "v_rcp_f32 v202, v202\n"
    "v_mul_f32 v44, v44, v201\n"
    "v_mul_f32 v45, v45, v201\n"
    "v_mul_f32 v46, v46, v201\n"
    "v_mul_f32 v47, v47, v201\n"
    "v_cvt_pk_bf16_f32 v210, v44, v45\n"
    "v_cvt_pk_bf16_f32 v211, v46, v47\n"
    "v_mfma_f32_16x16x32_bf16 v[116:119], a[212:215], v[172:175], v[116:119]\n"
    "s_or_b64 s[60:61], s[60:61], vcc\n"
    "ds_write_b64 v190, v[210:211] offset:16384\n"
    "v_mul_f32 v48, v48, v201\n"
    "v_mul_f32 v49, v49, v201\n"
    "v_mul_f32 v50, v50, v201\n"
    "v_mul_f32 v51, v51, v201\n"
    "v_cvt_pk_bf16_f32 v208, v48, v49\n"
    "v_cvt_pk_bf16_f32 v209, v50, v51\n"
    "v_mfma_f32_16x16x32_bf16 v[120:123], a[216:219], v[172:175], v[120:123]\n"
    "s_waitcnt lgkmcnt(2)\n"
    "buffer_store_dwordx4 v[216:219], v184, s[48:51], 0 offen offset:0 sc1\n"
    "ds_write_b64 v191, v[208:209] offset:16384\n"
    "v_mul_f32 v52, v52, v201\n"
    "v_mul_f32 v53, v53, v201\n"
    "v_mul_f32 v54, v54, v201\n"
    "v_mul_f32 v55, v55, v201\n"
    "v_cvt_pk_bf16_f32 v210, v52, v53\n"
    "v_cvt_pk_bf16_f32 v211, v54, v55\n"
    "v_mfma_f32_16x16x32_bf16 v[124:127], a[220:223], v[172:175], v[124:127]\n"
    "ds_write_b64 v192, v[210:211] offset:16384\n"
    "v_mul_f32 v56, v56, v201\n"
    "v_mul_f32 v57, v57, v201\n"
    "v_mul_f32 v58, v58, v201\n"
    "v_mul_f32 v59, v59, v201\n"
    "v_cvt_pk_bf16_f32 v208, v56, v57\n"
    "v_cvt_pk_bf16_f32 v209, v58, v59\n"
    "v_mfma_f32_16x16x32_bf16 a[236:239], a[240:243], v[172:175], a[236:239]\n"
    "buffer_store_dwordx4 v[220:223], v184, s[48:51], 0 offen offset:1024 sc1\n"
    "ds_write_b64 v193, v[208:209] offset:16384\n"
    "v_mul_f32 v60, v60, v201\n"
    "v_mul_f32 v61, v61, v201\n"
    "v_mul_f32 v62, v62, v201\n"
    "v_mul_f32 v63, v63, v201\n"
    "v_cvt_pk_bf16_f32 v210, v60, v61\n"
    "v_cvt_pk_bf16_f32 v211, v62, v63\n"
    "ds_write_b64 v194, v[210:211] offset:16384\n"
    "v_mul_f32 v64, v64, v202\n"
    "v_mul_f32 v65, v65, v202\n"
    "v_mul_f32 v66, v66, v202\n"
    "v_mul_f32 v67, v67, v202\n"
    "v_cvt_pk_bf16_f32 v208, v64, v65\n"
    "v_cvt_pk_bf16_f32 v209, v66, v67\n"
    "buffer_store_dwordx4 v[224:227], v184, s[48:51], 0 offen offset:2048 sc1\n"
    "ds_write_b64 v187, v[208:209] offset:32768\n"
    "v_mul_f32 v68, v68, v202\n"
    "v_mul_f32 v69, v69, v202\n"
    "v_mul_f32 v70, v70, v202\n"
    "v_mul_f32 v71, v71, v202\n"
    "v_cvt_pk_bf16_f32 v210, v68, v69\n"
    "v_cvt_pk_bf16_f32 v211, v70, v71\n"
    "s_waitcnt lgkmcnt(0)\n"
    "ds_read_b128 v[232:235], v185 offset:16384\n"
    "ds_read_b128 v[236:239], v186 offset:17408\n"
    "ds_write_b64 v188, v[210:211] offset:32768\n"
    "v_mul_f32 v72, v72, v202\n"
    "v_mul_f32 v73, v73, v202\n"
    "v_mul_f32 v74, v74, v202\n"
    "v_mul_f32 v75, v75, v202\n"
    "v_cvt_pk_bf16_f32 v208, v72, v73\n"
    "v_cvt_pk_bf16_f32 v209, v74, v75\n"
    "buffer_store_dwordx4 v[228:231], v184, s[48:51], 0 offen offset:3072 sc1\n"
    "ds_read_b128 v[240:243], v185 offset:18432\n"
    "ds_read_b128 v[244:247], v186 offset:19456\n"
    "ds_write_b64 v189, v[208:209] offset:32768\n"
    "v_accvgpr_read_b32 v203, a236\n"
    "v_mul_f32 v76, v76, v202\n"
    "v_mul_f32 v77, v77, v202\n"
    "v_mul_f32 v78, v78, v202\n"
    "v_mul_f32 v79, v79, v202\n"
    "v_cvt_pk_bf16_f32 v210, v76, v77\n"
    "v_cvt_pk_bf16_f32 v211, v78, v79\n"
    "ds_write_b64 v190, v[210:211] offset:32768\n"
    "v_cmp_nle_f32 vcc, v203, v206\n"
    "v_rcp_f32 v203, v203\n"
    "v_mul_f32 v80, v80, v202\n"
    "v_mul_f32 v81, v81, v202\n"
    "v_mul_f32 v82, v82, v202\n"
    "v_mul_f32 v83, v83, v202\n"
    "v_cvt_pk_bf16_f32 v208, v80, v81\n"
    "v_cvt_pk_bf16_f32 v209, v82, v83\n"
    "s_or_b64 s[60:61], s[60:61], vcc\n"
    "s_waitcnt lgkmcnt(2)\n"
    "buffer_store_dwordx4 v[232:235], v184, s[48:51], s66 offen offset:0 sc1\n"
    "ds_write_b64 v191, v[208:209] offset:32768\n"
    "v_mul_f32 v84, v84, v202\n"
    "v_mul_f32 v85, v85, v202\n"
    "v_mul_f32 v86, v86, v202\n"
    "v_mul_f32 v87, v87, v202\n"
    "v_cvt_pk_bf16_f32 v210, v84, v85\n"
    "v_cvt_pk_bf16_f32 v211, v86, v87\n"
    "ds_write_b64 v192, v[210:211] offset:32768\n"
    "v_mul_f32 v88, v88, v202\n"
    "v_mul_f32 v89, v89, v202\n"
    "v_mul_f32 v90, v90, v202\n"
    "v_mul_f32 v91, v91, v202\n"
    "v_cvt_pk_bf16_f32 v208, v88, v89\n"
    "v_cvt_pk_bf16_f32 v209, v90, v91\n"
    "s_cmp_lg_u64 s[60:61], 0\n"
    "s_cselect_b32 s59, 1, 0\n"
    "s_lshl_b32 s59, s59, s57\n"
    "s_or_b32 s62, s62, s59\n"
    "s_mov_b64 s[60:61], 0\n"
    "buffer_store_dwordx4 v[236:239], v184, s[48:51], s66 offen offset:1024 sc1\n"
    "ds_write_b64 v193, v[208:209] offset:32768\n"
    "v_mul_f32 v92, v92, v202\n"
    "v_mul_f32 v93, v93, v202\n"
    "v_mul_f32 v94, v94, v202\n"
    "v_mul_f32 v95, v95, v202\n"
    "v_cvt_pk_bf16_f32 v210, v92, v93\n"
    "v_cvt_pk_bf16_f32 v211, v94, v95\n"
    "ds_write_b64 v194, v[210:211] offset:32768\n"
    "v_mul_f32 v96, v96, v203\n"
    "v_mul_f32 v97, v97, v203\n"
    "v_mul_f32 v98, v98, v203\n"
    "v_mul_f32 v99, v99, v203\n"
    "v_cvt_pk_bf16_f32 v208, v96, v97\n"
    "v_cvt_pk_bf16_f32 v209, v98, v99\n"
    "buffer_store_dwordx4 v[240:243], v184, s[48:51], s66 offen offset:2048 sc1\n"
    "ds_write_b64 v187, v[208:209] offset:49152\n"
    "v_mul_f32 v100, v100, v203\n"
    "v_mul_f32 v101, v101, v203\n"
    "v_mul_f32 v102, v102, v203\n"
    "v_mul_f32 v103, v103, v203\n"
    "v_cvt_pk_bf16_f32 v210, v100, v101\n"
    "v_cvt_pk_bf16_f32 v211, v102, v103\n"
    "s_waitcnt lgkmcnt(0)\n"
    "ds_read_b128 v[216:219], v185 offset:32768\n"
    "ds_read_b128 v[220:223], v186 offset:33792\n"
    "ds_write_b64 v188, v[210:211] offset:49152\n"
    "v_mul_f32 v104, v104, v203\n"
    "v_mul_f32 v105, v105, v203\n"
    "v_mul_f32 v106, v106, v203\n"
    "v_mul_f32 v107, v107, v203\n"
    "v_cvt_pk_bf16_f32 v208, v104, v105\n"
    "v_cvt_pk_bf16_f32 v209, v106, v107\n"
    "buffer_store_dwordx4 v[244:247], v184, s[48:51], s66 offen offset:3072 sc1\n"
    "ds_read_b128 v[224:227], v185 offset:34816\n"
    "ds_read_b128 v[228:231], v186 offset:35840\n"
    "ds_write_b64 v189, v[208:209] offset:49152\n"
    "v_mul_f32 v108, v108, v203\n"
    "v_mul_f32 v109, v109, v203\n"
    "v_mul_f32 v110, v110, v203\n"
    "v_mul_f32 v111, v111, v203\n"
    "v_cvt_pk_bf16_f32 v210, v108, v109\n"
    "v_cvt_pk_bf16_f32 v211, v110, v111\n"
    "ds_write_b64 v190, v[210:211] offset:49152\n"
    "v_mul_f32 v112, v112, v203\n"
    "v_mul_f32 v113, v113, v203\n"
    "v_mul_f32 v114, v114, v203\n"
    "v_mul_f32 v115, v115, v203\n"
    "v_cvt_pk_bf16_f32 v208, v112, v113\n"
    "v_cvt_pk_bf16_f32 v209, v114, v115\n"
    "s_waitcnt lgkmcnt(2)\n"
    "buffer_store_dwordx4 v[216:219], v184, s[48:51], s67 offen offset:0 sc1\n"
    "ds_write_b64 v191, v[208:209] offset:49152\n"
    "v_mul_f32 v116, v116, v203\n"
    "v_mul_f32 v117, v117, v203\n"
    "v_mul_f32 v118, v118, v203\n"
    "v_mul_f32 v119, v119, v203\n"
    "v_cvt_pk_bf16_f32 v210, v116, v117\n"
    "v_cvt_pk_bf16_f32 v211, v118, v119\n"
    "buffer_store_dwordx4 v[220:223], v184, s[48:51], s67 offen offset:1024 sc1\n"
    "ds_write_b64 v192, v[210:211] offset:49152\n"
    "v_mul_f32 v120, v120, v203\n"
    "v_mul_f32 v121, v121, v203\n"
    "v_mul_f32 v122, v122, v203\n"
    "v_mul_f32 v123, v123, v203\n"
    "v_cvt_pk_bf16_f32 v208, v120, v121\n"
    "v_cvt_pk_bf16_f32 v209, v122, v123\n"
    "buffer_store_dwordx4 v[224:227], v184, s[48:51], s67 offen offset:2048 sc1\n"
    "ds_write_b64 v193, v[208:209] offset:49152\n"
    "v_mul_f32 v124, v124, v203\n"
    "v_mul_f32 v125, v125, v203\n"
    "v_mul_f32 v126, v126, v203\n"
    "v_mul_f32 v127, v127, v203\n"
    "v_cvt_pk_bf16_f32 v210, v124, v125\n"
    "v_cvt_pk_bf16_f32 v211, v126, v127\n"
    "buffer_store_dwordx4 v[228:231], v184, s[48:51], s67 offen offset:3072 sc1\n"
    "ds_write_b64 v194, v[210:211] offset:49152\n"
    "s_waitcnt lgkmcnt(0)\n"
    "ds_read_b128 v[232:235], v185 offset:49152\n"
    "ds_read_b128 v[236:239], v186 offset:50176\n"
    "ds_read_b128 v[240:243], v185 offset:51200\n"
    "ds_read_b128 v[244:247], v186 offset:52224\n"
    "s_waitcnt lgkmcnt(0)\n"
    "buffer_store_dwordx4 v[232:235], v184, s[48:51], s68 offen offset:0 sc1\n"
    "buffer_store_dwordx4 v[236:239], v184, s[48:51], s68 offen offset:1024 sc1\n"
    "buffer_store_dwordx4 v[240:243], v184, s[48:51], s68 offen offset:2048 sc1\n"
    "buffer_store_dwordx4 v[244:247], v184, s[48:51], s68 offen offset:3072 sc1\n"
    "s_waitcnt vmcnt(16) lgkmcnt(0)\n"
    "v_mov_b32 v128, s62\n"
    "v_mov_b32 v129, s52\n"
    "v_add_u32 v129, 0x20000, v129\n"
    "ds_write_b32 v129, v128\n"
    "s_waitcnt lgkmcnt(0)\n"
    "s_barrier\n"
    "v_mov_b32 v129, %[lds]\n"
    "v_add_u32 v129, 0x20000, v129\n"
    "ds_read_b32 v130, v129 offset:0\n"
    "ds_read_b32 v131, v129 offset:4096\n"
    "ds_read_b32 v132, v129 offset:8192\n"
    "ds_read_b32 v133, v129 offset:12288\n"
    "s_waitcnt lgkmcnt(0)\n"
    "v_or_b32 v130, v130, v131\n"
    "v_or3_b32 v130, v130, v132, v133\n"
    "s_nop 1\n"
    "v_readfirstlane_b32 s62, v130\n"
    "s_nop 3\n"
    "s_cmp_eq_u32 s62, 0\n"
    "s_cbranch_scc1 Lmha_END\n"
    "Lmha_SAFE:\n"
    "s_waitcnt vmcnt(0) lgkmcnt(0)\n"
    "s_barrier\n"
    "s_ff1_i32_b32 s57, s62\n"
    "s_bitset0_b32 s62, s57\n"
    "s_mul_i32 s59, s57, 256\n"
    "s_add_u32 s59, s59, s63\n"
    "s_lshl_b32 s58, s59, 16\n"
    "s_add_u32 s44, %[p0l], s58\n"
    "s_addc_u32 s45, %[p0h], 0\n"
    "s_and_b32 s45, s45, 0xffff\n"
    "s_mov_b32 s46, 0x10000\n"
    "s_mov_b32 s47, 0x20000\n"
    "s_lshl_b32 s58, s59, 17\n"
    "s_add_u32 s36, %[p1l], s58\n"
    "s_addc_u32 s37, %[p1h], 0\n"
    "s_and_b32 s37, s37, 0xffff\n"
    "s_mov_b32 s38, 0x20000\n"
    "s_mov_b32 s39, 0x20000\n"
    "s_lshl_b32 s58, s59, 17\n"
    "s_add_u32 s40, %[p2l], s58\n"
    "s_addc_u32 s41, %[p2h], 0\n"
    "s_and_b32 s41, s41, 0xffff\n"
    "s_mov_b32 s42, 0x20000\n"
    "s_mov_b32 s43, 0x20000\n"
    "s_lshl_b32 s58, s59, 16\n"
    "s_add_u32 s48, %[p3l], s58\n"
    "s_addc_u32 s49, %[p3h], 0\n"
    "s_and_b32 s49, s49, 0xffff\n"
    "s_mov_b32 s50, 0x10000\n"
    "s_mov_b32 s51, 0x20000\n"
    "s_add_u32 s58, s53, 0x0\n"
    "s_add_u32 m0, s52, 0x10000\n"
    "s_nop 0\n"
    "buffer_load_dwordx4 v181, s[44:47], s58 offen offset:0 nt lds\n"
    "s_add_u32 m0, s52, 0xfc00\n"
    "s_nop 0\n"
    "buffer_load_dwordx4 v182, s[44:47], s58 offen offset:2048 nt lds\n"
    "s_add_u32 s58, s53, 0x1000\n"
    "s_add_u32 m0, s52, 0x10800\n"
    "s_nop 0\n"
    "buffer_load_dwordx4 v181, s[44:47], s58 offen offset:0 nt lds\n"
    "s_add_u32 m0, s52, 0x10400\n"
    "s_nop 0\n"
    "buffer_load_dwordx4 v182, s[44:47], s58 offen offset:2048 nt lds\n"
    "s_add_u32 s58, s53, 0x80\n"
    "s_add_u32 m0, s52, 0x14000\n"
    "s_nop 0\n"
    "buffer_load_dwordx4 v181, s[44:47], s58 offen offset:0 nt lds\n"
    "s_add_u32 m0, s52, 0x13c00\n"
    "s_nop 0\n"
    "buffer_load_dwordx4 v182, s[44:47], s58 offen offset:2048 nt lds\n"
    "s_add_u32 s58, s53, 0x1080\n"
    "s_add_u32 m0, s52, 0x14800\n"
    "s_nop 0\n"
    "buffer_load_dwordx4 v181, s[44:47], s58 offen offset:0 nt lds\n"
    "s_add_u32 m0, s52, 0x14400\n"
    "s_nop 0\n"
    "buffer_load_dwordx4 v182, s[44:47], s58 offen offset:2048 nt lds\n"
    "s_add_u32 s58, s53, 0x2000\n"
    "s_add_u32 m0, s52, 0x18000\n"
    "s_nop 0\n"
    "buffer_load_dwordx4 v181, s[44:47], s58 offen offset:0 nt lds\n"
    "s_add_u32 m0, s52, 0x17c00\n"
    "s_nop 0\n"
    "buffer_load_dwordx4 v182, s[44:47], s58 offen offset:2048 nt lds\n"
    "s_add_u32 s58, s53, 0x3000\n"
    "s_add_u32 m0, s52, 0x18800\n"
    "s_nop 0\n"
    "buffer_load_dwordx4 v181, s[44:47], s58 offen offset:0 nt lds\n"
    "s_add_u32 m0, s52, 0x18400\n"
    "s_nop 0\n"
    "buffer_load_dwordx4 v182, s[44:47], s58 offen offset:2048 nt lds\n"
    "s_add_u32 s58, s53, 0x2080\n"
    "s_add_u32 m0, s52, 0x1c000\n"
    "s_nop 0\n"
    "buffer_load_dwordx4 v181, s[44:47], s58 offen offset:0 nt lds\n"
    "s_add_u32 m0, s52, 0x1bc00\n"
    "s_nop 0\n"
    "buffer_load_dwordx4 v182, s[44:47], s58 offen offset:2048 nt lds\n"
    "s_add_u32 s58, s53, 0x3080\n"
    "s_add_u32 m0, s52, 0x1c800\n"
    "s_nop 0\n"
    "buffer_load_dwordx4 v181, s[44:47], s58 offen offset:0 nt lds\n"
    "s_add_u32 m0, s52, 0x1c400\n"
    "s_nop 0\n"
    "buffer_load_dwordx4 v182, s[44:47], s58 offen offset:2048 nt lds\n"
    "s_waitcnt vmcnt(0)\n"
    "v_subrev_u32 v204, 0x10000, v180\n"
    "ds_read_b128 a[0:3], v204 offset:0\n"
    "ds_read_b128 a[4:7], v204 offset:512\n"
    "ds_read_b128 a[16:19], v204 offset:2048\n"
    "ds_read_b128 a[20:23], v204 offset:2560\n"
    "ds_read_b128 a[8:11], v204 offset:16384\n"
    "ds_read_b128 a[12:15], v204 offset:16896\n"
    "ds_read_b128 a[24:27], v204 offset:18432\n"
    "ds_read_b128 a[28:31], v204 offset:18944\n"
    "ds_read_b128 a[32:35], v204 offset:32768\n"
    "ds_read_b128 a[36:39], v204 offset:33280\n"
    "ds_read_b128 a[48:51], v204 offset:34816\n"
    "ds_read_b128 a[52:55], v204 offset:35328\n"
    "ds_read_b128 a[40:43], v204 offset:49152\n"
    "ds_read_b128 a[44:47], v204 offset:49664\n"
    "ds_read_b128 a[56:59], v204 offset:51200\n"
    "ds_read_b128 a[60:63], v204 offset:51712\n"
    "s_waitcnt lgkmcnt(0)\n"
    "v_mov_b32 v248, 0xff800000\n"
    "v_mov_b32 v249, 0xff800000\n"
    "v_mov_b32 v250, 0xff800000\n"
    "v_mov_b32 v251, 0xff800000\n"
    "s_mov_b32 s64, 0\n"
    "Lmha_SPASS:\n"
    "s_mov_b32 s65, 0\n"
    "s_mov_b32 s77, 0\n"
    "Lmha_STILE:\n"
    "s_waitcnt lgkmcnt(0)\n"
    "s_barrier\n"
    "s_add_u32 m0, s52, 0x0\n"
    "s_nop 0\n"
    "buffer_load_dwordx4 v181, s[36:39], s77 offen offset:0 nt lds\n"
    "s_nop 0\n"
    "buffer_load_dwordx4 v182, s[36:39], s77 offen offset:2048 nt lds\n"
    "s_add_u32 m0, s52, 0x380\n"
    "s_nop 0\n"
    "buffer_load_dwordx4 v181, s[36:39], s77 offen offset:128 nt lds\n"
    "s_nop 0\n"
    "buffer_load_dwordx4 v182, s[36:39], s77 offen offset:2176 nt lds\n"
    "s_add_u32 m0, s52, 0x4000\n"
    "s_nop 0\n"
    "buffer_load_dwordx4 v183, s[40:43], s77 offen offset:0 nt lds\n"
    "s_nop 0\n"
    "buffer_load_dwordx4 v183, s[40:43], s77 offen offset:2048 nt lds\n"
    "s_add_u32 m0, s52, 0x4380\n"
    "s_nop 0\n"
    "buffer_load_dwordx4 v183, s[40:43], s77 offen offset:128 nt lds\n"
    "s_nop 0\n"
    "buffer_load_dwordx4 v183, s[40:43], s77 offen offset:2176 nt lds\n"
    "s_waitcnt vmcnt(0)\n"
    "s_barrier\n"
    "ds_read_b128 a[128:131], v176 offset:0\n"
    "ds_read_b128 a[132:135], v176 offset:512\n"
    "ds_read_b128 a[136:139], v176 offset:1024\n"
    "ds_read_b128 a[140:143], v176 offset:1536\n"
    "ds_read_b128 a[144:147], v176 offset:4096\n"
    "ds_read_b128 a[148:151], v176 offset:4608\n"
    "ds_read_b128 a[152:155], v176 offset:5120\n"
    "ds_read_b128 a[156:159], v176 offset:5632\n"
    "s_waitcnt lgkmcnt(0)\n"
    "v_mfma_f32_16x16x32_bf16 v[128:131], a[128:131], a[0:3], 0\n"
    "v_mfma_f32_16x16x32_bf16 v[128:131], a[132:135], a[4:7], v[128:131]\n"
    "v_mfma_f32_16x16x32_bf16 v[128:131], a[136:139], a[8:11], v[128:131]\n"
    "v_mfma_f32_16x16x32_bf16 v[128:131], a[140:143], a[12:15], v[128:131]\n"
    "v_mfma_f32_16x16x32_bf16 v[132:135], a[144:147], a[0:3], 0\n"
    "v_mfma_f32_16x16x32_bf16 v[132:135], a[148:151], a[4:7], v[132:135]\n"
    "v_mfma_f32_16x16x32_bf16 v[132:135], a[152:155], a[8:11], v[132:135]\n"
    "v_mfma_f32_16x16x32_bf16 v[132:135], a[156:159], a[12:15], v[132:135]\n"
    "v_mfma_f32_16x16x32_bf16 v[136:139], a[128:131], a[16:19], 0\n"
    "v_mfma_f32_16x16x32_bf16 v[136:139], a[132:135], a[20:23], v[136:139]\n"
    "v_mfma_f32_16x16x32_bf16 v[136:139], a[136:139], a[24:27], v[136:139]\n"
    "v_mfma_f32_16x16x32_bf16 v[136:139], a[140:143], a[28:31], v[136:139]\n"
    "v_mfma_f32_16x16x32_bf16 v[140:143], a[144:147], a[16:19], 0\n"
    "v_mfma_f32_16x16x32_bf16 v[140:143], a[148:151], a[20:23], v[140:143]\n"
    "v_mfma_f32_16x16x32_bf16 v[140:143], a[152:155], a[24:27], v[140:143]\n"
    "v_mfma_f32_16x16x32_bf16 v[140:143], a[156:159], a[28:31], v[140:143]\n"
    "v_mfma_f32_16x16x32_bf16 v[144:147], a[128:131], a[32:35], 0\n"
    "v_mfma_f32_16x16x32_bf16 v[144:147], a[132:135], a[36:39], v[144:147]\n"
    "v_mfma_f32_16x16x32_bf16 v[144:147], a[136:139], a[40:43], v[144:147]\n"
    "v_mfma_f32_16x16x32_bf16 v[144:147], a[140:143], a[44:47], v[144:147]\n"
    "v_mfma_f32_16x16x32_bf16 v[148:151], a[144:147], a[32:35], 0\n"
    "v_mfma_f32_16x16x32_bf16 v[148:151], a[148:151], a[36:39], v[148:151]\n"
    "v_mfma_f32_16x16x32_bf16 v[148:151], a[152:155], a[40:43], v[148:151]\n"
    "v_mfma_f32_16x16x32_bf16 v[148:151], a[156:159], a[44:47], v[148:151]\n"
    "v_mfma_f32_16x16x32_bf16 v[152:155], a[128:131], a[48:51], 0\n"
    "v_mfma_f32_16x16x32_bf16 v[152:155], a[132:135], a[52:55], v[152:155]\n"
    "v_mfma_f32_16x16x32_bf16 v[152:155], a[136:139], a[56:59], v[152:155]\n"
    "v_mfma_f32_16x16x32_bf16 v[152:155], a[140:143], a[60:63], v[152:155]\n"
    "v_mfma_f32_16x16x32_bf16 v[156:159], a[144:147], a[48:51], 0\n"
    "v_mfma_f32_16x16x32_bf16 v[156:159], a[148:151], a[52:55], v[156:159]\n"
    "v_mfma_f32_16x16x32_bf16 v[156:159], a[152:155], a[56:59], v[156:159]\n"
    "v_mfma_f32_16x16x32_bf16 v[156:159], a[156:159], a[60:63], v[156:159]\n"
    "s_nop 7\n"
    "s_nop 3\n"
    "s_cmp_eq_u32 s64, 0\n"
    "s_cbranch_scc0 Lmha_SP1_0\n"
    "v_max3_f32 v248, v248, v128, v129\n"
    "v_max3_f32 v248, v248, v130, v131\n"
    "v_max3_f32 v248, v248, v132, v133\n"
    "v_max3_f32 v248, v248, v134, v135\n"
    "v_max3_f32 v249, v249, v136, v137\n"
    "v_max3_f32 v249, v249, v138, v139\n"
    "v_max3_f32 v249, v249, v140, v141\n"
    "v_max3_f32 v249, v249, v142, v143\n"
    "v_max3_f32 v250, v250, v144, v145\n"
    "v_max3_f32 v250, v250, v146, v147\n"
    "v_max3_f32 v250, v250, v148, v149\n"
    "v_max3_f32 v250, v250, v150, v151\n"
    "v_max3_f32 v251, v251, v152, v153\n"
    "v_max3_f32 v251, v251, v154, v155\n"
    "v_max3_f32 v251, v251, v156, v157\n"
    "v_max3_f32 v251, v251, v158, v159\n"
    "s_branch Lmha_SNEXT_0\n"
    "Lmha_SP1_0:\n"
    "v_sub_f32 v128, v128, v248\n"
    "v_sub_f32 v129, v129, v248\n"
    "v_sub_f32 v130, v130, v248\n"
    "v_sub_f32 v131, v131, v248\n"
    "v_sub_f32 v132, v132, v248\n"
    "v_sub_f32 v133, v133, v248\n"
    "v_sub_f32 v134, v134, v248\n"
    "v_sub_f32 v135, v135, v248\n"
    "v_mul_f32 v128, s54, v128\n"
    "v_mul_f32 v129, s54, v129\n"
    "v_mul_f32 v130, s54, v130\n"
    "v_mul_f32 v131, s54, v131\n"
    "v_mul_f32 v132, s54, v132\n"
    "v_mul_f32 v133, s54, v133\n"
    "v_mul_f32 v134, s54, v134\n"
    "v_mul_f32 v135, s54, v135\n"
    "v_exp_f32 v128, v128\n"
    "v_exp_f32 v129, v129\n"
    "v_exp_f32 v130, v130\n"
    "v_exp_f32 v131, v131\n"
    "v_exp_f32 v132, v132\n"
    "v_exp_f32 v133, v133\n"
    "v_exp_f32 v134, v134\n"
    "v_exp_f32 v135, v135\n"
    "s_nop 0\n"
    "v_cvt_pk_bf16_f32 v160, v128, v129\n"
    "v_cvt_pk_bf16_f32 v161, v130, v131\n"
    "v_cvt_pk_bf16_f32 v162, v132, v133\n"
    "v_cvt_pk_bf16_f32 v163, v134, v135\n"
    "v_sub_f32 v136, v136, v249\n"
    "v_sub_f32 v137, v137, v249\n"
    "v_sub_f32 v138, v138, v249\n"
    "v_sub_f32 v139, v139, v249\n"
    "v_sub_f32 v140, v140, v249\n"
    "v_sub_f32 v141, v141, v249\n"
    "v_sub_f32 v142, v142, v249\n"
    "v_sub_f32 v143, v143, v249\n"
    "v_mul_f32 v136, s54, v136\n"
    "v_mul_f32 v137, s54, v137\n"
    "v_mul_f32 v138, s54, v138\n"
    "v_mul_f32 v139, s54, v139\n"
    "v_mul_f32 v140, s54, v140\n"
    "v_mul_f32 v141, s54, v141\n"
    "v_mul_f32 v142, s54, v142\n"
    "v_mul_f32 v143, s54, v143\n"
    "v_exp_f32 v136, v136\n"
    "v_exp_f32 v137, v137\n"
    "v_exp_f32 v138, v138\n"
    "v_exp_f32 v139, v139\n"
    "v_exp_f32 v140, v140\n"
    "v_exp_f32 v141, v141\n"
    "v_exp_f32 v142, v142\n"
    "v_exp_f32 v143, v143\n"
    "s_nop 0\n"
    "v_cvt_pk_bf16_f32 v164, v136, v137\n"
    "v_cvt_pk_bf16_f32 v165, v138, v139\n"
    "v_cvt_pk_bf16_f32 v166, v140, v141\n"
    "v_cvt_pk_bf16_f32 v167, v142, v143\n"
    "v_sub_f32 v144, v144, v250\n"
    "v_sub_f32 v145, v145, v250\n"
    "v_sub_f32 v146, v146, v250\n"
    "v_sub_f32 v147, v147, v250\n"
    "v_sub_f32 v148, v148, v250\n"
    "v_sub_f32 v149, v149, v250\n"
    "v_sub_f32 v150, v150, v250\n"
    "v_sub_f32 v151, v151, v250\n"
    "v_mul_f32 v144, s54, v144\n"
    "v_mul_f32 v145, s54, v145\n"
    "v_mul_f32 v146, s54, v146\n"
    "v_mul_f32 v147, s54, v147\n"
    "v_mul_f32 v148, s54, v148\n"
    "v_mul_f32 v149, s54, v149\n"
    "v_mul_f32 v150, s54, v150\n"
    "v_mul_f32 v151, s54, v151\n"
    "v_exp_f32 v144, v144\n"
    "v_exp_f32 v145, v145\n"
    "v_exp_f32 v146, v146\n"
    "v_exp_f32 v147, v147\n"
    "v_exp_f32 v148, v148\n"
    "v_exp_f32 v149, v149\n"
    "v_exp_f32 v150, v150\n"
    "v_exp_f32 v151, v151\n"
    "s_nop 0\n"
    "v_cvt_pk_bf16_f32 v168, v144, v145\n"
    "v_cvt_pk_bf16_f32 v169, v146, v147\n"
    "v_cvt_pk_bf16_f32 v170, v148, v149\n"
    "v_cvt_pk_bf16_f32 v171, v150, v151\n"
    "v_sub_f32 v152, v152, v251\n"
    "v_sub_f32 v153, v153, v251\n"
    "v_sub_f32 v154, v154, v251\n"
    "v_sub_f32 v155, v155, v251\n"
    "v_sub_f32 v156, v156, v251\n"
    "v_sub_f32 v157, v157, v251\n"
    "v_sub_f32 v158, v158, v251\n"
    "v_sub_f32 v159, v159, v251\n"
    "v_mul_f32 v152, s54, v152\n"
    "v_mul_f32 v153, s54, v153\n"
    "v_mul_f32 v154, s54, v154\n"
    "v_mul_f32 v155, s54, v155\n"
    "v_mul_f32 v156, s54, v156\n"
    "v_mul_f32 v157, s54, v157\n"
    "v_mul_f32 v158, s54, v158\n"
    "v_mul_f32 v159, s54, v159\n"
    "v_exp_f32 v152, v152\n"
    "v_exp_f32 v153, v153\n"
    "v_exp_f32 v154, v154\n"
    "v_exp_f32 v155, v155\n"
    "v_exp_f32 v156, v156\n"
    "v_exp_f32 v157, v157\n"
    "v_exp_f32 v158, v158\n"
    "v_exp_f32 v159, v159\n"
    "s_nop 0\n"
    "v_cvt_pk_bf16_f32 v172, v152, v153\n"
    "v_cvt_pk_bf16_f32 v173, v154, v155\n"
    "v_cvt_pk_bf16_f32 v174, v156, v157\n"
    "v_cvt_pk_bf16_f32 v175, v158, v159\n"
    "ds_read_b64_tr_b16 a[160:161], v178 offset:0\n"
    "ds_read_b64_tr_b16 a[162:163], v178 offset:4096\n"
    "ds_read_b64_tr_b16 a[164:165], v178 offset:256\n"
    "ds_read_b64_tr_b16 a[166:167], v178 offset:4352\n"
    "ds_read_b64_tr_b16 a[168:169], v178 offset:512\n"
    "ds_read_b64_tr_b16 a[170:171], v178 offset:4608\n"
    "ds_read_b64_tr_b16 a[172:173], v178 offset:768\n"
    "ds_read_b64_tr_b16 a[174:175], v178 offset:4864\n"
    "ds_read_b64_tr_b16 a[176:177], v178 offset:1024\n"
    "ds_read_b64_tr_b16 a[178:179], v178 offset:5120\n"
    "ds_read_b64_tr_b16 a[180:181], v178 offset:1280\n"
    "ds_read_b64_tr_b16 a[182:183], v178 offset:5376\n"
    "ds_read_b64_tr_b16 a[184:185], v178 offset:1536\n"
    "ds_read_b64_tr_b16 a[186:187], v178 offset:5632\n"
    "ds_read_b64_tr_b16 a[188:189], v178 offset:1792\n"
    "ds_read_b64_tr_b16 a[190:191], v178 offset:5888\n"
    "s_waitcnt lgkmcnt(0)\n"
    "s_nop 1\n"
    "v_mfma_f32_16x16x32_bf16 v[0:3], a[160:163], v[160:163], v[0:3]\n"
    "v_mfma_f32_16x16x32_bf16 v[4:7], a[164:167], v[160:163], v[4:7]\n"
    "v_mfma_f32_16x16x32_bf16 v[8:11], a[168:171], v[160:163], v[8:11]\n"
    "v_mfma_f32_16x16x32_bf16 v[12:15], a[172:175], v[160:163], v[12:15]\n"
    "v_mfma_f32_16x16x32_bf16 v[16:19], a[176:179], v[160:163], v[16:19]\n"
    "v_mfma_f32_16x16x32_bf16 v[20:23], a[180:183], v[160:163], v[20:23]\n"
    "v_mfma_f32_16x16x32_bf16 v[24:27], a[184:187], v[160:163], v[24:27]\n"
    "v_mfma_f32_16x16x32_bf16 v[28:31], a[188:191], v[160:163], v[28:31]\n"
    "v_mfma_f32_16x16x32_bf16 a[224:227], a[240:243], v[160:163], a[224:227]\n"
    "v_mfma_f32_16x16x32_bf16 v[32:35], a[160:163], v[164:167], v[32:35]\n"
    "v_mfma_f32_16x16x32_bf16 v[36:39], a[164:167], v[164:167], v[36:39]\n"
    "v_mfma_f32_16x16x32_bf16 v[40:43], a[168:171], v[164:167], v[40:43]\n"
    "v_mfma_f32_16x16x32_bf16 v[44:47], a[172:175], v[164:167], v[44:47]\n"
    "v_mfma_f32_16x16x32_bf16 v[48:51], a[176:179], v[164:167], v[48:51]\n"
    "v_mfma_f32_16x16x32_bf16 v[52:55], a[180:183], v[164:167], v[52:55]\n"
    "v_mfma_f32_16x16x32_bf16 v[56:59], a[184:187], v[164:167], v[56:59]\n"
    "v_mfma_f32_16x16x32_bf16 v[60:63], a[188:191], v[164:167], v[60:63]\n"
    "v_mfma_f32_16x16x32_bf16 a[228:231], a[240:243], v[164:167], a[228:231]\n"
    "v_mfma_f32_16x16x32_bf16 v[64:67], a[160:163], v[168:171], v[64:67]\n"
    "v_mfma_f32_16x16x32_bf16 v[68:71], a[164:167], v[168:171], v[68:71]\n"
    "v_mfma_f32_16x16x32_bf16 v[72:75], a[168:171], v[168:171], v[72:75]\n"
    "v_mfma_f32_16x16x32_bf16 v[76:79], a[172:175], v[168:171], v[76:79]\n"
    "v_mfma_f32_16x16x32_bf16 v[80:83], a[176:179], v[168:171], v[80:83]\n"
    "v_mfma_f32_16x16x32_bf16 v[84:87], a[180:183], v[168:171], v[84:87]\n"
    "v_mfma_f32_16x16x32_bf16 v[88:91], a[184:187], v[168:171], v[88:91]\n"
    "v_mfma_f32_16x16x32_bf16 v[92:95], a[188:191], v[168:171], v[92:95]\n"
    "v_mfma_f32_16x16x32_bf16 a[232:235], a[240:243], v[168:171], a[232:235]\n"
    "v_mfma_f32_16x16x32_bf16 v[96:99], a[160:163], v[172:175], v[96:99]\n"
    "v_mfma_f32_16x16x32_bf16 v[100:103], a[164:167], v[172:175], v[100:103]\n"
    "v_mfma_f32_16x16x32_bf16 v[104:107], a[168:171], v[172:175], v[104:107]\n"
    "v_mfma_f32_16x16x32_bf16 v[108:111], a[172:175], v[172:175], v[108:111]\n"
    "v_mfma_f32_16x16x32_bf16 v[112:115], a[176:179], v[172:175], v[112:115]\n"
    "v_mfma_f32_16x16x32_bf16 v[116:119], a[180:183], v[172:175], v[116:119]\n"
    "v_mfma_f32_16x16x32_bf16 v[120:123], a[184:187], v[172:175], v[120:123]\n"
    "v_mfma_f32_16x16x32_bf16 v[124:127], a[188:191], v[172:175], v[124:127]\n"
    "v_mfma_f32_16x16x32_bf16 a[236:239], a[240:243], v[172:175], a[236:239]\n"
    "s_nop 7\n"
    "s_nop 3\n"
    "Lmha_SNEXT_0:\n"
    "ds_read_b128 a[128:131], v176 offset:8192\n"
    "ds_read_b128 a[132:135], v176 offset:8704\n"
    "ds_read_b128 a[136:139], v176 offset:9216\n"
    "ds_read_b128 a[140:143], v176 offset:9728\n"
    "ds_read_b128 a[144:147], v176 offset:12288\n"
    "ds_read_b128 a[148:151], v176 offset:12800\n"
    "ds_read_b128 a[152:155], v176 offset:13312\n"
    "ds_read_b128 a[156:159], v176 offset:13824\n"
    "s_waitcnt lgkmcnt(0)\n"
    "v_mfma_f32_16x16x32_bf16 v[128:131], a[128:131], a[0:3], 0\n"
    "v_mfma_f32_16x16x32_bf16 v[128:131], a[132:135], a[4:7], v[128:131]\n"
    "v_mfma_f32_16x16x32_bf16 v[128:131], a[136:139], a[8:11], v[128:131]\n"
    "v_mfma_f32_16x16x32_bf16 v[128:131], a[140:143], a[12:15], v[128:131]\n"
    "v_mfma_f32_16x16x32_bf16 v[132:135], a[144:147], a[0:3], 0\n"
    "v_mfma_f32_16x16x32_bf16 v[132:135], a[148:151], a[4:7], v[132:135]\n"
    "v_mfma_f32_16x16x32_bf16 v[132:135], a[152:155], a[8:11], v[132:135]\n"
    "v_mfma_f32_16x16x32_bf16 v[132:135], a[156:159], a[12:15], v[132:135]\n"
    "v_mfma_f32_16x16x32_bf16 v[136:139], a[128:131], a[16:19], 0\n"
    "v_mfma_f32_16x16x32_bf16 v[136:139], a[132:135], a[20:23], v[136:139]\n"
    "v_mfma_f32_16x16x32_bf16 v[136:139], a[136:139], a[24:27], v[136:139]\n"
    "v_mfma_f32_16x16x32_bf16 v[136:139], a[140:143], a[28:31], v[136:139]\n"
    "v_mfma_f32_16x16x32_bf16 v[140:143], a[144:147], a[16:19], 0\n"
    "v_mfma_f32_16x16x32_bf16 v[140:143], a[148:151], a[20:23], v[140:143]\n"
    "v_mfma_f32_16x16x32_bf16 v[140:143], a[152:155], a[24:27], v[140:143]\n"
    "v_mfma_f32_16x16x32_bf16 v[140:143], a[156:159], a[28:31], v[140:143]\n"
    "v_mfma_f32_16x16x32_bf16 v[144:147], a[128:131], a[32:35], 0\n"
    "v_mfma_f32_16x16x32_bf16 v[144:147], a[132:135], a[36:39], v[144:147]\n"
    "v_mfma_f32_16x16x32_bf16 v[144:147], a[136:139], a[40:43], v[144:147]\n"
    "v_mfma_f32_16x16x32_bf16 v[144:147], a[140:143], a[44:47], v[144:147]\n"
    "v_mfma_f32_16x16x32_bf16 v[148:151], a[144:147], a[32:35], 0\n"
    "v_mfma_f32_16x16x32_bf16 v[148:151], a[148:151], a[36:39], v[148:151]\n"
    "v_mfma_f32_16x16x32_bf16 v[148:151], a[152:155], a[40:43], v[148:151]\n"
    "v_mfma_f32_16x16x32_bf16 v[148:151], a[156:159], a[44:47], v[148:151]\n"
    "v_mfma_f32_16x16x32_bf16 v[152:155], a[128:131], a[48:51], 0\n"
    "v_mfma_f32_16x16x32_bf16 v[152:155], a[132:135], a[52:55], v[152:155]\n"
    "v_mfma_f32_16x16x32_bf16 v[152:155], a[136:139], a[56:59], v[152:155]\n"
    "v_mfma_f32_16x16x32_bf16 v[152:155], a[140:143], a[60:63], v[152:155]\n"
    "v_mfma_f32_16x16x32_bf16 v[156:159], a[144:147], a[48:51], 0\n"
    "v_mfma_f32_16x16x32_bf16 v[156:159], a[148:151], a[52:55], v[156:159]\n"
    "v_mfma_f32_16x16x32_bf16 v[156:159], a[152:155], a[56:59], v[156:159]\n"
    "v_mfma_f32_16x16x32_bf16 v[156:159], a[156:159], a[60:63], v[156:159]\n"
    "s_nop 7\n"
    "s_nop 3\n"
    "s_cmp_eq_u32 s64, 0\n"
    "s_cbranch_scc0 Lmha_SP1_1\n"
    "v_max3_f32 v248, v248, v128, v129\n"
    "v_max3_f32 v248, v248, v130, v131\n"
    "v_max3_f32 v248, v248, v132, v133\n"
    "v_max3_f32 v248, v248, v134, v135\n"
    "v_max3_f32 v249, v249, v136, v137\n"
    "v_max3_f32 v249, v249, v138, v139\n"
    "v_max3_f32 v249, v249, v140, v141\n"
    "v_max3_f32 v249, v249, v142, v143\n"
    "v_max3_f32 v250, v250, v144, v145\n"
    "v_max3_f32 v250, v250, v146, v147\n"
    "v_max3_f32 v250, v250, v148, v149\n"
    "v_max3_f32 v250, v250, v150, v151\n"
    "v_max3_f32 v251, v251, v152, v153\n"
    "v_max3_f32 v251, v251, v154, v155\n"
    "v_max3_f32 v251, v251, v156, v157\n"
    "v_max3_f32 v251, v251, v158, v159\n"
    "s_branch Lmha_SNEXT_1\n"
    "Lmha_SP1_1:\n"
    "v_sub_f32 v128, v128, v248\n"
    "v_sub_f32 v129, v129, v248\n"
    "v_sub_f32 v130, v130, v248\n"
    "v_sub_f32 v131, v131, v248\n"
    "v_sub_f32 v132, v132, v248\n"
    "v_sub_f32 v133, v133, v248\n"
    "v_sub_f32 v134, v134, v248\n"
    "v_sub_f32 v135, v135, v248\n"
    "v_mul_f32 v128, s54, v128\n"
    "v_mul_f32 v129, s54, v129\n"
    "v_mul_f32 v130, s54, v130\n"
    "v_mul_f32 v131, s54, v131\n"
    "v_mul_f32 v132, s54, v132\n"
    "v_mul_f32 v133, s54, v133\n"
    "v_mul_f32 v134, s54, v134\n"
    "v_mul_f32 v135, s54, v135\n"
    "v_exp_f32 v128, v128\n"
    "v_exp_f32 v129, v129\n"
    "v_exp_f32 v130, v130\n"
    "v_exp_f32 v131, v131\n"
    "v_exp_f32 v132, v132\n"
    "v_exp_f32 v133, v133\n"
    "v_exp_f32 v134, v134\n"
    "v_exp_f32 v135, v135\n"
    "s_nop 0\n"
    "v_cvt_pk_bf16_f32 v160, v128, v129\n"
    "v_cvt_pk_bf16_f32 v161, v130, v131\n"
    "v_cvt_pk_bf16_f32 v162, v132, v133\n"
    "v_cvt_pk_bf16_f32 v163, v134, v135\n"
    "v_sub_f32 v136, v136, v249\n"
    "v_sub_f32 v137, v137, v249\n"
    "v_sub_f32 v138, v138, v249\n"
    "v_sub_f32 v139, v139, v249\n"
    "v_sub_f32 v140, v140, v249\n"
    "v_sub_f32 v141, v141, v249\n"
    "v_sub_f32 v142, v142, v249\n"
    "v_sub_f32 v143, v143, v249\n"
    "v_mul_f32 v136, s54, v136\n"
    "v_mul_f32 v137, s54, v137\n"
    "v_mul_f32 v138, s54, v138\n"
    "v_mul_f32 v139, s54, v139\n"
    "v_mul_f32 v140, s54, v140\n"
    "v_mul_f32 v141, s54, v141\n"
    "v_mul_f32 v142, s54, v142\n"
    "v_mul_f32 v143, s54, v143\n"
    "v_exp_f32 v136, v136\n"
    "v_exp_f32 v137, v137\n"
    "v_exp_f32 v138, v138\n"
    "v_exp_f32 v139, v139\n"
    "v_exp_f32 v140, v140\n"
    "v_exp_f32 v141, v141\n"
    "v_exp_f32 v142, v142\n"
    "v_exp_f32 v143, v143\n"
    "s_nop 0\n"
    "v_cvt_pk_bf16_f32 v164, v136, v137\n"
    "v_cvt_pk_bf16_f32 v165, v138, v139\n"
    "v_cvt_pk_bf16_f32 v166, v140, v141\n"
    "v_cvt_pk_bf16_f32 v167, v142, v143\n"
    "v_sub_f32 v144, v144, v250\n"
    "v_sub_f32 v145, v145, v250\n"
    "v_sub_f32 v146, v146, v250\n"
    "v_sub_f32 v147, v147, v250\n"
    "v_sub_f32 v148, v148, v250\n"
    "v_sub_f32 v149, v149, v250\n"
    "v_sub_f32 v150, v150, v250\n"
    "v_sub_f32 v151, v151, v250\n"
    "v_mul_f32 v144, s54, v144\n"
    "v_mul_f32 v145, s54, v145\n"
    "v_mul_f32 v146, s54, v146\n"
    "v_mul_f32 v147, s54, v147\n"
    "v_mul_f32 v148, s54, v148\n"
    "v_mul_f32 v149, s54, v149\n"
    "v_mul_f32 v150, s54, v150\n"
    "v_mul_f32 v151, s54, v151\n"
    "v_exp_f32 v144, v144\n"
    "v_exp_f32 v145, v145\n"
    "v_exp_f32 v146, v146\n"
    "v_exp_f32 v147, v147\n"
    "v_exp_f32 v148, v148\n"
    "v_exp_f32 v149, v149\n"
    "v_exp_f32 v150, v150\n"
    "v_exp_f32 v151, v151\n"
    "s_nop 0\n"
    "v_cvt_pk_bf16_f32 v168, v144, v145\n"
    "v_cvt_pk_bf16_f32 v169, v146, v147\n"
    "v_cvt_pk_bf16_f32 v170, v148, v149\n"
    "v_cvt_pk_bf16_f32 v171, v150, v151\n"
    "v_sub_f32 v152, v152, v251\n"
    "v_sub_f32 v153, v153, v251\n"
    "v_sub_f32 v154, v154, v251\n"
    "v_sub_f32 v155, v155, v251\n"
    "v_sub_f32 v156, v156, v251\n"
    "v_sub_f32 v157, v157, v251\n"
    "v_sub_f32 v158, v158, v251\n"
    "v_sub_f32 v159, v159, v251\n"
    "v_mul_f32 v152, s54, v152\n"
    "v_mul_f32 v153, s54, v153\n"
    "v_mul_f32 v154, s54, v154\n"
    "v_mul_f32 v155, s54, v155\n"
    "v_mul_f32 v156, s54, v156\n"
    "v_mul_f32 v157, s54, v157\n"
    "v_mul_f32 v158, s54, v158\n"
    "v_mul_f32 v159, s54, v159\n"
    "v_exp_f32 v152, v152\n"
    "v_exp_f32 v153, v153\n"
    "v_exp_f32 v154, v154\n"
    "v_exp_f32 v155, v155\n"
    "v_exp_f32 v156, v156\n"
    "v_exp_f32 v157, v157\n"
    "v_exp_f32 v158, v158\n"
    "v_exp_f32 v159, v159\n"
    "s_nop 0\n"
    "v_cvt_pk_bf16_f32 v172, v152, v153\n"
    "v_cvt_pk_bf16_f32 v173, v154, v155\n"
    "v_cvt_pk_bf16_f32 v174, v156, v157\n"
    "v_cvt_pk_bf16_f32 v175, v158, v159\n"
    "ds_read_b64_tr_b16 a[192:193], v178 offset:8192\n"
    "ds_read_b64_tr_b16 a[194:195], v178 offset:12288\n"
    "ds_read_b64_tr_b16 a[196:197], v178 offset:8448\n"
    "ds_read_b64_tr_b16 a[198:199], v178 offset:12544\n"
    "ds_read_b64_tr_b16 a[200:201], v178 offset:8704\n"
    "ds_read_b64_tr_b16 a[202:203], v178 offset:12800\n"
    "ds_read_b64_tr_b16 a[204:205], v178 offset:8960\n"
    "ds_read_b64_tr_b16 a[206:207], v178 offset:13056\n"
    "ds_read_b64_tr_b16 a[208:209], v178 offset:9216\n"
    "ds_read_b64_tr_b16 a[210:211], v178 offset:13312\n"
    "ds_read_b64_tr_b16 a[212:213], v178 offset:9472\n"
    "ds_read_b64_tr_b16 a[214:215], v178 offset:13568\n"
    "ds_read_b64_tr_b16 a[216:217], v178 offset:9728\n"
    "ds_read_b64_tr_b16 a[218:219], v178 offset:13824\n"
    "ds_read_b64_tr_b16 a[220:221], v178 offset:9984\n"
    "ds_read_b64_tr_b16 a[222:223], v178 offset:14080\n"
    "s_waitcnt lgkmcnt(0)\n"
    "s_nop 1\n"
    "v_mfma_f32_16x16x32_bf16 v[0:3], a[192:195], v[160:163], v[0:3]\n"
    "v_mfma_f32_16x16x32_bf16 v[4:7], a[196:199], v[160:163], v[4:7]\n"
    "v_mfma_f32_16x16x32_bf16 v[8:11], a[200:203], v[160:163], v[8:11]\n"
    "v_mfma_f32_16x16x32_bf16 v[12:15], a[204:207], v[160:163], v[12:15]\n"
    "v_mfma_f32_16x16x32_bf16 v[16:19], a[208:211], v[160:163], v[16:19]\n"
    "v_mfma_f32_16x16x32_bf16 v[20:23], a[212:215], v[160:163], v[20:23]\n"
    "v_mfma_f32_16x16x32_bf16 v[24:27], a[216:219], v[160:163], v[24:27]\n"
    "v_mfma_f32_16x16x32_bf16 v[28:31], a[220:223], v[160:163], v[28:31]\n"
    "v_mfma_f32_16x16x32_bf16 a[224:227], a[240:243], v[160:163], a[224:227]\n"
    "v_mfma_f32_16x16x32_bf16 v[32:35], a[192:195], v[164:167], v[32:35]\n"
    "v_mfma_f32_16x16x32_bf16 v[36:39], a[196:199], v[164:167], v[36:39]\n"
    "v_mfma_f32_16x16x32_bf16 v[40:43], a[200:203], v[164:167], v[40:43]\n"
    "v_mfma_f32_16x16x32_bf16 v[44:47], a[204:207], v[164:167], v[44:47]\n"
    "v_mfma_f32_16x16x32_bf16 v[48:51], a[208:211], v[164:167], v[48:51]\n"
    "v_mfma_f32_16x16x32_bf16 v[52:55], a[212:215], v[164:167], v[52:55]\n"
    "v_mfma_f32_16x16x32_bf16 v[56:59], a[216:219], v[164:167], v[56:59]\n"
    "v_mfma_f32_16x16x32_bf16 v[60:63], a[220:223], v[164:167], v[60:63]\n"
    "v_mfma_f32_16x16x32_bf16 a[228:231], a[240:243], v[164:167], a[228:231]\n"
    "v_mfma_f32_16x16x32_bf16 v[64:67], a[192:195], v[168:171], v[64:67]\n"
    "v_mfma_f32_16x16x32_bf16 v[68:71], a[196:199], v[168:171], v[68:71]\n"
    "v_mfma_f32_16x16x32_bf16 v[72:75], a[200:203], v[168:171], v[72:75]\n"
    "v_mfma_f32_16x16x32_bf16 v[76:79], a[204:207], v[168:171], v[76:79]\n"
    "v_mfma_f32_16x16x32_bf16 v[80:83], a[208:211], v[168:171], v[80:83]\n"
    "v_mfma_f32_16x16x32_bf16 v[84:87], a[212:215], v[168:171], v[84:87]\n"
    "v_mfma_f32_16x16x32_bf16 v[88:91], a[216:219], v[168:171], v[88:91]\n"
    "v_mfma_f32_16x16x32_bf16 v[92:95], a[220:223], v[168:171], v[92:95]\n"
    "v_mfma_f32_16x16x32_bf16 a[232:235], a[240:243], v[168:171], a[232:235]\n"
    "v_mfma_f32_16x16x32_bf16 v[96:99], a[192:195], v[172:175], v[96:99]\n"
    "v_mfma_f32_16x16x32_bf16 v[100:103], a[196:199], v[172:175], v[100:103]\n"
    "v_mfma_f32_16x16x32_bf16 v[104:107], a[200:203], v[172:175], v[104:107]\n"
    "v_mfma_f32_16x16x32_bf16 v[108:111], a[204:207], v[172:175], v[108:111]\n"
    "v_mfma_f32_16x16x32_bf16 v[112:115], a[208:211], v[172:175], v[112:115]\n"
    "v_mfma_f32_16x16x32_bf16 v[116:119], a[212:215], v[172:175], v[116:119]\n"
    "v_mfma_f32_16x16x32_bf16 v[120:123], a[216:219], v[172:175], v[120:123]\n"
    "v_mfma_f32_16x16x32_bf16 v[124:127], a[220:223], v[172:175], v[124:127]\n"
    "v_mfma_f32_16x16x32_bf16 a[236:239], a[240:243], v[172:175], a[236:239]\n"
    "s_nop 7\n"
    "s_nop 3\n"
    "Lmha_SNEXT_1:\n"
    "s_add_u32 s77, s77, 0x4000\n"
    "s_add_u32 s65, s65, 1\n"
    "s_cmp_lt_u32 s65, 8\n"
    "s_cbranch_scc1 Lmha_STILE\n"
    "s_cmp_eq_u32 s64, 0\n"
    "s_cbranch_scc0 Lmha_SDONE\n"
    "v_mov_b32 v205, v248\n"
    "s_nop 1\n"
    "v_permlane32_swap_b32 v248, v205\n"
    "s_nop 1\n"
    "v_max_f32 v248, v248, v205\n"
    "v_mov_b32 v205, v248\n"
    "s_nop 1\n"
    "v_permlane16_swap_b32 v248, v205\n"
    "s_nop 1\n"
    "v_max_f32 v248, v248, v205\n"
    "v_mov_b32 v205, v249\n"
    "s_nop 1\n"
    "v_permlane32_swap_b32 v249, v205\n"
    "s_nop 1\n"
    "v_max_f32 v249, v249, v205\n"
    "v_mov_b32 v205, v249\n"
    "s_nop 1\n"
    "v_permlane16_swap_b32 v249, v205\n"
    "s_nop 1\n"
    "v_max_f32 v249, v249, v205\n"
    "v_mov_b32 v205, v250\n"
    "s_nop 1\n"
    "v_permlane32_swap_b32 v250, v205\n"
    "s_nop 1\n"
    "v_max_f32 v250, v250, v205\n"
    "v_mov_b32 v205, v250\n"
    "s_nop 1\n"
    "v_permlane16_swap_b32 v250, v205\n"
    "s_nop 1\n"
    "v_max_f32 v250, v250, v205\n"
    "v_mov_b32 v205, v251\n"
    "s_nop 1\n"
    "v_permlane32_swap_b32 v251, v205\n"
    "s_nop 1\n"
    "v_max_f32 v251, v251, v205\n"
    "v_mov_b32 v205, v251\n"
    "s_nop 1\n"
    "v_permlane16_swap_b32 v251, v205\n"
    "s_nop 1\n"
    "v_max_f32 v251, v251, v205\n"
    "v_mov_b32 v0, 0\n"
    "v_mov_b32 v1, 0\n"
    "v_mov_b32 v2, 0\n"
    "v_mov_b32 v3, 0\n"
    "v_mov_b32 v4, 0\n"
    "v_mov_b32 v5, 0\n"
    "v_mov_b32 v6, 0\n"
    "v_mov_b32 v7, 0\n"
    "v_mov_b32 v8, 0\n"
    "v_mov_b32 v9, 0\n"
    "v_mov_b32 v10, 0\n"
    "v_mov_b32 v11, 0\n"
    "v_mov_b32 v12, 0\n"
    "v_mov_b32 v13, 0\n"
    "v_mov_b32 v14, 0\n"
    "v_mov_b32 v15, 0\n"
    "v_mov_b32 v16, 0\n"
    "v_mov_b32 v17, 0\n"
    "v_mov_b32 v18, 0\n"
    "v_mov_b32 v19, 0\n"
    "v_mov_b32 v20, 0\n"
    "v_mov_b32 v21, 0\n"
    "v_mov_b32 v22, 0\n"
    "v_mov_b32 v23, 0\n"
    "v_mov_b32 v24, 0\n"
    "v_mov_b32 v25, 0\n"
    "v_mov_b32 v26, 0\n"
    "v_mov_b32 v27, 0\n"
    "v_mov_b32 v28, 0\n"
    "v_mov_b32 v29, 0\n"
    "v_mov_b32 v30, 0\n"
    "v_mov_b32 v31, 0\n"
    "v_mov_b32 v32, 0\n"
    "v_mov_b32 v33, 0\n"
    "v_mov_b32 v34, 0\n"
    "v_mov_b32 v35, 0\n"
    "v_mov_b32 v36, 0\n"
    "v_mov_b32 v37, 0\n"
    "v_mov_b32 v38, 0\n"
    "v_mov_b32 v39, 0\n"
    "v_mov_b32 v40, 0\n"
    "v_mov_b32 v41, 0\n"
    "v_mov_b32 v42, 0\n"
    "v_mov_b32 v43, 0\n"
    "v_mov_b32 v44, 0\n"
    "v_mov_b32 v45, 0\n"
    "v_mov_b32 v46, 0\n"
    "v_mov_b32 v47, 0\n"
    "v_mov_b32 v48, 0\n"
    "v_mov_b32 v49, 0\n"
    "v_mov_b32 v50, 0\n"
    "v_mov_b32 v51, 0\n"
    "v_mov_b32 v52, 0\n"
    "v_mov_b32 v53, 0\n"
    "v_mov_b32 v54, 0\n"
    "v_mov_b32 v55, 0\n"
    "v_mov_b32 v56, 0\n"
    "v_mov_b32 v57, 0\n"
    "v_mov_b32 v58, 0\n"
    "v_mov_b32 v59, 0\n"
    "v_mov_b32 v60, 0\n"
    "v_mov_b32 v61, 0\n"
    "v_mov_b32 v62, 0\n"
    "v_mov_b32 v63, 0\n"
    "v_mov_b32 v64, 0\n"
    "v_mov_b32 v65, 0\n"
    "v_mov_b32 v66, 0\n"
    "v_mov_b32 v67, 0\n"
    "v_mov_b32 v68, 0\n"
    "v_mov_b32 v69, 0\n"
    "v_mov_b32 v70, 0\n"
    "v_mov_b32 v71, 0\n"
    "v_mov_b32 v72, 0\n"
    "v_mov_b32 v73, 0\n"
    "v_mov_b32 v74, 0\n"
    "v_mov_b32 v75, 0\n"
    "v_mov_b32 v76, 0\n"
    "v_mov_b32 v77, 0\n"
    "v_mov_b32 v78, 0\n"
    "v_mov_b32 v79, 0\n"
    "v_mov_b32 v80, 0\n"
    "v_mov_b32 v81, 0\n"
    "v_mov_b32 v82, 0\n"
    "v_mov_b32 v83, 0\n"
    "v_mov_b32 v84, 0\n"
    "v_mov_b32 v85, 0\n"
    "v_mov_b32 v86, 0\n"
    "v_mov_b32 v87, 0\n"
    "v_mov_b32 v88, 0\n"
    "v_mov_b32 v89, 0\n"
    "v_mov_b32 v90, 0\n"
    "v_mov_b32 v91, 0\n"
    "v_mov_b32 v92, 0\n"
    "v_mov_b32 v93, 0\n"
    "v_mov_b32 v94, 0\n"
    "v_mov_b32 v95, 0\n"
    "v_mov_b32 v96, 0\n"
    "v_mov_b32 v97, 0\n"
    "v_mov_b32 v98, 0\n"
    "v_mov_b32 v99, 0\n"
    "v_mov_b32 v100, 0\n"
    "v_mov_b32 v101, 0\n"
    "v_mov_b32 v102, 0\n"
    "v_mov_b32 v103, 0\n"
    "v_mov_b32 v104, 0\n"
    "v_mov_b32 v105, 0\n"
    "v_mov_b32 v106, 0\n"
    "v_mov_b32 v107, 0\n"
    "v_mov_b32 v108, 0\n"
    "v_mov_b32 v109, 0\n"
    "v_mov_b32 v110, 0\n"
    "v_mov_b32 v111, 0\n"
    "v_mov_b32 v112, 0\n"
    "v_mov_b32 v113, 0\n"
    "v_mov_b32 v114, 0\n"
    "v_mov_b32 v115, 0\n"
    "v_mov_b32 v116, 0\n"
    "v_mov_b32 v117, 0\n"
    "v_mov_b32 v118, 0\n"
    "v_mov_b32 v119, 0\n"
    "v_mov_b32 v120, 0\n"
    "v_mov_b32 v121, 0\n"
    "v_mov_b32 v122, 0\n"
    "v_mov_b32 v123, 0\n"
    "v_mov_b32 v124, 0\n"
    "v_mov_b32 v125, 0\n"
    "v_mov_b32 v126, 0\n"
    "v_mov_b32 v127, 0\n"
    "v_mov_b32 v205, 0\n"
    "v_accvgpr_write_b32 a224, v205\n"
    "v_accvgpr_write_b32 a225, v205\n"
    "v_accvgpr_write_b32 a226, v205\n"
    "v_accvgpr_write_b32 a227, v205\n"
    "v_accvgpr_write_b32 a228, v205\n"
    "v_accvgpr_write_b32 a229, v205\n"
    "v_accvgpr_write_b32 a230, v205\n"
    "v_accvgpr_write_b32 a231, v205\n"
    "v_accvgpr_write_b32 a232, v205\n"
    "v_accvgpr_write_b32 a233, v205\n"
    "v_accvgpr_write_b32 a234, v205\n"
    "v_accvgpr_write_b32 a235, v205\n"
    "v_accvgpr_write_b32 a236, v205\n"
    "v_accvgpr_write_b32 a237, v205\n"
    "v_accvgpr_write_b32 a238, v205\n"
    "v_accvgpr_write_b32 a239, v205\n"
    "s_mov_b32 s64, 1\n"
    "s_branch Lmha_SPASS\n"
    "Lmha_SDONE:\n"
    "v_accvgpr_read_b32 v200, a224\n"
    "s_nop 0\n"
    "v_rcp_f32 v200, v200\n"
    "s_nop 0\n"
    "s_waitcnt lgkmcnt(0)\n"
    "v_mul_f32 v0, v0, v200\n"
    "v_mul_f32 v1, v1, v200\n"
    "v_mul_f32 v2, v2, v200\n"
    "v_mul_f32 v3, v3, v200\n"
    "v_cvt_pk_bf16_f32 v208, v0, v1\n"
    "v_cvt_pk_bf16_f32 v209, v2, v3\n"
    "ds_write_b64 v187, v[208:209] offset:0\n"
    "v_mul_f32 v4, v4, v200\n"
    "v_mul_f32 v5, v5, v200\n"
    "v_mul_f32 v6, v6, v200\n"
    "v_mul_f32 v7, v7, v200\n"
    "v_cvt_pk_bf16_f32 v210, v4, v5\n"
    "v_cvt_pk_bf16_f32 v211, v6, v7\n"
    "ds_write_b64 v188, v[210:211] offset:0\n"
    "v_mul_f32 v8, v8, v200\n"
    "v_mul_f32 v9, v9, v200\n"
    "v_mul_f32 v10, v10, v200\n"
    "v_mul_f32 v11, v11, v200\n"
    "v_cvt_pk_bf16_f32 v208, v8, v9\n"
    "v_cvt_pk_bf16_f32 v209, v10, v11\n"
    "ds_write_b64 v189, v[208:209] offset:0\n"
    "v_mul_f32 v12, v12, v200\n"
    "v_mul_f32 v13, v13, v200\n"
    "v_mul_f32 v14, v14, v200\n"
    "v_mul_f32 v15, v15, v200\n"
    "v_cvt_pk_bf16_f32 v210, v12, v13\n"
    "v_cvt_pk_bf16_f32 v211, v14, v15\n"
    "ds_write_b64 v190, v[210:211] offset:0\n"
    "v_mul_f32 v16, v16, v200\n"
    "v_mul_f32 v17, v17, v200\n"
    "v_mul_f32 v18, v18, v200\n"
    "v_mul_f32 v19, v19, v200\n"
    "v_cvt_pk_bf16_f32 v208, v16, v17\n"
    "v_cvt_pk_bf16_f32 v209, v18, v19\n"
    "ds_write_b64 v191, v[208:209] offset:0\n"
    "v_mul_f32 v20, v20, v200\n"
    "v_mul_f32 v21, v21, v200\n"
    "v_mul_f32 v22, v22, v200\n"
    "v_mul_f32 v23, v23, v200\n"
    "v_cvt_pk_bf16_f32 v210, v20, v21\n"
    "v_cvt_pk_bf16_f32 v211, v22, v23\n"
    "ds_write_b64 v192, v[210:211] offset:0\n"
    "v_mul_f32 v24, v24, v200\n"
    "v_mul_f32 v25, v25, v200\n"
    "v_mul_f32 v26, v26, v200\n"
    "v_mul_f32 v27, v27, v200\n"
    "v_cvt_pk_bf16_f32 v208, v24, v25\n"
    "v_cvt_pk_bf16_f32 v209, v26, v27\n"
    "ds_write_b64 v193, v[208:209] offset:0\n"
    "v_mul_f32 v28, v28, v200\n"
    "v_mul_f32 v29, v29, v200\n"
    "v_mul_f32 v30, v30, v200\n"
    "v_mul_f32 v31, v31, v200\n"
    "v_cvt_pk_bf16_f32 v210, v28, v29\n"
    "v_cvt_pk_bf16_f32 v211, v30, v31\n"
    "ds_write_b64 v194, v[210:211] offset:0\n"
    "s_waitcnt lgkmcnt(0)\n"
    "ds_read_b128 v[216:219], v185 offset:0\n"
    "ds_read_b128 v[220:223], v186 offset:1024\n"
    "ds_read_b128 v[224:227], v185 offset:2048\n"
    "ds_read_b128 v[228:231], v186 offset:3072\n"
    "s_waitcnt lgkmcnt(0)\n"
    "buffer_store_dwordx4 v[216:219], v184, s[48:51], 0 offen offset:0 sc1\n"
    "buffer_store_dwordx4 v[220:223], v184, s[48:51], 0 offen offset:1024 sc1\n"
    "buffer_store_dwordx4 v[224:227], v184, s[48:51], 0 offen offset:2048 sc1\n"
    "buffer_store_dwordx4 v[228:231], v184, s[48:51], 0 offen offset:3072 sc1\n"
    "s_nop 1\n"
    "v_accvgpr_read_b32 v201, a228\n"
    "s_nop 0\n"
    "v_rcp_f32 v201, v201\n"
    "s_nop 0\n"
    "s_waitcnt lgkmcnt(0)\n"
    "v_mul_f32 v32, v32, v201\n"
    "v_mul_f32 v33, v33, v201\n"
    "v_mul_f32 v34, v34, v201\n"
    "v_mul_f32 v35, v35, v201\n"
    "v_cvt_pk_bf16_f32 v208, v32, v33\n"
    "v_cvt_pk_bf16_f32 v209, v34, v35\n"
    "ds_write_b64 v187, v[208:209] offset:0\n"
    "v_mul_f32 v36, v36, v201\n"
    "v_mul_f32 v37, v37, v201\n"
    "v_mul_f32 v38, v38, v201\n"
    "v_mul_f32 v39, v39, v201\n"
    "v_cvt_pk_bf16_f32 v210, v36, v37\n"
    "v_cvt_pk_bf16_f32 v211, v38, v39\n"
    "ds_write_b64 v188, v[210:211] offset:0\n"
    "v_mul_f32 v40, v40, v201\n"
    "v_mul_f32 v41, v41, v201\n"
    "v_mul_f32 v42, v42, v201\n"
    "v_mul_f32 v43, v43, v201\n"
    "v_cvt_pk_bf16_f32 v208, v40, v41\n"
    "v_cvt_pk_bf16_f32 v209, v42, v43\n"
    "ds_write_b64 v189, v[208:209] offset:0\n"
    "v_mul_f32 v44, v44, v201\n"
    "v_mul_f32 v45, v45, v201\n"
    "v_mul_f32 v46, v46, v201\n"
    "v_mul_f32 v47, v47, v201\n"
    "v_cvt_pk_bf16_f32 v210, v44, v45\n"
    "v_cvt_pk_bf16_f32 v211, v46, v47\n"
    "ds_write_b64 v190, v[210:211] offset:0\n"
    "v_mul_f32 v48, v48, v201\n"
    "v_mul_f32 v49, v49, v201\n"
    "v_mul_f32 v50, v50, v201\n"
    "v_mul_f32 v51, v51, v201\n"
    "v_cvt_pk_bf16_f32 v208, v48, v49\n"
    "v_cvt_pk_bf16_f32 v209, v50, v51\n"
    "ds_write_b64 v191, v[208:209] offset:0\n"
    "v_mul_f32 v52, v52, v201\n"
    "v_mul_f32 v53, v53, v201\n"
    "v_mul_f32 v54, v54, v201\n"
    "v_mul_f32 v55, v55, v201\n"
    "v_cvt_pk_bf16_f32 v210, v52, v53\n"
    "v_cvt_pk_bf16_f32 v211, v54, v55\n"
    "ds_write_b64 v192, v[210:211] offset:0\n"
    "v_mul_f32 v56, v56, v201\n"
    "v_mul_f32 v57, v57, v201\n"
    "v_mul_f32 v58, v58, v201\n"
    "v_mul_f32 v59, v59, v201\n"
    "v_cvt_pk_bf16_f32 v208, v56, v57\n"
    "v_cvt_pk_bf16_f32 v209, v58, v59\n"
    "ds_write_b64 v193, v[208:209] offset:0\n"
    "v_mul_f32 v60, v60, v201\n"
    "v_mul_f32 v61, v61, v201\n"
    "v_mul_f32 v62, v62, v201\n"
    "v_mul_f32 v63, v63, v201\n"
    "v_cvt_pk_bf16_f32 v210, v60, v61\n"
    "v_cvt_pk_bf16_f32 v211, v62, v63\n"
    "ds_write_b64 v194, v[210:211] offset:0\n"
    "s_waitcnt lgkmcnt(0)\n"
    "ds_read_b128 v[232:235], v185 offset:0\n"
    "ds_read_b128 v[236:239], v186 offset:1024\n"
    "ds_read_b128 v[240:243], v185 offset:2048\n"
    "ds_read_b128 v[244:247], v186 offset:3072\n"
    "s_waitcnt lgkmcnt(0)\n"
    "buffer_store_dwordx4 v[232:235], v184, s[48:51], s66 offen offset:0 sc1\n"
    "buffer_store_dwordx4 v[236:239], v184, s[48:51], s66 offen offset:1024 sc1\n"
    "buffer_store_dwordx4 v[240:243], v184, s[48:51], s66 offen offset:2048 sc1\n"
    "buffer_store_dwordx4 v[244:247], v184, s[48:51], s66 offen offset:3072 sc1\n"
    "s_nop 1\n"
    "v_accvgpr_read_b32 v202, a232\n"
    "s_nop 0\n"
    "v_rcp_f32 v202, v202\n"
    "s_nop 0\n"
    "s_waitcnt lgkmcnt(0)\n"
    "v_mul_f32 v64, v64, v202\n"
    "v_mul_f32 v65, v65, v202\n"
    "v_mul_f32 v66, v66, v202\n"
    "v_mul_f32 v67, v67, v202\n"
    "v_cvt_pk_bf16_f32 v208, v64, v65\n"
    "v_cvt_pk_bf16_f32 v209, v66, v67\n"
    "ds_write_b64 v187, v[208:209] offset:0\n"
    "v_mul_f32 v68, v68, v202\n"
    "v_mul_f32 v69, v69, v202\n"
    "v_mul_f32 v70, v70, v202\n"
    "v_mul_f32 v71, v71, v202\n"
    "v_cvt_pk_bf16_f32 v210, v68, v69\n"
    "v_cvt_pk_bf16_f32 v211, v70, v71\n"
    "ds_write_b64 v188, v[210:211] offset:0\n"
    "v_mul_f32 v72, v72, v202\n"
    "v_mul_f32 v73, v73, v202\n"
    "v_mul_f32 v74, v74, v202\n"
    "v_mul_f32 v75, v75, v202\n"
    "v_cvt_pk_bf16_f32 v208, v72, v73\n"
    "v_cvt_pk_bf16_f32 v209, v74, v75\n"
    "ds_write_b64 v189, v[208:209] offset:0\n"
    "v_mul_f32 v76, v76, v202\n"
    "v_mul_f32 v77, v77, v202\n"
    "v_mul_f32 v78, v78, v202\n"
    "v_mul_f32 v79, v79, v202\n"
    "v_cvt_pk_bf16_f32 v210, v76, v77\n"
    "v_cvt_pk_bf16_f32 v211, v78, v79\n"
    "ds_write_b64 v190, v[210:211] offset:0\n"
    "v_mul_f32 v80, v80, v202\n"
    "v_mul_f32 v81, v81, v202\n"
    "v_mul_f32 v82, v82, v202\n"
    "v_mul_f32 v83, v83, v202\n"
    "v_cvt_pk_bf16_f32 v208, v80, v81\n"
    "v_cvt_pk_bf16_f32 v209, v82, v83\n"
    "ds_write_b64 v191, v[208:209] offset:0\n"
    "v_mul_f32 v84, v84, v202\n"
    "v_mul_f32 v85, v85, v202\n"
    "v_mul_f32 v86, v86, v202\n"
    "v_mul_f32 v87, v87, v202\n"
    "v_cvt_pk_bf16_f32 v210, v84, v85\n"
    "v_cvt_pk_bf16_f32 v211, v86, v87\n"
    "ds_write_b64 v192, v[210:211] offset:0\n"
    "v_mul_f32 v88, v88, v202\n"
    "v_mul_f32 v89, v89, v202\n"
    "v_mul_f32 v90, v90, v202\n"
    "v_mul_f32 v91, v91, v202\n"
    "v_cvt_pk_bf16_f32 v208, v88, v89\n"
    "v_cvt_pk_bf16_f32 v209, v90, v91\n"
    "ds_write_b64 v193, v[208:209] offset:0\n"
    "v_mul_f32 v92, v92, v202\n"
    "v_mul_f32 v93, v93, v202\n"
    "v_mul_f32 v94, v94, v202\n"
    "v_mul_f32 v95, v95, v202\n"
    "v_cvt_pk_bf16_f32 v210, v92, v93\n"
    "v_cvt_pk_bf16_f32 v211, v94, v95\n"
    "ds_write_b64 v194, v[210:211] offset:0\n"
    "s_waitcnt lgkmcnt(0)\n"
    "ds_read_b128 v[216:219], v185 offset:0\n"
    "ds_read_b128 v[220:223], v186 offset:1024\n"
    "ds_read_b128 v[224:227], v185 offset:2048\n"
    "ds_read_b128 v[228:231], v186 offset:3072\n"
    "s_waitcnt lgkmcnt(0)\n"
    "buffer_store_dwordx4 v[216:219], v184, s[48:51], s67 offen offset:0 sc1\n"
    "buffer_store_dwordx4 v[220:223], v184, s[48:51], s67 offen offset:1024 sc1\n"
    "buffer_store_dwordx4 v[224:227], v184, s[48:51], s67 offen offset:2048 sc1\n"
    "buffer_store_dwordx4 v[228:231], v184, s[48:51], s67 offen offset:3072 sc1\n"
    "s_nop 1\n"
    "v_accvgpr_read_b32 v203, a236\n"
    "s_nop 0\n"
    "v_rcp_f32 v203, v203\n"
    "s_nop 0\n"
    "s_waitcnt lgkmcnt(0)\n"
    "v_mul_f32 v96, v96, v203\n"
    "v_mul_f32 v97, v97, v203\n"
    "v_mul_f32 v98, v98, v203\n"
    "v_mul_f32 v99, v99, v203\n"
    "v_cvt_pk_bf16_f32 v208, v96, v97\n"
    "v_cvt_pk_bf16_f32 v209, v98, v99\n"
    "ds_write_b64 v187, v[208:209] offset:0\n"
    "v_mul_f32 v100, v100, v203\n"
    "v_mul_f32 v101, v101, v203\n"
    "v_mul_f32 v102, v102, v203\n"
    "v_mul_f32 v103, v103, v203\n"
    "v_cvt_pk_bf16_f32 v210, v100, v101\n"
    "v_cvt_pk_bf16_f32 v211, v102, v103\n"
    "ds_write_b64 v188, v[210:211] offset:0\n"
    "v_mul_f32 v104, v104, v203\n"
    "v_mul_f32 v105, v105, v203\n"
    "v_mul_f32 v106, v106, v203\n"
    "v_mul_f32 v107, v107, v203\n"
    "v_cvt_pk_bf16_f32 v208, v104, v105\n"
    "v_cvt_pk_bf16_f32 v209, v106, v107\n"
    "ds_write_b64 v189, v[208:209] offset:0\n"
    "v_mul_f32 v108, v108, v203\n"
    "v_mul_f32 v109, v109, v203\n"
    "v_mul_f32 v110, v110, v203\n"
    "v_mul_f32 v111, v111, v203\n"
    "v_cvt_pk_bf16_f32 v210, v108, v109\n"
    "v_cvt_pk_bf16_f32 v211, v110, v111\n"
    "ds_write_b64 v190, v[210:211] offset:0\n"
    "v_mul_f32 v112, v112, v203\n"
    "v_mul_f32 v113, v113, v203\n"
    "v_mul_f32 v114, v114, v203\n"
    "v_mul_f32 v115, v115, v203\n"
    "v_cvt_pk_bf16_f32 v208, v112, v113\n"
    "v_cvt_pk_bf16_f32 v209, v114, v115\n"
    "ds_write_b64 v191, v[208:209] offset:0\n"
    "v_mul_f32 v116, v116, v203\n"
    "v_mul_f32 v117, v117, v203\n"
    "v_mul_f32 v118, v118, v203\n"
    "v_mul_f32 v119, v119, v203\n"
    "v_cvt_pk_bf16_f32 v210, v116, v117\n"
    "v_cvt_pk_bf16_f32 v211, v118, v119\n"
    "ds_write_b64 v192, v[210:211] offset:0\n"
    "v_mul_f32 v120, v120, v203\n"
    "v_mul_f32 v121, v121, v203\n"
    "v_mul_f32 v122, v122, v203\n"
    "v_mul_f32 v123, v123, v203\n"
    "v_cvt_pk_bf16_f32 v208, v120, v121\n"
    "v_cvt_pk_bf16_f32 v209, v122, v123\n"
    "ds_write_b64 v193, v[208:209] offset:0\n"
    "v_mul_f32 v124, v124, v203\n"
    "v_mul_f32 v125, v125, v203\n"
    "v_mul_f32 v126, v126, v203\n"
    "v_mul_f32 v127, v127, v203\n"
    "v_cvt_pk_bf16_f32 v210, v124, v125\n"
    "v_cvt_pk_bf16_f32 v211, v126, v127\n"
    "ds_write_b64 v194, v[210:211] offset:0\n"
    "s_waitcnt lgkmcnt(0)\n"
    "ds_read_b128 v[232:235], v185 offset:0\n"
    "ds_read_b128 v[236:239], v186 offset:1024\n"
    "ds_read_b128 v[240:243], v185 offset:2048\n"
    "ds_read_b128 v[244:247], v186 offset:3072\n"
    "s_waitcnt lgkmcnt(0)\n"
    "buffer_store_dwordx4 v[232:235], v184, s[48:51], s68 offen offset:0 sc1\n"
    "buffer_store_dwordx4 v[236:239], v184, s[48:51], s68 offen offset:1024 sc1\n"
    "buffer_store_dwordx4 v[240:243], v184, s[48:51], s68 offen offset:2048 sc1\n"
    "buffer_store_dwordx4 v[244:247], v184, s[48:51], s68 offen offset:3072 sc1\n"
    "s_nop 1\n"
    "s_cmp_lg_u32 s62, 0\n"
    "s_cbranch_scc1 Lmha_SAFE\n"
    "Lmha_END:\n"
    "s_waitcnt vmcnt(0)\n"
    :
    : [p0l] "s"(ql), [p0h] "s"(qh), [p1l] "s"(kl), [p1h] "s"(kh), [p2l] "s"(vl), [p2h] "s"(vh), [p3l] "s"(ol), [p3h] "s"(oh),
      [p4l] "s"(wl), [p4h] "s"(wh), [wg] "s"(wg), [wv] "s"(wv), [lds] "s"(lds)
    : "v0", "v1", "v2", "v3", "v4", "v5", "v6", "v7", "v8", "v9", "v10", "v11", "v12", "v13", "v14", "v15", "v16", "v17", "v18", "v19", "v20", "v21", "v22", "v23", "v24", "v25", "v26", "v27", "v28", "v29", "v30", "v31", "v32", "v33", "v34", "v35", "v36", "v37", "v38", "v39", "v40", "v41", "v42", "v43", "v44", "v45", "v46", "v47", "v48", "v49", "v50", "v51", "v52", "v53", "v54", "v55", "v56", "v57", "v58", "v59", "v60", "v61", "v62", "v63", "v64", "v65", "v66", "v67", "v68", "v69", "v70", "v71", "v72", "v73", "v74", "v75", "v76", "v77", "v78", "v79", "v80", "v81", "v82", "v83", "v84", "v85", "v86", "v87", "v88", "v89", "v90", "v91", "v92", "v93", "v94", "v95", "v96", "v97", "v98", "v99", "v100", "v101", "v102", "v103", "v104", "v105", "v106", "v107", "v108", "v109", "v110", "v111", "v112", "v113", "v114", "v115", "v116", "v117", "v118", "v119", "v120", "v121", "v122", "v123", "v124", "v125", "v126", "v127", "v128", "v129", "v130", "v131", "v132", "v133", "v134", "v135", "v136", "v137", "v138", "v139", "v140", "v141", "v142", "v143", "v144", "v145", "v146", "v147", "v148", "v149", "v150", "v151", "v152", "v153", "v154", "v155", "v156", "v157", "v158", "v159", "v160", "v161", "v162", "v163", "v164", "v165", "v166", "v167", "v168", "v169", "v170", "v171", "v172", "v173", "v174", "v175", "v176", "v177", "v178", "v179", "v180", "v181", "v182", "v183", "v184", "v185", "v186", "v187", "v188", "v189", "v190", "v191", "v192", "v193", "v194", "v195", "v196", "v197", "v198", "v199", "v200", "v201", "v202", "v203", "v204", "v205", "v206", "v207", "v208", "v209", "v210", "v211", "v212", "v213", "v214", "v215", "v216", "v217", "v218", "v219", "v220", "v221", "v222", "v223", "v224", "v225", "v226", "v227", "v228", "v229", "v230", "v231", "v232", "v233", "v234", "v235", "v236", "v237", "v238", "v239", "v240", "v241", "v242", "v243", "v244", "v245", "v246", "v247", "v248", "v249", "v250", "v251", "v252", "v253", "v254", "v255", "a0", "a1", "a2", "a3", "a4", "a5", "a6", "a7", "a8", "a9", "a10", "a11", "a12", "a13", "a14", "a15", "a16", "a17", "a18", "a19", "a20", "a21", "a22", "a23", "a24", "a25", "a26", "a27", "a28", "a29", "a30", "a31", "a32", "a33", "a34", "a35", "a36", "a37", "a38", "a39", "a40", "a41", "a42", "a43", "a44", "a45", "a46", "a47", "a48", "a49", "a50", "a51", "a52", "a53", "a54", "a55", "a56", "a57", "a58", "a59", "a60", "a61", "a62", "a63", "a64", "a65", "a66", "a67", "a68", "a69", "a70", "a71", "a72", "a73", "a74", "a75", "a76", "a77", "a78", "a79", "a80", "a81", "a82", "a83", "a84", "a85", "a86", "a87", "a88", "a89", "a90", "a91", "a92", "a93", "a94", "a95", "a96", "a97", "a98", "a99", "a100", "a101", "a102", "a103", "a104", "a105", "a106", "a107", "a108", "a109", "a110", "a111", "a112", "a113", "a114", "a115", "a116", "a117", "a118", "a119", "a120", "a121", "a122", "a123", "a124", "a125", "a126", "a127", "a128", "a129", "a130", "a131", "a132", "a133", "a134", "a135", "a136", "a137", "a138", "a139", "a140", "a141", "a142", "a143", "a144", "a145", "a146", "a147", "a148", "a149", "a150", "a151", "a152", "a153", "a154", "a155", "a156", "a157", "a158", "a159", "a160", "a161", "a162", "a163", "a164", "a165", "a166", "a167", "a168", "a169", "a170", "a171", "a172", "a173", "a174", "a175", "a176", "a177", "a178", "a179", "a180", "a181", "a182", "a183", "a184", "a185", "a186", "a187", "a188", "a189", "a190", "a191", "a192", "a193", "a194", "a195", "a196", "a197", "a198", "a199", "a200", "a201", "a202", "a203", "a204", "a205", "a206", "a207", "a208", "a209", "a210", "a211", "a212", "a213", "a214", "a215", "a216", "a217", "a218", "a219", "a220", "a221", "a222", "a223", "a224", "a225", "a226", "a227", "a228", "a229", "a230", "a231", "a232", "a233", "a234", "a235", "a236", "a237", "a238", "a239", "a240", "a241", "a242", "a243", "a244", "a245", "a246", "a247", "a248", "a249", "a250", "a251", "a252", "a253", "a254", "a255", "s32", "s33", "s34", "s35", "s36", "s37", "s38", "s39", "s40", "s41", "s42", "s43", "s44", "s45", "s46", "s47", "s48", "s49", "s50", "s51", "s52", "s53", "s54", "s55", "s56", "s57", "s58", "s59", "s60", "s61", "s62", "s63", "s64", "s65", "s66", "s67", "s68", "s69", "s70", "s71", "s72", "s73", "s74", "s75", "s76", "s77", "s78", "s79", "s80", "s81", "s82", "s83", "s84", "s85", "s86", "s87", "s88", "s89", "s90", "s91", "s92", "s93", "s94", "s95", "s96", "s97", "s98", "s99", "vcc", "scc", "memory");
}
extern "C" __global__ __launch_bounds__(256, 1) void standalone__mha_softmax__near_ridge_kernel(const void* q, const void* k, const void* v, void* o, void* ws) {
  extern __shared__ __attribute__((aligned(16))) char lds[];
  const unsigned long long qp = (unsigned long long)q, kp = (unsigned long long)k, vp = (unsigned long long)v, op = (unsigned long long)o, wp = (unsigned long long)ws;
  const unsigned wv = __builtin_amdgcn_readfirstlane(threadIdx.x >> 6);
  const unsigned ldsb = __builtin_amdgcn_readfirstlane((unsigned)(size_t)lds);
  mha_body((unsigned)qp, (unsigned)(qp >> 32), (unsigned)kp, (unsigned)(kp >> 32), (unsigned)vp, (unsigned)(vp >> 32), (unsigned)op, (unsigned)(op >> 32),
           (unsigned)wp, (unsigned)(wp >> 32), blockIdx.x, wv, ldsb);
}

extern "C" void kernel_launch(void* const* d_in, const int* in_sizes, int n_in, void* d_out, int out_size, void* d_ws, size_t ws_size, hipStream_t stream) {
  (void)in_sizes; (void)n_in; (void)out_size; (void)ws_size;
  (void)hipFuncSetAttribute((const void*)standalone__mha_softmax__near_ridge_kernel, hipFuncAttributeMaxDynamicSharedMemorySize, 163840);
  hipLaunchKernelGGL(standalone__mha_softmax__near_ridge_kernel, dim3(256), dim3(256), 163840, stream, (const void*)d_in[0], (const void*)d_in[1], (const void*)d_in[2], d_out, d_ws);
}
```
